```python
import torch

def _softmax(x, axis=-1):
    return torch.softmax(x, dim=axis)

def _gelu(x):
    return torch.nn.functional.gelu(x, approximate='tanh')

def _silu(x):
    return torch.nn.functional.silu(x)

def _sigmoid(x):
    return torch.sigmoid(x)

def _layernorm(x, axis=-1, eps=1e-05):
    m = x.mean(dim=axis, keepdim=True)
    v = x.var(dim=axis, keepdim=True, unbiased=False)
    return (x - m) / torch.sqrt(v + eps)

def _scatter_add(tgt, idx, src, axis=0):
    return tgt.clone().index_add_(axis, idx, src)

def _segment_sum(src, seg, n):
    out = torch.zeros((n,) + src.shape[1:], dtype=src.dtype)
    return out.index_add_(0, seg, src)

def _pad(x, pw):
    flat = tuple((v for pair in reversed(pw) for v in pair))
    return torch.nn.functional.pad(x, flat)
kernel_name = 'adaLN_cond__gqa_block_cached__moe_arch_mod__prompt'

def setup_inputs():

    _w = lambda a: (lambda t: t.to(torch.bfloat16) if t.is_floating_point() else t)(torch.as_tensor(a))

    def _A_setup():
        B, Sq, D = (1, 2048, 576)
        Hq, Hkv, Sk, Dh = (32, 16, 2048, 128)
        assert Hq % Hkv == 0, (Hq, Hkv)
        return {'x': _w((lambda *s: torch.randn(*s, dtype=torch.bfloat16) * 0.05)(B, Sq, D)), 'k_cache': _w((lambda *s: torch.randn(*s, dtype=torch.bfloat16) * 3.2)(B, Hkv, Sk, Dh)), 'v_cache': _w((lambda *s: torch.randn(*s, dtype=torch.bfloat16) * 0.4)(B, Hkv, Sk, Dh)), 'wq': _w((lambda *s: torch.randn(*s, dtype=torch.bfloat16) * 3.2)(D, Hq * Dh) * D ** (-0.5)), 'wo': _w((lambda *s: torch.randn(*s, dtype=torch.bfloat16) * 0.4)(Hq * Dh, D) * (Hq * Dh) ** (-0.5))}
    _A_in = _A_setup()

    def _B_setup():
        B, S, D, F, C = (1, 2048, 576, 4608, 8)
        return {'x': _w((lambda *s: torch.randn(*s, dtype=torch.bfloat16) * 0.05)(B, S, D)), 'w_r': _w((lambda *s: torch.randn(*s, dtype=torch.bfloat16) * 0.05)(D)), 'w_gate': _w((lambda *s: torch.randn(*s, dtype=torch.bfloat16) * 0.05)(D, F)), 'w_up': _w((lambda *s: torch.randn(*s, dtype=torch.bfloat16) * 0.05)(D, F)), 'w_down': _w((lambda *s: torch.randn(*s, dtype=torch.bfloat16) * 0.05)(F, D))}
    _B_in = _B_setup()
    return {'A_x': _A_in['x'], 'A_k_cache': _A_in['k_cache'], 'A_v_cache': _A_in['v_cache'], 'A_wq': _A_in['wq'], 'A_wo': _A_in['wo'], 'B_x': _B_in['x'], 'B_w_r': _B_in['w_r'], 'B_w_gate': _B_in['w_gate'], 'B_w_up': _B_in['w_up'], 'B_w_down': _B_in['w_down'], 'c': _w((lambda *s: torch.randn(*s, dtype=torch.bfloat16) * 0.1)(1, 576) * 8.0), 'w_mod': _w((lambda *s: torch.randn(*s, dtype=torch.bfloat16) * 0.1)(576, 6 * 576) * 576 ** (-0.5) * 8.0)}

def reference(A_x, A_k_cache, A_v_cache, A_wq, A_wo, B_x, B_w_r, B_w_gate, B_w_up, B_w_down, c, w_mod):

    def _A_ref(x, k_cache, v_cache, wq, wo):
        B, Sq, D = x.shape
        Hq, Dh = (32, 128)
        Hkv = k_cache.shape[1]
        rep = Hq // Hkv
        q = (lambda x, *d: x.permute(*d))((x @ wq).reshape(B, Sq, Hq, Dh), 0, 2, 1, 3)
        k = (lambda x, n, axis: torch.repeat_interleave(x, n, dim=axis))(k_cache, rep, axis=1)
        v = (lambda x, n, axis: torch.repeat_interleave(x, n, dim=axis))(v_cache, rep, axis=1)
        scores = torch.einsum('bhqd,bhkd->bhqk', q, k) * Dh ** (-0.5)
        p = _softmax(scores, axis=-1)
        o = torch.einsum('bhqk,bhkd->bhqd', p, v)
        return (lambda x, *d: x.permute(*d))(o, 0, 2, 1, 3).reshape(B, Sq, Hq * Dh) @ wo

    def _B_ref(x, w_r, w_gate, w_up, w_down):
        B, S, D = x.shape
        C = 8
        r = x @ w_r
        idx = torch.argsort(-r, axis=-1)[:, :C]
        idx_e = (lambda x, n, axis: torch.repeat_interleave(x, n, dim=axis))(idx.reshape(B, C, 1), D, axis=2)
        x_sel = (lambda x, i, axis: torch.take_along_dim(x, i, dim=axis))(x, idx_e, axis=1)
        g = _silu(x_sel @ w_gate)
        u = x_sel @ w_up
        y = g * u @ w_down
        r_sel = _sigmoid((lambda x, i, axis: torch.take_along_dim(x, i, dim=axis))(r, idx, axis=1))
        y = y * r_sel.reshape(B, C, 1)
        b_off = (torch.arange(B) * S).reshape(B, 1)
        flat_idx = (idx + b_off).reshape(B * C)
        zero = (x * 0.0).reshape(B * S, D)
        upd = _scatter_add(zero, flat_idx, y.reshape(B * C, D), axis=0)
        return x + upd.reshape(B, S, D)
    x = A_x
    mod = (_silu(c) @ w_mod).reshape(c.shape[0], 6, 1, -1)
    sa, ca, ga, sb, cb, gb = (mod[:, 0].clone(), mod[:, 1].clone(), mod[:, 2].clone(), mod[:, 3].clone(), mod[:, 4].clone(), mod[:, 5].clone())
    h = _layernorm(x, axis=-1) * (1 + ca) + sa
    x = x + ga * _A_ref(h, A_k_cache, A_v_cache, A_wq, A_wo)
    h = _layernorm(x, axis=-1) * (1 + cb) + sb
    x = x + gb * _B_ref(h, B_w_r, B_w_gate, B_w_up, B_w_down)
    return x
```

```cpp
#include <hip/hip_runtime.h>
#include <hip/hip_bf16.h>
typedef unsigned short u16;
typedef unsigned u32x4 __attribute__((ext_vector_type(4)));
typedef unsigned u32x2 __attribute__((ext_vector_type(2)));
typedef float f32x2 __attribute__((ext_vector_type(2)));
typedef __bf16 bf16x2 __attribute__((ext_vector_type(2)));
#define DI __device__ __forceinline__
DI float bf2f(u16 v) { return __uint_as_float((unsigned)v << 16); }
DI float bflo(unsigned v) { return __uint_as_float(v << 16); }
DI float bfhi(unsigned v) { return __uint_as_float(v & 0xffff0000u); }
DI unsigned cvtpk(float lo, float hi) { f32x2 v = {lo, hi}; bf16x2 b = __builtin_convertvector(v, bf16x2); return __builtin_bit_cast(unsigned, b); }
DI float rbf(float x) { return bflo(cvtpk(x, 0.f)); }

constexpr int D = 576, SQ = 2048;
constexpr unsigned WS_TAG = 0, WS_FLAGA = 1024, WS_FLAGH = 3072, WS_FLAGD = 5120, WS_FLAGC = 6144, WS_MG = 8192, WS_RG = 36864, WS_H = 65536,
                   WS_HG = 3u << 20, WS_X1 = 8u << 20, WS_YP = 11u << 20, WS_PART = 32u << 20;
constexpr unsigned PSTR = 128 * 18432 + 0;

typedef __bf16 bf16x4 __attribute__((ext_vector_type(4)));
typedef __bf16 bf16x8 __attribute__((ext_vector_type(8)));
typedef float f32x4 __attribute__((ext_vector_type(4)));
#define LAS __attribute__((address_space(3)))
#define GAS __attribute__((address_space(1)))
typedef __amdgpu_buffer_rsrc_t rsrc_t;
constexpr int SC1 = 16;
DI u32x4 ld16c(rsrc_t r, unsigned off) { return __builtin_amdgcn_raw_buffer_load_b128(r, off, 0, SC1); }
DI u32x4 ld16(rsrc_t r, unsigned off) { return __builtin_amdgcn_raw_buffer_load_b128(r, off, 0, 0); }
DI u32x2 ld8c(rsrc_t r, unsigned off) { return __builtin_amdgcn_raw_buffer_load_b64(r, off, 0, SC1); }
DI unsigned ld4c(rsrc_t r, unsigned off) { return __builtin_amdgcn_raw_buffer_load_b32(r, off, 0, SC1); }
DI void st16c(rsrc_t r, unsigned off, u32x4 v) { __builtin_amdgcn_raw_buffer_store_b128(v, r, off, 0, SC1); }
DI void st8c(rsrc_t r, unsigned off, u32x2 v) { __builtin_amdgcn_raw_buffer_store_b64(v, r, off, 0, SC1); }
DI void st4c(rsrc_t r, unsigned off, unsigned v) { __builtin_amdgcn_raw_buffer_store_b32(v, r, off, 0, SC1); }
DI void drain() { asm volatile("s_waitcnt vmcnt(0)" ::: "memory"); }
DI void bar() { asm volatile("s_waitcnt lgkmcnt(0)\n\ts_barrier" ::: "memory"); }
template <int C> DI unsigned dppu(unsigned x) { return (unsigned)__builtin_amdgcn_update_dpp(0, (int)x, C, 0xf, 0xf, true); }
template <int C> DI float dppf(float x) { return __uint_as_float(dppu<C>(__float_as_uint(x))); }
DI float sw16(float x) { auto r = __builtin_amdgcn_permlane16_swap(__float_as_uint(x), __float_as_uint(x), false, false); return __uint_as_float(r[0]) + __uint_as_float(r[1]); }
DI float sw32(float x) { auto r = __builtin_amdgcn_permlane32_swap(__float_as_uint(x), __float_as_uint(x), false, false); return __uint_as_float(r[0]) + __uint_as_float(r[1]); }
DI float sum32(float v) { v += dppf<0xB1>(v); v += dppf<0x4E>(v); v += dppf<0x141>(v); v += dppf<0x140>(v); return sw16(v); }
DI float sum_s8(float v) { v += dppf<0x128>(v); v = sw16(v); return sw32(v); }
DI unsigned umax2(unsigned a, unsigned b) { return a > b ? a : b; }
DI unsigned umin64s(unsigned v) {
  v = min(v, dppu<0xB1>(v)); v = min(v, dppu<0x4E>(v)); v = min(v, dppu<0x141>(v)); v = min(v, dppu<0x140>(v));
  v = min(v, (unsigned)__builtin_amdgcn_update_dpp((int)v, (int)v, 0x142, 0xa, 0xf, false));
  v = min(v, (unsigned)__builtin_amdgcn_update_dpp((int)v, (int)v, 0x143, 0xc, 0xf, false));
  return __builtin_amdgcn_readlane(v, 63);
}
DI unsigned umax64(unsigned v) {
  v = umax2(v, dppu<0xB1>(v)); v = umax2(v, dppu<0x4E>(v)); v = umax2(v, dppu<0x141>(v)); v = umax2(v, dppu<0x140>(v));
  auto a = __builtin_amdgcn_permlane16_swap(v, v, false, false); v = umax2(a[0], a[1]);
  auto b = __builtin_amdgcn_permlane32_swap(v, v, false, false); return umax2(b[0], b[1]);
}
DI bf16x8 trfrag(const char* base, int second) {
  const bf16x4 a0 = __builtin_amdgcn_ds_read_tr16_b64_v4bf16((LAS bf16x4*)base);
  const bf16x4 a1 = __builtin_amdgcn_ds_read_tr16_b64_v4bf16((LAS bf16x4*)(base + second));
  return __builtin_shufflevector(a0, a1, 0, 1, 2, 3, 4, 5, 6, 7);
}
constexpr unsigned SPIN_CAP = 1u << 18;
#define SPIN(cond) _Pragma("nounroll") for (unsigned sp_ = 0; (cond) && sp_ < SPIN_CAP; ++sp_) { __builtin_amdgcn_s_sleep(1); asm volatile("" ::: "memory"); }

DI f32x4 mf(bf16x8 a, u32x4 b, f32x4 c) { return __builtin_amdgcn_mfma_f32_16x16x32_bf16(a, __builtin_bit_cast(bf16x8, b), c, 0, 0, 0); }
DI u32x4 pack8(f32x4 a, f32x4 b) { return u32x4{cvtpk(a[0], a[1]), cvtpk(a[2], a[3]), cvtpk(b[0], b[1]), cvtpk(b[2], b[3])}; }
DI float hsum(f32x4 a) { return (a[0] + a[1]) + (a[2] + a[3]); }
constexpr unsigned OOB = 0x20000000u;


constexpr unsigned L_HV = 32768, L_HR = 40960;
template <class PRE>
DI unsigned head_phase(char* lds, char* ws, const u16* __restrict__ x, const u16* __restrict__ c, const u16* __restrict__ wm,
                       unsigned rb, unsigned kvh, const unsigned w, PRE&& issue_prefetch) {
  unsigned l; asm volatile("v_mbcnt_lo_u32_b32 %0, -1, 0\n\tv_mbcnt_hi_u32_b32 %0, -1, %0" : "=v"(l));
  const unsigned t = 64 * w + l, b = blockIdx.x;
  const rsrc_t rw = __builtin_amdgcn_make_buffer_rsrc((void*)ws, (short)0, 0x10000000, 0x00020000);
  const rsrc_t rx = __builtin_amdgcn_make_buffer_rsrc((void*)x, (short)0, SQ * D * 2, 0x00020000);
  const rsrc_t rm = __builtin_amdgcn_make_buffer_rsrc((void*)wm, (short)0, b < 216 ? D * 3456 * 2 : 0, 0x00020000);
  const rsrc_t rc = __builtin_amdgcn_make_buffer_rsrc((void*)c, (short)0, D * 2, 0x00020000);
  const unsigned tagv = ld4c(rw, WS_TAG);
  const unsigned cg = (b & 7) * 27 + (b >> 3), r0 = t >> 1;
  u32x4 wv[5]; unsigned cv[5];
#pragma unroll
  for (int i = 0; i < 5; ++i) { const unsigned row = min(r0 + 128 * i, 575u);
    wv[i] = ld16(rm, row * 6912 + (cg * 16 + 8 * (t & 1)) * 2); cv[i] = __builtin_amdgcn_raw_buffer_load_b16(rc, row * 2, 0, 0); }
  const unsigned rl = l & 7, ci = l >> 3, row = rb * 128 + 8 * kvh + rl;
  u32x2 xa[3], xb[3];
#pragma unroll
  for (int i = 0; i < 3; ++i) { const unsigned p = w + 4 * i, o = p < 9 ? row * 1152 + (32 * (2 * p + (ci >> 2)) + 4 * (ci & 3)) * 2 : OOB;
    xa[i] = __builtin_amdgcn_raw_buffer_load_b64(rx, o, 0, 0); xb[i] = __builtin_amdgcn_raw_buffer_load_b64(rx, o, 32, 0); }
  unsigned long long pc_; unsigned len_; unsigned cpf[3];
  asm volatile("s_getpc_b64 %0\n.Lk_attn_here:\n\ts_mov_b32 %1, .Lk_attn_end-.Lk_attn_here" : "=s"(pc_), "=s"(len_));
  { const rsrc_t rcode = __builtin_amdgcn_make_buffer_rsrc((void*)pc_, (short)0, (int)len_, 0x00020000);
#pragma unroll
    for (int i = 0; i < 3; ++i) cpf[i] = __builtin_amdgcn_raw_buffer_load_b32(rcode, ((3 * w + i) * 64 + l) * 128, 0, 0); }
  const unsigned tag = __builtin_amdgcn_readfirstlane(tagv) + 1u;
  float* red = (float*)(lds + L_HR);
  {
    float acc[8] = {0, 0, 0, 0, 0, 0, 0, 0};
#pragma unroll
    for (int i = 0; i < 5; ++i) { const float cf = __uint_as_float(cv[i] << 16); float sv = rbf(cf * __builtin_amdgcn_rcpf(1.f + __expf(-cf))); if (i == 4 && r0 >= 64) sv = 0.f;
#pragma unroll
      for (int e = 0; e < 4; ++e) { acc[2 * e] += sv * bflo(wv[i][e]); acc[2 * e + 1] += sv * bfhi(wv[i][e]); } }
#pragma unroll
    for (int e = 0; e < 8; ++e) { float v = acc[e]; v += dppf<0x4E>(v); v += dppf<0x124>(v); v += dppf<0x128>(v); v = sw16(v); acc[e] = sw32(v); }
    if (l < 2) {
#pragma unroll
      for (int e = 0; e < 8; ++e) red[w * 16 + 8 * l + e] = acc[e];
    }
  }
  f32x4 v0[3], v1[3]; f32x4 sa4 = {0, 0, 0, 0};
#pragma unroll
  for (int i = 0; i < 3; ++i) { v0[i] = f32x4{bflo(xa[i][0]), bfhi(xa[i][0]), bflo(xa[i][1]), bfhi(xa[i][1])}; v1[i] = f32x4{bflo(xb[i][0]), bfhi(xb[i][0]), bflo(xb[i][1]), bfhi(xb[i][1])}; sa4 += v0[i] + v1[i]; }
  { const float s = sum_s8(hsum(sa4)); if (l < 8) red[64 + w * 8 + l] = s; }
  bar();
  if (t < 16 && b < 216) st8c(rw, WS_MG + (cg * 16 + t) * 8, u32x2{__float_as_uint(rbf((red[t] + red[16 + t]) + (red[32 + t] + red[48 + t]))), tag});
  const float mean = ((red[64 + rl] + red[72 + rl]) + (red[80 + rl] + red[88 + rl])) * (1.f / D);
  { f32x4 q4 = {0, 0, 0, 0};
#pragma unroll
    for (int i = 0; i < 3; ++i) if (w + 4 * i < 9) { const f32x4 d0 = v0[i] - mean, d1 = v1[i] - mean; q4 += d0 * d0 + d1 * d1; }
    const float q = sum_s8(hsum(q4)); if (l < 8) red[96 + w * 8 + l] = q; }
  bar();
  {
    const float var = ((red[96 + rl] + red[104 + rl]) + (red[112 + rl] + red[120 + rl])) * (1.f / D);
    const float mb = rbf(mean), sd = rbf(__builtin_sqrtf(rbf(rbf(var) + 1e-5f)));
#pragma unroll
    for (int i = 0; i < 3; ++i)
#pragma unroll
      for (int e = 0; e < 4; ++e) { v0[i][e] = rbf(rbf(v0[i][e] - mb) / sd); v1[i][e] = rbf(rbf(v1[i][e] - mb) / sd); }
  }
  if (w < 2) {
    u32x4 g[5];
    const unsigned gb_ = WS_MG + t * 16;
#pragma nounroll
    for (unsigned sp = 0; sp < SPIN_CAP; ++sp) {
      asm volatile("" ::: "memory");
#pragma unroll
      for (int i = 0; i < 4; ++i) g[i] = __builtin_amdgcn_raw_buffer_load_b128(rw, gb_, 2048 * i, SC1);
      g[4] = __builtin_amdgcn_raw_buffer_load_b128(rw, t < 64 ? gb_ : OOB, 8192, SC1);
      unsigned bad = t < 64 ? (g[4][1] ^ tag) | (g[4][3] ^ tag) : 0u;
#pragma unroll
      for (int i = 0; i < 4; ++i) bad |= (g[i][1] ^ tag) | (g[i][3] ^ tag);
      if (__all(bad == 0)) break;
      __builtin_amdgcn_s_sleep(1);
    }
#pragma unroll
    for (int i = 0; i < 5; ++i) if (i < 4 || t < 64) *(u32x2*)(lds + L_HV + (t + 128 * i) * 8) = u32x2{g[i][0], g[i][2]};
  } else {
    issue_prefetch(w - 2); issue_prefetch(w);
  }
  bar();
#pragma unroll
  for (int i = 0; i < 3; ++i) { const unsigned p = w + 4 * i;
    if (p < 9) {
      const unsigned kk = 2 * p + (ci >> 2), k0 = 32 * kk + 4 * (ci & 3);
      const f32x4 s0 = *(const f32x4*)(lds + L_HV + k0 * 4), s1 = *(const f32x4*)(lds + L_HV + k0 * 4 + 64), c0 = *(const f32x4*)(lds + L_HV + (D + k0) * 4), c1 = *(const f32x4*)(lds + L_HV + (D + k0) * 4 + 64);
      f32x4 o0, o1;
#pragma unroll
      for (int e = 0; e < 4; ++e) { o0[e] = rbf(v0[i][e] * rbf(1.f + c0[e])) + s0[e]; o1[e] = rbf(v1[i][e] * rbf(1.f + c1[e])) + s1[e]; }
      st16c(rw, WS_H + (((row >> 4) * 18 + kk) * 64 + (row & 15) + 16 * (ci & 3)) * 16, pack8(o0, o1));
    } }
#pragma unroll
  for (int i = 0; i < 3; ++i) asm volatile("" :: "v"(cpf[i]));
  drain(); bar();
  if (t == 0) st4c(rw, WS_FLAGH + rb * 128 + kvh * 4, tag);
  if (w == 0) { SPIN(!__all(l >= 16 || ld4c(rw, WS_FLAGH + rb * 128 + (l & 15) * 4) == tag)); }
  bar();
  return tag;
}
constexpr unsigned L_WD0 = 98304, L_WD8 = 73728, L_SCR = 131072, L_SCR3 = 77824, L_VEC = 87040, L_WR = 96256, L_XS = 131072, L_EX = 158720, L_CAND = 162816, L_RED = 163072;
DI unsigned scr_base(unsigned w) { return w < 3 ? L_SCR + 9216 * w : L_SCR3; }
DI void tail_phase(char* lds, char* ws, const u16* __restrict__ x, const u16* __restrict__ wr, const u16* __restrict__ wg, const u16* __restrict__ wu,
                   const u16* __restrict__ wd, u16* __restrict__ out,
                   unsigned tag, unsigned rb, unsigned kvh, const unsigned w) {
  unsigned l; asm volatile("v_mbcnt_lo_u32_b32 %0, -1, 0\n\tv_mbcnt_hi_u32_b32 %0, -1, %0" : "=v"(l));
  const unsigned t = 64 * w + l, b = blockIdx.x;
  const rsrc_t rw = __builtin_amdgcn_make_buffer_rsrc((void*)ws, (short)0, 0x10000000, 0x00020000);
  const rsrc_t rx = __builtin_amdgcn_make_buffer_rsrc((void*)x, (short)0, SQ * D * 2, 0x00020000);
  const rsrc_t ro = __builtin_amdgcn_make_buffer_rsrc((void*)out, (short)0, SQ * D * 2, 0x00020000);
  drain(); bar();
  if (t == 0) st4c(rw, WS_FLAGA + rb * 128 + kvh * 4, tag);
  const unsigned rl2 = 2 * w + (l >> 5), j = l & 31, row = rb * 128 + 8 * kvh + rl2;
  const bool act = j < 24;
  const unsigned jc = act ? j : 0;
  u32x4 xv[3];
#pragma unroll
  for (int i = 0; i < 3; ++i) xv[i] = ld16(rx, row * 1152 + (jc + 24 * i) * 16);
  if (w >= 2) {
    const rsrc_t rr = __builtin_amdgcn_make_buffer_rsrc((void*)wr, (short)0, D * 2, 0x00020000);
    __builtin_amdgcn_raw_ptr_buffer_load_lds(rr, (LAS void*)(lds + L_WR + 1024 * (w - 2)), 16, 1024 * (w - 2) + 16 * l, 0, 0, 0);
  }
  {
    u32x4 g[5];
    const unsigned gb_ = WS_MG + 2 * D * 8 + t * 16;
#pragma nounroll
    for (unsigned sp = 0; sp < SPIN_CAP; ++sp) {
      asm volatile("" ::: "memory");
#pragma unroll
      for (int i = 0; i < 4; ++i) g[i] = __builtin_amdgcn_raw_buffer_load_b128(rw, gb_, 4096 * i, SC1);
      g[4] = __builtin_amdgcn_raw_buffer_load_b128(rw, t < 128 ? gb_ : OOB, 4096 * 4, SC1);
      unsigned bad = t < 128 ? (g[4][1] ^ tag) | (g[4][3] ^ tag) : 0u;
#pragma unroll
      for (int i = 0; i < 4; ++i) bad |= (g[i][1] ^ tag) | (g[i][3] ^ tag);
      if (__all(bad == 0)) break;
      __builtin_amdgcn_s_sleep(1);
    }
#pragma unroll
    for (int i = 0; i < 5; ++i) if (i < 4 || t < 128) {
      const unsigned e = 2 * (t + 256 * i); const float add = (e >= 2 * D && e < 3 * D) ? 1.f : 0.f;
      *(f32x2*)(lds + L_VEC + e * 4) = f32x2{__uint_as_float(g[i][0]) + add, __uint_as_float(g[i][2]) + add};
    }
  }
  const unsigned one = ((l >> 4) == ((l & 15) >> 2)) ? (0x3F80u << (16 * (l & 1))) : 0u, od0 = (l & 2) ? 0u : one, od1 = (l & 2) ? one : 0u;
  const bf16x8 Alo = __builtin_bit_cast(bf16x8, u32x4{od0, od1, 0u, 0u}), Ahi = __builtin_bit_cast(bf16x8, u32x4{0u, 0u, od0, od1});
  const f32x4 Z4 = {0.f, 0.f, 0.f, 0.f};
  if (w == 0) { SPIN(!__all(l >= 16 || ld4c(rw, WS_FLAGA + rb * 128 + (l & 15) * 4) == tag)); }
  drain(); bar();
  {
    const unsigned rl = l & 7, ci = l >> 3, G = 8 * rb + (kvh >> 1), r16 = 8 * (kvh & 1) + rl;
    const unsigned base = WS_PART + 4 * w * PSTR + G * 18432 + (ci >> 2) * 1024 + (r16 + 16 * (ci & 3)) * 16;
    u32x4 pv[9][4];
#pragma unroll
    for (int i = 0; i < 9; ++i)
#pragma unroll
      for (int h = 0; h < 4; ++h) pv[i][h] = __builtin_amdgcn_raw_buffer_load_b128(rw, base, h * PSTR + i * 2048, SC1);
    char* scr = lds + scr_base(w) + rl * 1152 + (32 * (ci >> 2) + 4 * (ci & 3)) * 2;
#pragma unroll
    for (int i = 0; i < 9; ++i) {
      f32x4 lo = Z4, hi = Z4;
#pragma unroll
      for (int h = 0; h < 4; ++h) { lo = mf(Alo, pv[i][h], lo); hi = mf(Ahi, pv[i][h], hi); }
      *(u32x2*)(scr + 128 * i) = u32x2{cvtpk(lo[0], lo[1]), cvtpk(lo[2], lo[3])};
      *(u32x2*)(scr + 128 * i + 32) = u32x2{cvtpk(hi[0], hi[1]), cvtpk(hi[2], hi[3])};
    }
  }
  bar();
  f32x4 v[6], sacc = Z4;
  const unsigned cc = jc * 16;
#pragma unroll
  for (int i = 0; i < 3; ++i) {
    const unsigned o = rl2 * 1152 + cc + 384 * i;
    f32x4 lo = Z4, hi = Z4;
#pragma unroll
    for (int h = 0; h < 4; ++h) { const u32x4 p = *(const u32x4*)(lds + (h < 3 ? L_SCR + 9216 * h : L_SCR3) + o); lo = mf(Alo, p, lo); hi = mf(Ahi, p, hi); }
    const f32x4 xl = mf(Alo, xv[i], Z4), xh = mf(Ahi, xv[i], Z4);
    const f32x4 g0 = *(const f32x4*)(lds + L_VEC + 2 * cc + 768 * i), g1 = *(const f32x4*)(lds + L_VEC + 2 * cc + 768 * i + 16);
    v[2 * i] = __builtin_elementwise_fma(g0, lo, xl); v[2 * i + 1] = __builtin_elementwise_fma(g1, hi, xh);
    sacc += v[2 * i] + v[2 * i + 1];
  }
  const float am = act ? 1.f : 0.f;
  const float mean = sum32(hsum(sacc) * am) * (1.f / D);
  f32x4 qacc = Z4, d[6];
#pragma unroll
  for (int i = 0; i < 6; ++i) { d[i] = v[i] - mean; qacc = __builtin_elementwise_fma(d[i], d[i], qacc); }
  const float isd = __builtin_amdgcn_rsqf(sum32(hsum(qacc) * am) * (1.f / D) + 1e-5f);
  f32x4 racc = Z4; u32x4 hp[3], xp[3], op[3];
#pragma unroll
  for (int i = 0; i < 3; ++i) {
    const char* vb_ = lds + L_VEC + 2 * cc + 768 * i;
    const f32x4 s0 = *(const f32x4*)(vb_ + 2304), s1 = *(const f32x4*)(vb_ + 2304 + 16), c0 = *(const f32x4*)(vb_ + 4608), c1 = *(const f32x4*)(vb_ + 4608 + 16);
    const f32x4 b0 = *(const f32x4*)(vb_ + 6912), b1 = *(const f32x4*)(vb_ + 6912 + 16);
    const u32x4 wv = *(const u32x4*)(lds + L_WR + cc + 384 * i);
    const f32x4 w0 = mf(Alo, wv, Z4), w1 = mf(Ahi, wv, Z4);
    const f32x4 h0 = __builtin_elementwise_fma(d[2 * i] * isd, c0, s0), h1 = __builtin_elementwise_fma(d[2 * i + 1] * isd, c1, s1);
    racc = __builtin_elementwise_fma(h0, w0, racc); racc = __builtin_elementwise_fma(h1, w1, racc);
    hp[i] = pack8(h0, h1); xp[i] = pack8(v[2 * i], v[2 * i + 1]);
    op[i] = pack8(__builtin_elementwise_fma(b0, h0, v[2 * i]), __builtin_elementwise_fma(b1, h1, v[2 * i + 1]));
  }
  const float rsc = sum32(hsum(racc) * am);
  if (j == 0) st8c(rw, WS_RG + row * 8, u32x2{__float_as_uint(rsc), tag});
  auto store_h2 = [&]() {
    const unsigned hb = act ? WS_HG + row * 2304 + j * 32 : OOB;
#pragma unroll
    for (int i = 0; i < 3; ++i) {
      __builtin_amdgcn_raw_buffer_store_b128(u32x4{hp[i][0], tag, hp[i][1], tag}, rw, hb, 768 * i, SC1);
      __builtin_amdgcn_raw_buffer_store_b128(u32x4{hp[i][2], tag, hp[i][3], tag}, rw, hb + 16, 768 * i, SC1);
    } };
  auto store_rows = [&]() {
    const unsigned xb = act ? row * 1152 + j * 16 : OOB;
#pragma unroll
    for (int i = 0; i < 3; ++i) {
      __builtin_amdgcn_raw_buffer_store_b128(xp[i], rw, xb, WS_X1 + 384 * i, SC1);
      __builtin_amdgcn_raw_buffer_store_b128(op[i], ro, xb, 384 * i, SC1);
    } };
  if (b >= 144) {
    store_h2(); store_rows(); drain(); bar();
    if (t == 0) st4c(rw, WS_FLAGD + b * 4, tag);
    return;
  }
  unsigned key[8];
  {
    u32x4 g[4];
    const unsigned gb_ = WS_RG + (512 * w + 2 * l) * 8;
#pragma unroll
    for (int i = 0; i < 4; ++i) g[i] = __builtin_amdgcn_raw_buffer_load_b128(rw, gb_, 1024 * i, SC1);
#pragma nounroll
    for (unsigned sp = 0; sp < SPIN_CAP; ++sp) {
      asm volatile("" ::: "memory");
      unsigned bad = 0;
#pragma unroll
      for (int i = 0; i < 4; ++i) bad |= (g[i][1] ^ tag) | (g[i][3] ^ tag);
      if (__all(bad == 0)) break;
      __builtin_amdgcn_s_sleep(1);
#pragma unroll
      for (int i = 0; i < 4; ++i) g[i] = __builtin_amdgcn_raw_buffer_load_b128(rw, gb_, 1024 * i, SC1);
    }
    store_h2();
    const unsigned ib = 2047u - (512 * w + 2 * l);
#pragma unroll
    for (int i = 0; i < 8; ++i) {
      const unsigned bits = g[i >> 1][2 * (i & 1)];
      const unsigned u = bits ^ ((unsigned)((int)bits >> 31) | 0x80000000u);
      key[i] = (u & 0xFFFFF800u) | (ib - (128 * (i >> 1) + (i & 1)));
    }
  }
  unsigned* cand = (unsigned*)(lds + L_CAND);
  {
    unsigned top = 0xFFFFFFFFu, mine = 0;
#pragma unroll
    for (int it = 0; it < 8; ++it) {
      const unsigned dm = umin64s(min(min(min(top - key[0], top - key[1]), min(top - key[2], top - key[3])), min(min(top - key[4], top - key[5]), min(top - key[6], top - key[7]))));
      const unsigned m = top - dm;
      mine = l == (unsigned)it ? m : mine;
      top = m - 1u;
    }
    if (l < 8) cand[w * 8 + l] = mine;
  }
  bar();
  if (b == 0 && t == 0) st4c(rw, WS_TAG, tag);
  unsigned* selp = cand + 32 + 8 * w;
  {
    const unsigned ck = cand[l & 31];
    unsigned rank = 0;
#pragma unroll
    for (int i = 0; i < 8; ++i) { const u32x4 c4 = *(const u32x4*)(cand + 4 * i); rank += (c4[0] > ck) + (c4[1] > ck) + (c4[2] > ck) + (c4[3] > ck); }
    if (l < 32 && rank < 8) selp[rank] = 2047u - (ck & 0x7FFu);
    asm volatile("s_waitcnt lgkmcnt(0)" ::: "memory");
  }
  const unsigned fr = b / 18, fc = 32 * (b - 18 * fr), frow = selp[fr];
  {
    const unsigned r = t >> 5, m0 = t & 31, gb_ = WS_HG + selp[r] * 2304 + m0 * 16;
    u32x4 g[5];
#pragma unroll
    for (int i = 0; i < 4; ++i) g[i] = __builtin_amdgcn_raw_buffer_load_b128(rw, gb_, 512 * i, SC1);
    g[4] = __builtin_amdgcn_raw_buffer_load_b128(rw, m0 < 16 ? gb_ : OOB, 2048, SC1);
    store_rows();
#pragma nounroll
    for (unsigned sp = 0; sp < SPIN_CAP; ++sp) {
      asm volatile("" ::: "memory");
      unsigned bad = m0 < 16 ? (g[4][1] ^ tag) | (g[4][3] ^ tag) : 0u;
#pragma unroll
      for (int i = 0; i < 4; ++i) bad |= (g[i][1] ^ tag) | (g[i][3] ^ tag);
      if (__all(bad == 0)) break;
      __builtin_amdgcn_s_sleep(1);
#pragma unroll
      for (int i = 0; i < 4; ++i) g[i] = __builtin_amdgcn_raw_buffer_load_b128(rw, gb_, 512 * i, SC1);
      g[4] = __builtin_amdgcn_raw_buffer_load_b128(rw, m0 < 16 ? gb_ : OOB, 2048, SC1);
    }
    char* xs = lds + L_XS + (((m0 >> 3) * 4 + (m0 & 3)) * 8 + r) * 16 + ((m0 >> 2) & 1) * 8;
#pragma unroll
    for (int i = 0; i < 4; ++i) *(u32x2*)(xs + 2048 * i) = u32x2{g[i][0], g[i][2]};
    if (m0 < 16) *(u32x2*)(xs + 2048 * 4) = u32x2{g[4][0], g[4][2]};
  }
  const u32x2 rsel = ld8c(rw, WS_RG + frow * 8);
  const f32x4 gbf = *(const f32x4*)(lds + L_VEC + 3 * 2304 + (fc + 4 * (t & 7)) * 4);
  bar();
  const unsigned g4 = l >> 4, q4 = (l >> 2) & 3, p4 = l & 3;
  {
    f32x4 acc = Z4;
    const char* wa = lds + 1024 * w + 32 * (4 * g4 + q4) + 8 * p4; const char* xb = lds + L_XS + (g4 * 8 + (l & 7)) * 16;
#pragma unroll
    for (int kk = 0; kk < 18; ++kk)
      acc = __builtin_amdgcn_mfma_f32_16x16x32_bf16(trfrag(wa + (kk < 16 ? 4096 * kk : 65536 + 4096 * (kk - 16)), 512), *(const bf16x8*)(xb + 512 * kk), acc, 0, 0, 0);
    *(f32x4*)(lds + L_EX + t * 16) = acc;
  }
  drain(); bar();
  if (t == 0) st4c(rw, WS_FLAGD + b * 4, tag);
  bf16x8 GUf;
  {
    const char* ex = lds + L_EX + l * 16;
    const f32x4 g0 = *(const f32x4*)ex, g1 = *(const f32x4*)(ex + 1024), u0 = *(const f32x4*)(ex + 2048), u1 = *(const f32x4*)(ex + 3072);
    f32x4 z0, z1;
#pragma unroll
    for (int i = 0; i < 4; ++i) {
      z0[i] = g0[i] * __builtin_amdgcn_rcpf(1.f + __expf(-g0[i])) * u0[i];
      z1[i] = g1[i] * __builtin_amdgcn_rcpf(1.f + __expf(-g1[i])) * u1[i];
    }
    GUf = __builtin_bit_cast(bf16x8, pack8(z0, z1));
  }
  {
    const unsigned yb = (l & 15) < 8 ? WS_YP + ((b * 8 + (l & 15)) * 144 + 36 * w + g4) * 16 : OOB;
    const char* wa = lds + 1024 * (g4 >> 1) + 32 * (4 * (g4 & 1) + q4) + 8 * p4;
#pragma unroll
    for (int i = 0; i < 9; ++i) {
      const unsigned nt = 9 * w + i, nq = nt >> 2;
      const f32x4 y = __builtin_amdgcn_mfma_f32_16x16x32_bf16(trfrag(wa + (nq < 8 ? L_WD0 + 4096 * nq : L_WD8) + 256 * (nt & 3), 2048), GUf, Z4, 0, 0, 0);
      __builtin_amdgcn_raw_buffer_store_b128(u32x4{cvtpk(y[0], y[1]), tag, cvtpk(y[2], y[3]), tag}, rw, yb, 64 * i, SC1);
    }
  }
  {
    const unsigned okv = (frow >> 3) & 15, ob = (okv >> 1) | ((okv & 1) << 3) | ((frow >> 7) << 4);
    SPIN(__builtin_amdgcn_readfirstlane(ld4c(rw, WS_FLAGD + ob * 4)) != tag);
    const unsigned c = t & 7, jj = t >> 3, yb = WS_YP + ((jj * 8 + fr) * 144 + (fc >> 2) + c) * 16;
    const u32x2 x1v = ld8c(rw, WS_X1 + frow * 1152 + (fc + 4 * c) * 2);
    u32x4 pg[5];
#pragma nounroll
    for (unsigned sp = 0; sp < SPIN_CAP; ++sp) {
      asm volatile("" ::: "memory");
#pragma unroll
      for (int i = 0; i < 4; ++i) pg[i] = __builtin_amdgcn_raw_buffer_load_b128(rw, yb, 32 * 8 * 144 * 16 * i, SC1);
      pg[4] = __builtin_amdgcn_raw_buffer_load_b128(rw, jj < 16 ? yb : OOB, 32 * 8 * 144 * 16 * 4, SC1);
      unsigned bad = jj < 16 ? (pg[4][1] ^ tag) | (pg[4][3] ^ tag) : 0u;
#pragma unroll
      for (int i = 0; i < 4; ++i) bad |= (pg[i][1] ^ tag) | (pg[i][3] ^ tag);
      if (__all(bad == 0)) break;
      __builtin_amdgcn_s_sleep(1);
    }
    f32x4 pv[5];
#pragma unroll
    for (int i = 0; i < 5; ++i) pv[i] = f32x4{bflo(pg[i][0]), bfhi(pg[i][0]), bflo(pg[i][2]), bfhi(pg[i][2])};
    f32x4 sa = (pv[0] + pv[1]) + (pv[2] + pv[3]) + pv[4];
#pragma unroll
    for (int e = 0; e < 4; ++e) sa[e] = sum_s8(sa[e]);
    f32x4* red = (f32x4*)(lds + L_RED);
    if (l < 8) red[w * 8 + l] = sa;
    bar();
    if (t < 8) {
      const f32x4 y = (red[t] + red[8 + t]) + (red[16 + t] + red[24 + t]);
      const float sg = __builtin_amdgcn_rcpf(1.f + __expf(-__uint_as_float(rsel[0])));
      const unsigned n = fc + 4 * t;
      const u32x2 hw = *(const u32x2*)(lds + L_XS + (((n >> 5) * 4 + ((n >> 2) & 3)) * 8 + fr) * 16 + ((n >> 4) & 1) * 8);
      const f32x4 hh = {bflo(hw[0]), bfhi(hw[0]), bflo(hw[1]), bfhi(hw[1])}, xx = {bflo(x1v[0]), bfhi(x1v[0]), bflo(x1v[1]), bfhi(x1v[1])};
      const f32x4 o = xx + gbf * (hh + y * sg);
      st8c(ro, frow * 1152 + n * 2, u32x2{cvtpk(o[0], o[1]), cvtpk(o[2], o[3])});
    }
  }
}

__global__ __launch_bounds__(256, 1) void k_attn(const u16* __restrict__ wq, const u16* __restrict__ kc, const u16* __restrict__ vc,
                                                 const u16* __restrict__ wo, const u16* __restrict__ x, const u16* __restrict__ wr, const u16* __restrict__ wg, const u16* __restrict__ wu,
                                                 const u16* __restrict__ wd, const u16* __restrict__ cvec, const u16* __restrict__ wm, u16* __restrict__ out, char* __restrict__ ws) {
  extern __shared__ __attribute__((aligned(16))) char lds[];
  const u16* hf = (const u16*)(ws + WS_H); u16* part = (u16*)(ws + WS_PART);
  const unsigned b = blockIdx.x, i5 = b >> 3, kvh = 2 * (b & 7) + (i5 & 1), rb = i5 >> 1;
  const unsigned l = threadIdx.x & 63, w = __builtin_amdgcn_readfirstlane(threadIdx.x >> 6);
  const unsigned ldsb = (unsigned)(size_t)lds;
  const unsigned i16 = l & 15, g = l >> 4, p = l & 3, q = (l >> 2) & 3;
  const __amdgpu_buffer_rsrc_t srdK = __builtin_amdgcn_make_buffer_rsrc((void*)((const char*)kc + (size_t)kvh * 524288), (short)0, 524288, 0x00020000);
  const __amdgpu_buffer_rsrc_t srdV = __builtin_amdgcn_make_buffer_rsrc((void*)((const char*)vc + (size_t)kvh * 524288 - 1024), (short)0, 524288 + 1024, 0x00020000);
  const __amdgpu_buffer_rsrc_t srdW = __builtin_amdgcn_make_buffer_rsrc((void*)((const char*)wq + (size_t)kvh * 512 - 1024), (short)0, (int)(576 * 8192 - kvh * 512 + 1024), 0x00020000);
  const __amdgpu_buffer_rsrc_t srdH = __builtin_amdgcn_make_buffer_rsrc((void*)hf, (short)0, 64 * 36 * 1024, 0x00020000);
  const __amdgpu_buffer_rsrc_t srdG = __builtin_amdgcn_make_buffer_rsrc((void*)((w >> 1) ? wu : wg), (short)0, b < 144 ? 576 * 9216 : 0, 0x00020000);
  const __amdgpu_buffer_rsrc_t srdDn = __builtin_amdgcn_make_buffer_rsrc((void*)wd, (short)0, b < 144 ? 4608 * 1152 : 0, 0x00020000);
  const __amdgpu_buffer_rsrc_t srdWo = __builtin_amdgcn_make_buffer_rsrc((void*)((const char*)wo + (size_t)kvh * 256 * 1152), (short)0, 256 * 1152, 0x00020000);
  const __amdgpu_buffer_rsrc_t srdP = __builtin_amdgcn_make_buffer_rsrc((void*)part, (short)0, 16 * PSTR, 0x00020000);
  const unsigned ob0 = ldsb + 1024 * (g >> 1) + 32 * (4 * (g & 1) + q) + 8 * p;
  const unsigned obase = (64 * w + ((l >> 1) & 7)) * 1152 + 32 * g + 16 * (l & 1);
  const unsigned osrc0 = obase;
  const unsigned kb0 = ldsb + 256 * i16 + 16 * (g ^ i16);
  const unsigned vb = ldsb + 65536 + 2048 * (g >> 1) + 32 * (4 * (g & 1) + q) + 8 * p;
  const unsigned krow = 32 * w + g;
  const unsigned ksrc0 = krow * 256 + 16 * (i16 ^ g);
  const unsigned vsrc0 = (32 * w + ((l >> 1) & 7)) * 256 + 32 * g + 16 * (l & 1) + 1024;
  const unsigned wbase = (32 * (w & 1) + ((l >> 1) & 7)) * 8192 + (w >> 1) * 256 + 32 * g + 16 * (l & 1) + 1024;
  const unsigned wsrc0 = wbase;
  const unsigned g32 = 4 * rb + w;
  const unsigned hoff = 2 * g32 * 18 * 1024 + 16 * l;
  const unsigned poff = kvh * PSTR + 2 * g32 * 18 * 1024 + 16 * l;
  const unsigned gsrc = (l >> 1) * 9216 + 64 * ((b & 7) * 18 + (b >> 3)) + 32 * (w & 1) + 16 * (l & 1), dsrc = (32 * ((b & 7) * 18 + (b >> 3)) + 8 * w + ((l >> 1) & 7)) * 1152 + 32 * g + 16 * (l & 1);
  const unsigned jm = (b & 7) * 18 + (b >> 3);
  const char* pfb = w == 0 ? (const char*)wg : w == 1 ? (const char*)wu : w == 2 ? (const char*)wd + (size_t)jm * 32 * 1152 : (const char*)wo + (size_t)kvh * 256 * 1152 + rb * 18432;
  const int pfn = w < 2 ? (b < 144 ? 576 * 9216 : 0) : w == 2 ? (b < 144 ? 32 * 1152 : 0) : 18432;
  const __amdgpu_buffer_rsrc_t srdPF = __builtin_amdgcn_make_buffer_rsrc((void*)pfb, (short)0, pfn, 0x00020000);
  const unsigned pfv = w < 2 ? l * 9216 + 64 * jm : l * 128, pfstep = w < 2 ? 64 * 9216 : 64 * 128;
  const unsigned ones = (l & 3) == 0 ? 0x3f803f80u : 0u;
  const unsigned tag = head_phase(lds, ws, x, cvec, wm, rb, kvh, w, [&](unsigned wp) {
    const unsigned wpu = __builtin_amdgcn_readfirstlane(wp);
    const unsigned ksrcp = (32 * wpu + g) * 256 + 16 * (i16 ^ g);
    const unsigned wsrcp = (32 * (wpu & 1) + ((l >> 1) & 7)) * 8192 + (wpu >> 1) * 256 + 32 * g + 16 * (l & 1) + 1024;
    asm volatile(
    "v_xor_b32 v242, 64, %[ksrc0]\n\t"
    "v_xor_b32 v243, 128, %[ksrc0]\n\t"
    "v_xor_b32 v244, 192, %[ksrc0]\n\t"
    "v_subrev_u32 v247, 896, %[wsrc0]\n\t"
    "v_add_u32 v250, 0xf800, %[wsrc0]\n\t"
    "v_add_u32 v251, 0xf480, %[wsrc0]\n\t"
    "s_lshl_b32 s45, %[wid], 13\n\t"
    "s_add_u32 s42, %[ldsb], s45\n\t"
    "s_add_u32 s43, s42, 65536\n\t"
    "s_add_u32 m0, s43, 0\n\t"
    "s_mov_b32 s45, 0\n\t"
    "buffer_load_dwordx4 %[wsrc0], %[srdW], s45 offen offset:0 lds\n\t"
    "buffer_load_dwordx4 v247, %[srdW], s45 offen offset:1024 lds\n\t"
    "buffer_load_dwordx4 v250, %[srdW], s45 offen offset:2048 lds\n\t"
    "buffer_load_dwordx4 v251, %[srdW], s45 offen offset:3072 lds\n\t"
    "s_add_u32 m0, s43, 4096\n\t"
    "s_mov_b32 s45, 131072\n\t"
    "buffer_load_dwordx4 %[wsrc0], %[srdW], s45 offen offset:0 lds\n\t"
    "buffer_load_dwordx4 v247, %[srdW], s45 offen offset:1024 lds\n\t"
    "buffer_load_dwordx4 v250, %[srdW], s45 offen offset:2048 lds\n\t"
    "buffer_load_dwordx4 v251, %[srdW], s45 offen offset:3072 lds\n\t"
    "s_add_u32 m0, s43, 32768\n\t"
    "s_mov_b32 s45, 524288\n\t"
    "buffer_load_dwordx4 %[wsrc0], %[srdW], s45 offen offset:0 lds\n\t"
    "buffer_load_dwordx4 v247, %[srdW], s45 offen offset:1024 lds\n\t"
    "buffer_load_dwordx4 v250, %[srdW], s45 offen offset:2048 lds\n\t"
    "buffer_load_dwordx4 v251, %[srdW], s45 offen offset:3072 lds\n\t"
    "s_add_u32 m0, s43, 36864\n\t"
    "s_mov_b32 s45, 655360\n\t"
    "buffer_load_dwordx4 %[wsrc0], %[srdW], s45 offen offset:0 lds\n\t"
    "buffer_load_dwordx4 v247, %[srdW], s45 offen offset:1024 lds\n\t"
    "buffer_load_dwordx4 v250, %[srdW], s45 offen offset:2048 lds\n\t"
    "buffer_load_dwordx4 v251, %[srdW], s45 offen offset:3072 lds\n\t"
    "s_add_u32 m0, s43, 65536\n\t"
    "s_mov_b32 s45, 1048576\n\t"
    "buffer_load_dwordx4 %[wsrc0], %[srdW], s45 offen offset:0 lds\n\t"
    "buffer_load_dwordx4 v247, %[srdW], s45 offen offset:1024 lds\n\t"
    "buffer_load_dwordx4 v250, %[srdW], s45 offen offset:2048 lds\n\t"
    "buffer_load_dwordx4 v251, %[srdW], s45 offen offset:3072 lds\n\t"
    "s_add_u32 m0, s43, 69632\n\t"
    "s_mov_b32 s45, 1179648\n\t"
    "buffer_load_dwordx4 %[wsrc0], %[srdW], s45 offen offset:0 lds\n\t"
    "buffer_load_dwordx4 v247, %[srdW], s45 offen offset:1024 lds\n\t"
    "buffer_load_dwordx4 v250, %[srdW], s45 offen offset:2048 lds\n\t"
    "buffer_load_dwordx4 v251, %[srdW], s45 offen offset:3072 lds\n\t"
    "s_add_u32 m0, s42, 0\n\t"
    "s_mov_b32 s47, 0\n\t"
    "buffer_load_dwordx4 %[ksrc0], %[srdK], s47 offen offset:0 lds\n\t"
    "buffer_load_dwordx4 v242, %[srdK], s47 offen offset:1024 lds\n\t"
    "buffer_load_dwordx4 v243, %[srdK], s47 offen offset:2048 lds\n\t"
    "buffer_load_dwordx4 v244, %[srdK], s47 offen offset:3072 lds\n\t"
    "s_add_u32 m0, s42, 4096\n\t"
    "s_mov_b32 s47, 4096\n\t"
    "buffer_load_dwordx4 %[ksrc0], %[srdK], s47 offen offset:0 lds\n\t"
    "buffer_load_dwordx4 v242, %[srdK], s47 offen offset:1024 lds\n\t"
    "buffer_load_dwordx4 v243, %[srdK], s47 offen offset:2048 lds\n\t"
    "buffer_load_dwordx4 v244, %[srdK], s47 offen offset:3072 lds\n\t"
      :
      : [srdK] "s"(srdK), [srdW] "s"(srdW), [wid] "s"(wpu), [ldsb] "s"(ldsb), [ksrc0] "v"(ksrcp), [wsrc0] "v"(wsrcp)
      : "v242", "v243", "v244", "v247", "v250", "v251", "s40", "s41", "s42", "s43", "s44", "s45", "s46", "s47", "s48", "s49", "s50", "s51", "scc", "memory");
  });
  asm volatile(
    "s_mov_b32 s60, 0x3e0293ee\n\t"
    "s_mov_b32 s56, 0x3db504f3\n\t"
    "s_mov_b32 s57, 0x3db504f3\n\t"
    "s_mov_b32 s58, 0x3fb8aa3b\n\t"
    "s_mov_b32 s59, 0x3fb8aa3b\n\t"
    "v_add_u32 v236, 0x2000, %[osrc0]\n\t"
    "v_add_u32 v237, 0x4000, %[osrc0]\n\t"
    "v_add_u32 v238, 0x6000, %[osrc0]\n\t"
    "v_add_u32 v239, 0x10000, %[ob0]\n\t"
    "v_add_u32 v240, 0x20000, %[ob0]\n\t"
    "v_add_u32 v241, 0x10000, %[vb]\n\t"
    "v_xor_b32 v242, 64, %[ksrc0]\n\t"
    "v_xor_b32 v243, 128, %[ksrc0]\n\t"
    "v_xor_b32 v244, 192, %[ksrc0]\n\t"
    "v_subrev_u32 v245, 896, %[vsrc0]\n\t"
    "v_subrev_u32 v247, 896, %[wsrc0]\n\t"
    "v_add_u32 v250, 0xf800, %[wsrc0]\n\t"
    "v_add_u32 v251, 0xf480, %[wsrc0]\n\t"
    "s_lshl_b32 s45, %[wid], 13\n\t"
    "s_add_u32 s42, %[ldsb], s45\n\t"
    "s_add_u32 s43, s42, 65536\n\t"
    "s_mov_b32 s49, s42\n\t"
    "s_lshl_b32 s45, %[wid], 10\n\t"
    "s_add_u32 s50, %[ldsb], s45\n\t"
    "s_mov_b32 s51, 0\n\t"
    "s_mov_b32 s61, 0\n\t"
    "s_mov_b32 s46, 0\n\t"
    "buffer_load_dwordx4 a[0:3], %[hoff], %[srdH], s46 offen offset:0 sc1\n\t"
    "s_mov_b32 s46, 18432\n\t"
    "buffer_load_dwordx4 a[4:7], %[hoff], %[srdH], s46 offen offset:0 sc1\n\t"
    "s_mov_b32 s46, 0\n\t"
    "buffer_load_dwordx4 a[8:11], %[hoff], %[srdH], s46 offen offset:1024 sc1\n\t"
    "s_mov_b32 s46, 18432\n\t"
    "buffer_load_dwordx4 a[12:15], %[hoff], %[srdH], s46 offen offset:1024 sc1\n\t"
    "s_mov_b32 s46, 0\n\t"
    "buffer_load_dwordx4 a[16:19], %[hoff], %[srdH], s46 offen offset:2048 sc1\n\t"
    "s_mov_b32 s46, 18432\n\t"
    "buffer_load_dwordx4 a[20:23], %[hoff], %[srdH], s46 offen offset:2048 sc1\n\t"
    "s_mov_b32 s46, 0\n\t"
    "buffer_load_dwordx4 a[24:27], %[hoff], %[srdH], s46 offen offset:3072 sc1\n\t"
    "s_mov_b32 s46, 18432\n\t"
    "buffer_load_dwordx4 a[28:31], %[hoff], %[srdH], s46 offen offset:3072 sc1\n\t"
    "v_mov_b32 v252, 0\n\t"
    "v_mov_b32 v253, 0\n\t"
    "v_mov_b32 v254, 0\n\t"
    "v_mov_b32 v255, 0\n\t"
    "v_xor_b32 v228, 0, %[kb0]\n\t"
    "v_xor_b32 v229, 64, %[kb0]\n\t"
    "v_xor_b32 v230, 128, %[kb0]\n\t"
    "v_xor_b32 v231, 192, %[kb0]\n\t"
    "v_mov_b32 v224, %[ones]\n\t"
    "v_mov_b32 v225, %[ones]\n\t"
    "v_mov_b32 v226, %[ones]\n\t"
    "v_mov_b32 v227, %[ones]\n\t"
    "s_waitcnt vmcnt(4)\n\t"
    "ds_read_b64_tr_b16 v[160:161], %[vb] offset:0\n\t"
    "ds_read_b64_tr_b16 v[162:163], %[vb] offset:4096\n\t"
    "ds_read_b64_tr_b16 v[164:165], %[vb] offset:8192\n\t"
    "ds_read_b64_tr_b16 v[166:167], %[vb] offset:12288\n\t"
    "ds_read_b64_tr_b16 v[168:169], %[vb] offset:256\n\t"
    "ds_read_b64_tr_b16 v[170:171], %[vb] offset:4352\n\t"
    "ds_read_b64_tr_b16 v[172:173], %[vb] offset:8448\n\t"
    "ds_read_b64_tr_b16 v[174:175], %[vb] offset:12544\n\t"
    "ds_read_b64_tr_b16 v[176:177], %[vb] offset:512\n\t"
    "ds_read_b64_tr_b16 v[178:179], %[vb] offset:4608\n\t"
    "ds_read_b64_tr_b16 v[180:181], %[vb] offset:8704\n\t"
    "ds_read_b64_tr_b16 v[182:183], %[vb] offset:12800\n\t"
    "ds_read_b64_tr_b16 v[184:185], %[vb] offset:768\n\t"
    "ds_read_b64_tr_b16 v[186:187], %[vb] offset:4864\n\t"
    "ds_read_b64_tr_b16 v[188:189], %[vb] offset:8960\n\t"
    "ds_read_b64_tr_b16 v[190:191], %[vb] offset:13056\n\t"
    "s_waitcnt lgkmcnt(8)\n\t"
    "v_mfma_f32_16x16x32_bf16 v[32:35], v[160:163], a[0:3], 0\n\t"
    "ds_read_b64_tr_b16 v[192:193], %[vb] offset:1024\n\t"
    "v_mfma_f32_16x16x32_bf16 v[32:35], v[164:167], a[8:11], v[32:35]\n\t"
    "ds_read_b64_tr_b16 v[194:195], %[vb] offset:5120\n\t"
    "s_mov_b32 s46, 4096\n\t"
    "v_mfma_f32_16x16x32_bf16 v[36:39], v[160:163], a[4:7], 0\n\t"
    "ds_read_b64_tr_b16 v[196:197], %[vb] offset:9216\n\t"
    "buffer_load_dwordx4 a[32:35], %[hoff], %[srdH], s46 offen offset:0 sc1\n\t"
    "v_mfma_f32_16x16x32_bf16 v[36:39], v[164:167], a[12:15], v[36:39]\n\t"
    "ds_read_b64_tr_b16 v[198:199], %[vb] offset:13312\n\t"
    "v_mfma_f32_16x16x32_bf16 v[40:43], v[168:171], a[0:3], 0\n\t"
    "ds_read_b64_tr_b16 v[200:201], %[vb] offset:1280\n\t"
    "v_mfma_f32_16x16x32_bf16 v[40:43], v[172:175], a[8:11], v[40:43]\n\t"
    "ds_read_b64_tr_b16 v[202:203], %[vb] offset:5376\n\t"
    "s_mov_b32 s46, 22528\n\t"
    "v_mfma_f32_16x16x32_bf16 v[44:47], v[168:171], a[4:7], 0\n\t"
    "ds_read_b64_tr_b16 v[204:205], %[vb] offset:9472\n\t"
    "buffer_load_dwordx4 a[36:39], %[hoff], %[srdH], s46 offen offset:0 sc1\n\t"
    "v_mfma_f32_16x16x32_bf16 v[44:47], v[172:175], a[12:15], v[44:47]\n\t"
    "ds_read_b64_tr_b16 v[206:207], %[vb] offset:13568\n\t"
    "s_waitcnt lgkmcnt(8)\n\t"
    "v_mfma_f32_16x16x32_bf16 v[48:51], v[176:179], a[0:3], 0\n\t"
    "ds_read_b64_tr_b16 v[208:209], %[vb] offset:1536\n\t"
    "v_mfma_f32_16x16x32_bf16 v[48:51], v[180:183], a[8:11], v[48:51]\n\t"
    "ds_read_b64_tr_b16 v[210:211], %[vb] offset:5632\n\t"
    "s_mov_b32 s46, 4096\n\t"
    "v_mfma_f32_16x16x32_bf16 v[52:55], v[176:179], a[4:7], 0\n\t"
    "ds_read_b64_tr_b16 v[212:213], %[vb] offset:9728\n\t"
    "buffer_load_dwordx4 a[40:43], %[hoff], %[srdH], s46 offen offset:1024 sc1\n\t"
    "v_mfma_f32_16x16x32_bf16 v[52:55], v[180:183], a[12:15], v[52:55]\n\t"
    "ds_read_b64_tr_b16 v[214:215], %[vb] offset:13824\n\t"
    "v_mfma_f32_16x16x32_bf16 v[56:59], v[184:187], a[0:3], 0\n\t"
    "ds_read_b64_tr_b16 v[216:217], %[vb] offset:1792\n\t"
    "v_mfma_f32_16x16x32_bf16 v[56:59], v[188:191], a[8:11], v[56:59]\n\t"
    "ds_read_b64_tr_b16 v[218:219], %[vb] offset:5888\n\t"
    "s_mov_b32 s46, 22528\n\t"
    "v_mfma_f32_16x16x32_bf16 v[60:63], v[184:187], a[4:7], 0\n\t"
    "ds_read_b64_tr_b16 v[220:221], %[vb] offset:9984\n\t"
    "buffer_load_dwordx4 a[44:47], %[hoff], %[srdH], s46 offen offset:1024 sc1\n\t"
    "v_mfma_f32_16x16x32_bf16 v[60:63], v[188:191], a[12:15], v[60:63]\n\t"
    "ds_read_b64_tr_b16 v[222:223], %[vb] offset:14080\n\t"
    "s_waitcnt lgkmcnt(8)\n\t"
    "v_mfma_f32_16x16x32_bf16 v[64:67], v[192:195], a[0:3], 0\n\t"
    "ds_read_b64_tr_b16 v[160:161], %[vb] offset:16384\n\t"
    "v_mfma_f32_16x16x32_bf16 v[64:67], v[196:199], a[8:11], v[64:67]\n\t"
    "ds_read_b64_tr_b16 v[162:163], %[vb] offset:20480\n\t"
    "v_mfma_f32_16x16x32_bf16 v[68:71], v[192:195], a[4:7], 0\n\t"
    "ds_read_b64_tr_b16 v[164:165], %[vb] offset:24576\n\t"
    "v_mfma_f32_16x16x32_bf16 v[68:71], v[196:199], a[12:15], v[68:71]\n\t"
    "ds_read_b64_tr_b16 v[166:167], %[vb] offset:28672\n\t"
    "v_mfma_f32_16x16x32_bf16 v[72:75], v[200:203], a[0:3], 0\n\t"
    "ds_read_b64_tr_b16 v[168:169], %[vb] offset:16640\n\t"
    "v_mfma_f32_16x16x32_bf16 v[72:75], v[204:207], a[8:11], v[72:75]\n\t"
    "ds_read_b64_tr_b16 v[170:171], %[vb] offset:20736\n\t"
    "v_mfma_f32_16x16x32_bf16 v[76:79], v[200:203], a[4:7], 0\n\t"
    "ds_read_b64_tr_b16 v[172:173], %[vb] offset:24832\n\t"
    "v_mfma_f32_16x16x32_bf16 v[76:79], v[204:207], a[12:15], v[76:79]\n\t"
    "ds_read_b64_tr_b16 v[174:175], %[vb] offset:28928\n\t"
    "s_waitcnt lgkmcnt(8)\n\t"
    "v_mfma_f32_16x16x32_bf16 v[80:83], v[208:211], a[0:3], 0\n\t"
    "ds_read_b64_tr_b16 v[176:177], %[vb] offset:16896\n\t"
    "v_mfma_f32_16x16x32_bf16 v[80:83], v[212:215], a[8:11], v[80:83]\n\t"
    "ds_read_b64_tr_b16 v[178:179], %[vb] offset:20992\n\t"
    "v_mfma_f32_16x16x32_bf16 v[84:87], v[208:211], a[4:7], 0\n\t"
    "ds_read_b64_tr_b16 v[180:181], %[vb] offset:25088\n\t"
    "v_mfma_f32_16x16x32_bf16 v[84:87], v[212:215], a[12:15], v[84:87]\n\t"
    "ds_read_b64_tr_b16 v[182:183], %[vb] offset:29184\n\t"
    "v_mfma_f32_16x16x32_bf16 v[88:91], v[216:219], a[0:3], 0\n\t"
    "ds_read_b64_tr_b16 v[184:185], %[vb] offset:17152\n\t"
    "v_mfma_f32_16x16x32_bf16 v[88:91], v[220:223], a[8:11], v[88:91]\n\t"
    "ds_read_b64_tr_b16 v[186:187], %[vb] offset:21248\n\t"
    "v_mfma_f32_16x16x32_bf16 v[92:95], v[216:219], a[4:7], 0\n\t"
    "ds_read_b64_tr_b16 v[188:189], %[vb] offset:25344\n\t"
    "v_mfma_f32_16x16x32_bf16 v[92:95], v[220:223], a[12:15], v[92:95]\n\t"
    "ds_read_b64_tr_b16 v[190:191], %[vb] offset:29440\n\t"
    "s_waitcnt lgkmcnt(8)\n\t"
    "v_mfma_f32_16x16x32_bf16 v[96:99], v[160:163], a[0:3], 0\n\t"
    "ds_read_b64_tr_b16 v[192:193], %[vb] offset:17408\n\t"
    "v_mfma_f32_16x16x32_bf16 v[96:99], v[164:167], a[8:11], v[96:99]\n\t"
    "ds_read_b64_tr_b16 v[194:195], %[vb] offset:21504\n\t"
    "v_mfma_f32_16x16x32_bf16 v[100:103], v[160:163], a[4:7], 0\n\t"
    "ds_read_b64_tr_b16 v[196:197], %[vb] offset:25600\n\t"
    "v_mfma_f32_16x16x32_bf16 v[100:103], v[164:167], a[12:15], v[100:103]\n\t"
    "ds_read_b64_tr_b16 v[198:199], %[vb] offset:29696\n\t"
    "v_mfma_f32_16x16x32_bf16 v[104:107], v[168:171], a[0:3], 0\n\t"
    "ds_read_b64_tr_b16 v[200:201], %[vb] offset:17664\n\t"
    "v_mfma_f32_16x16x32_bf16 v[104:107], v[172:175], a[8:11], v[104:107]\n\t"
    "ds_read_b64_tr_b16 v[202:203], %[vb] offset:21760\n\t"
    "v_mfma_f32_16x16x32_bf16 v[108:111], v[168:171], a[4:7], 0\n\t"
    "ds_read_b64_tr_b16 v[204:205], %[vb] offset:25856\n\t"
    "v_mfma_f32_16x16x32_bf16 v[108:111], v[172:175], a[12:15], v[108:111]\n\t"
    "ds_read_b64_tr_b16 v[206:207], %[vb] offset:29952\n\t"
    "s_waitcnt lgkmcnt(8)\n\t"
    "v_mfma_f32_16x16x32_bf16 v[112:115], v[176:179], a[0:3], 0\n\t"
    "ds_read_b64_tr_b16 v[208:209], %[vb] offset:17920\n\t"
    "v_mfma_f32_16x16x32_bf16 v[112:115], v[180:183], a[8:11], v[112:115]\n\t"
    "ds_read_b64_tr_b16 v[210:211], %[vb] offset:22016\n\t"
    "v_mfma_f32_16x16x32_bf16 v[116:119], v[176:179], a[4:7], 0\n\t"
    "ds_read_b64_tr_b16 v[212:213], %[vb] offset:26112\n\t"
    "v_mfma_f32_16x16x32_bf16 v[116:119], v[180:183], a[12:15], v[116:119]\n\t"
    "ds_read_b64_tr_b16 v[214:215], %[vb] offset:30208\n\t"
    "v_mfma_f32_16x16x32_bf16 v[120:123], v[184:187], a[0:3], 0\n\t"
    "ds_read_b64_tr_b16 v[216:217], %[vb] offset:18176\n\t"
    "v_mfma_f32_16x16x32_bf16 v[120:123], v[188:191], a[8:11], v[120:123]\n\t"
    "ds_read_b64_tr_b16 v[218:219], %[vb] offset:22272\n\t"
    "v_mfma_f32_16x16x32_bf16 v[124:127], v[184:187], a[4:7], 0\n\t"
    "ds_read_b64_tr_b16 v[220:221], %[vb] offset:26368\n\t"
    "v_mfma_f32_16x16x32_bf16 v[124:127], v[188:191], a[12:15], v[124:127]\n\t"
    "ds_read_b64_tr_b16 v[222:223], %[vb] offset:30464\n\t"
    "s_waitcnt lgkmcnt(0)\n\t"
    "s_barrier\n\t"
    "s_waitcnt lgkmcnt(8)\n\t"
    "v_mfma_f32_16x16x32_bf16 v[128:131], v[192:195], a[0:3], 0\n\t"
    "ds_read_b64_tr_b16 v[160:161], %[vb] offset:32768\n\t"
    "v_mfma_f32_16x16x32_bf16 v[128:131], v[196:199], a[8:11], v[128:131]\n\t"
    "ds_read_b64_tr_b16 v[162:163], %[vb] offset:36864\n\t"
    "s_add_u32 m0, s43, 0\n\t"
    "s_mov_b32 s45, 1572864\n\t"
    "v_mfma_f32_16x16x32_bf16 v[132:135], v[192:195], a[4:7], 0\n\t"
    "ds_read_b64_tr_b16 v[164:165], %[vb] offset:40960\n\t"
    "buffer_load_dwordx4 %[wsrc0], %[srdW], s45 offen offset:0 lds\n\t"
    "v_mfma_f32_16x16x32_bf16 v[132:135], v[196:199], a[12:15], v[132:135]\n\t"
    "ds_read_b64_tr_b16 v[166:167], %[vb] offset:45056\n\t"
    "v_mfma_f32_16x16x32_bf16 v[136:139], v[200:203], a[0:3], 0\n\t"
    "ds_read_b64_tr_b16 v[168:169], %[vb] offset:33024\n\t"
    "v_mfma_f32_16x16x32_bf16 v[136:139], v[204:207], a[8:11], v[136:139]\n\t"
    "ds_read_b64_tr_b16 v[170:171], %[vb] offset:37120\n\t"
    "v_mfma_f32_16x16x32_bf16 v[140:143], v[200:203], a[4:7], 0\n\t"
    "ds_read_b64_tr_b16 v[172:173], %[vb] offset:41216\n\t"
    "buffer_load_dwordx4 v247, %[srdW], s45 offen offset:1024 lds\n\t"
    "v_mfma_f32_16x16x32_bf16 v[140:143], v[204:207], a[12:15], v[140:143]\n\t"
    "ds_read_b64_tr_b16 v[174:175], %[vb] offset:45312\n\t"
    "s_waitcnt lgkmcnt(8)\n\t"
    "v_mfma_f32_16x16x32_bf16 v[144:147], v[208:211], a[0:3], 0\n\t"
    "ds_read_b64_tr_b16 v[176:177], %[vb] offset:33280\n\t"
    "v_mfma_f32_16x16x32_bf16 v[144:147], v[212:215], a[8:11], v[144:147]\n\t"
    "ds_read_b64_tr_b16 v[178:179], %[vb] offset:37376\n\t"
    "v_mfma_f32_16x16x32_bf16 v[148:151], v[208:211], a[4:7], 0\n\t"
    "ds_read_b64_tr_b16 v[180:181], %[vb] offset:41472\n\t"
    "buffer_load_dwordx4 v250, %[srdW], s45 offen offset:2048 lds\n\t"
    "v_mfma_f32_16x16x32_bf16 v[148:151], v[212:215], a[12:15], v[148:151]\n\t"
    "ds_read_b64_tr_b16 v[182:183], %[vb] offset:45568\n\t"
    "v_mfma_f32_16x16x32_bf16 v[152:155], v[216:219], a[0:3], 0\n\t"
    "ds_read_b64_tr_b16 v[184:185], %[vb] offset:33536\n\t"
    "v_mfma_f32_16x16x32_bf16 v[152:155], v[220:223], a[8:11], v[152:155]\n\t"
    "ds_read_b64_tr_b16 v[186:187], %[vb] offset:37632\n\t"
    "v_mfma_f32_16x16x32_bf16 v[156:159], v[216:219], a[4:7], 0\n\t"
    "ds_read_b64_tr_b16 v[188:189], %[vb] offset:41728\n\t"
    "buffer_load_dwordx4 v251, %[srdW], s45 offen offset:3072 lds\n\t"
    "v_mfma_f32_16x16x32_bf16 v[156:159], v[220:223], a[12:15], v[156:159]\n\t"
    "ds_read_b64_tr_b16 v[190:191], %[vb] offset:45824\n\t"
    "s_waitcnt vmcnt(10)\n\t"
    "s_waitcnt vmcnt(8)\n\t"
    "s_waitcnt lgkmcnt(8)\n\t"
    "v_mfma_f32_16x16x32_bf16 v[32:35], v[160:163], a[16:19], v[32:35]\n\t"
    "ds_read_b64_tr_b16 v[192:193], %[vb] offset:33792\n\t"
    "v_mfma_f32_16x16x32_bf16 v[32:35], v[164:167], a[24:27], v[32:35]\n\t"
    "ds_read_b64_tr_b16 v[194:195], %[vb] offset:37888\n\t"
    "s_add_u32 m0, s43, 4096\n\t"
    "s_mov_b32 s45, 1703936\n\t"
    "v_mfma_f32_16x16x32_bf16 v[36:39], v[160:163], a[20:23], v[36:39]\n\t"
    "ds_read_b64_tr_b16 v[196:197], %[vb] offset:41984\n\t"
    "buffer_load_dwordx4 %[wsrc0], %[srdW], s45 offen offset:0 lds\n\t"
    "v_mfma_f32_16x16x32_bf16 v[36:39], v[164:167], a[28:31], v[36:39]\n\t"
    "ds_read_b64_tr_b16 v[198:199], %[vb] offset:46080\n\t"
    "v_mfma_f32_16x16x32_bf16 v[40:43], v[168:171], a[16:19], v[40:43]\n\t"
    "ds_read_b64_tr_b16 v[200:201], %[vb] offset:34048\n\t"
    "v_mfma_f32_16x16x32_bf16 v[40:43], v[172:175], a[24:27], v[40:43]\n\t"
    "ds_read_b64_tr_b16 v[202:203], %[vb] offset:38144\n\t"
    "v_mfma_f32_16x16x32_bf16 v[44:47], v[168:171], a[20:23], v[44:47]\n\t"
    "ds_read_b64_tr_b16 v[204:205], %[vb] offset:42240\n\t"
    "buffer_load_dwordx4 v247, %[srdW], s45 offen offset:1024 lds\n\t"
    "v_mfma_f32_16x16x32_bf16 v[44:47], v[172:175], a[28:31], v[44:47]\n\t"
    "ds_read_b64_tr_b16 v[206:207], %[vb] offset:46336\n\t"
    "s_waitcnt lgkmcnt(8)\n\t"
    "v_mfma_f32_16x16x32_bf16 v[48:51], v[176:179], a[16:19], v[48:51]\n\t"
    "ds_read_b64_tr_b16 v[208:209], %[vb] offset:34304\n\t"
    "v_mfma_f32_16x16x32_bf16 v[48:51], v[180:183], a[24:27], v[48:51]\n\t"
    "ds_read_b64_tr_b16 v[210:211], %[vb] offset:38400\n\t"
    "v_mfma_f32_16x16x32_bf16 v[52:55], v[176:179], a[20:23], v[52:55]\n\t"
    "ds_read_b64_tr_b16 v[212:213], %[vb] offset:42496\n\t"
    "buffer_load_dwordx4 v250, %[srdW], s45 offen offset:2048 lds\n\t"
    "v_mfma_f32_16x16x32_bf16 v[52:55], v[180:183], a[28:31], v[52:55]\n\t"
    "ds_read_b64_tr_b16 v[214:215], %[vb] offset:46592\n\t"
    "v_mfma_f32_16x16x32_bf16 v[56:59], v[184:187], a[16:19], v[56:59]\n\t"
    "ds_read_b64_tr_b16 v[216:217], %[vb] offset:34560\n\t"
    "v_mfma_f32_16x16x32_bf16 v[56:59], v[188:191], a[24:27], v[56:59]\n\t"
    "ds_read_b64_tr_b16 v[218:219], %[vb] offset:38656\n\t"
    "v_mfma_f32_16x16x32_bf16 v[60:63], v[184:187], a[20:23], v[60:63]\n\t"
    "ds_read_b64_tr_b16 v[220:221], %[vb] offset:42752\n\t"
    "buffer_load_dwordx4 v251, %[srdW], s45 offen offset:3072 lds\n\t"
    "v_mfma_f32_16x16x32_bf16 v[60:63], v[188:191], a[28:31], v[60:63]\n\t"
    "ds_read_b64_tr_b16 v[222:223], %[vb] offset:46848\n\t"
    "s_waitcnt lgkmcnt(8)\n\t"
    "v_mfma_f32_16x16x32_bf16 v[64:67], v[192:195], a[16:19], v[64:67]\n\t"
    "ds_read_b64_tr_b16 v[160:161], %[vb] offset:49152\n\t"
    "v_mfma_f32_16x16x32_bf16 v[64:67], v[196:199], a[24:27], v[64:67]\n\t"
    "ds_read_b64_tr_b16 v[162:163], %[vb] offset:53248\n\t"
    "s_mov_b32 s46, 4096\n\t"
    "v_mfma_f32_16x16x32_bf16 v[68:71], v[192:195], a[20:23], v[68:71]\n\t"
    "ds_read_b64_tr_b16 v[164:165], %[vb] offset:57344\n\t"
    "buffer_load_dwordx4 a[48:51], %[hoff], %[srdH], s46 offen offset:2048 sc1\n\t"
    "v_mfma_f32_16x16x32_bf16 v[68:71], v[196:199], a[28:31], v[68:71]\n\t"
    "ds_read_b64_tr_b16 v[166:167], %[vb] offset:61440\n\t"
    "v_mfma_f32_16x16x32_bf16 v[72:75], v[200:203], a[16:19], v[72:75]\n\t"
    "ds_read_b64_tr_b16 v[168:169], %[vb] offset:49408\n\t"
    "v_mfma_f32_16x16x32_bf16 v[72:75], v[204:207], a[24:27], v[72:75]\n\t"
    "ds_read_b64_tr_b16 v[170:171], %[vb] offset:53504\n\t"
    "s_mov_b32 s46, 22528\n\t"
    "v_mfma_f32_16x16x32_bf16 v[76:79], v[200:203], a[20:23], v[76:79]\n\t"
    "ds_read_b64_tr_b16 v[172:173], %[vb] offset:57600\n\t"
    "buffer_load_dwordx4 a[52:55], %[hoff], %[srdH], s46 offen offset:2048 sc1\n\t"
    "v_mfma_f32_16x16x32_bf16 v[76:79], v[204:207], a[28:31], v[76:79]\n\t"
    "ds_read_b64_tr_b16 v[174:175], %[vb] offset:61696\n\t"
    "s_waitcnt lgkmcnt(8)\n\t"
    "v_mfma_f32_16x16x32_bf16 v[80:83], v[208:211], a[16:19], v[80:83]\n\t"
    "ds_read_b64_tr_b16 v[176:177], %[vb] offset:49664\n\t"
    "v_mfma_f32_16x16x32_bf16 v[80:83], v[212:215], a[24:27], v[80:83]\n\t"
    "ds_read_b64_tr_b16 v[178:179], %[vb] offset:53760\n\t"
    "s_mov_b32 s46, 4096\n\t"
    "v_mfma_f32_16x16x32_bf16 v[84:87], v[208:211], a[20:23], v[84:87]\n\t"
    "ds_read_b64_tr_b16 v[180:181], %[vb] offset:57856\n\t"
    "buffer_load_dwordx4 a[56:59], %[hoff], %[srdH], s46 offen offset:3072 sc1\n\t"
    "v_mfma_f32_16x16x32_bf16 v[84:87], v[212:215], a[28:31], v[84:87]\n\t"
    "ds_read_b64_tr_b16 v[182:183], %[vb] offset:61952\n\t"
    "v_mfma_f32_16x16x32_bf16 v[88:91], v[216:219], a[16:19], v[88:91]\n\t"
    "ds_read_b64_tr_b16 v[184:185], %[vb] offset:49920\n\t"
    "v_mfma_f32_16x16x32_bf16 v[88:91], v[220:223], a[24:27], v[88:91]\n\t"
    "ds_read_b64_tr_b16 v[186:187], %[vb] offset:54016\n\t"
    "s_mov_b32 s46, 22528\n\t"
    "v_mfma_f32_16x16x32_bf16 v[92:95], v[216:219], a[20:23], v[92:95]\n\t"
    "ds_read_b64_tr_b16 v[188:189], %[vb] offset:58112\n\t"
    "buffer_load_dwordx4 a[60:63], %[hoff], %[srdH], s46 offen offset:3072 sc1\n\t"
    "v_mfma_f32_16x16x32_bf16 v[92:95], v[220:223], a[28:31], v[92:95]\n\t"
    "ds_read_b64_tr_b16 v[190:191], %[vb] offset:62208\n\t"
    "s_waitcnt lgkmcnt(8)\n\t"
    "v_mfma_f32_16x16x32_bf16 v[96:99], v[160:163], a[16:19], v[96:99]\n\t"
    "ds_read_b64_tr_b16 v[192:193], %[vb] offset:50176\n\t"
    "v_mfma_f32_16x16x32_bf16 v[96:99], v[164:167], a[24:27], v[96:99]\n\t"
    "ds_read_b64_tr_b16 v[194:195], %[vb] offset:54272\n\t"
    "v_mfma_f32_16x16x32_bf16 v[100:103], v[160:163], a[20:23], v[100:103]\n\t"
    "ds_read_b64_tr_b16 v[196:197], %[vb] offset:58368\n\t"
    "v_mfma_f32_16x16x32_bf16 v[100:103], v[164:167], a[28:31], v[100:103]\n\t"
    "ds_read_b64_tr_b16 v[198:199], %[vb] offset:62464\n\t"
    "v_mfma_f32_16x16x32_bf16 v[104:107], v[168:171], a[16:19], v[104:107]\n\t"
    "ds_read_b64_tr_b16 v[200:201], %[vb] offset:50432\n\t"
    "v_mfma_f32_16x16x32_bf16 v[104:107], v[172:175], a[24:27], v[104:107]\n\t"
    "ds_read_b64_tr_b16 v[202:203], %[vb] offset:54528\n\t"
    "v_mfma_f32_16x16x32_bf16 v[108:111], v[168:171], a[20:23], v[108:111]\n\t"
    "ds_read_b64_tr_b16 v[204:205], %[vb] offset:58624\n\t"
    "v_mfma_f32_16x16x32_bf16 v[108:111], v[172:175], a[28:31], v[108:111]\n\t"
    "ds_read_b64_tr_b16 v[206:207], %[vb] offset:62720\n\t"
    "s_waitcnt lgkmcnt(8)\n\t"
    "v_mfma_f32_16x16x32_bf16 v[112:115], v[176:179], a[16:19], v[112:115]\n\t"
    "ds_read_b64_tr_b16 v[208:209], %[vb] offset:50688\n\t"
    "v_mfma_f32_16x16x32_bf16 v[112:115], v[180:183], a[24:27], v[112:115]\n\t"
    "ds_read_b64_tr_b16 v[210:211], %[vb] offset:54784\n\t"
    "v_mfma_f32_16x16x32_bf16 v[116:119], v[176:179], a[20:23], v[116:119]\n\t"
    "ds_read_b64_tr_b16 v[212:213], %[vb] offset:58880\n\t"
    "v_mfma_f32_16x16x32_bf16 v[116:119], v[180:183], a[28:31], v[116:119]\n\t"
    "ds_read_b64_tr_b16 v[214:215], %[vb] offset:62976\n\t"
    "v_mfma_f32_16x16x32_bf16 v[120:123], v[184:187], a[16:19], v[120:123]\n\t"
    "ds_read_b64_tr_b16 v[216:217], %[vb] offset:50944\n\t"
    "v_mfma_f32_16x16x32_bf16 v[120:123], v[188:191], a[24:27], v[120:123]\n\t"
    "ds_read_b64_tr_b16 v[218:219], %[vb] offset:55040\n\t"
    "v_mfma_f32_16x16x32_bf16 v[124:127], v[184:187], a[20:23], v[124:127]\n\t"
    "ds_read_b64_tr_b16 v[220:221], %[vb] offset:59136\n\t"
    "v_mfma_f32_16x16x32_bf16 v[124:127], v[188:191], a[28:31], v[124:127]\n\t"
    "ds_read_b64_tr_b16 v[222:223], %[vb] offset:63232\n\t"
    "s_waitcnt lgkmcnt(0)\n\t"
    "s_barrier\n\t"
    "s_waitcnt lgkmcnt(8)\n\t"
    "v_mfma_f32_16x16x32_bf16 v[128:131], v[192:195], a[16:19], v[128:131]\n\t"
    "ds_read_b64_tr_b16 v[160:161], v241 offset:0\n\t"
    "v_mfma_f32_16x16x32_bf16 v[128:131], v[196:199], a[24:27], v[128:131]\n\t"
    "ds_read_b64_tr_b16 v[162:163], v241 offset:4096\n\t"
    "s_add_u32 m0, s43, 32768\n\t"
    "s_mov_b32 s45, 2097152\n\t"
    "v_mfma_f32_16x16x32_bf16 v[132:135], v[192:195], a[20:23], v[132:135]\n\t"
    "ds_read_b64_tr_b16 v[164:165], v241 offset:8192\n\t"
    "buffer_load_dwordx4 %[wsrc0], %[srdW], s45 offen offset:0 lds\n\t"
    "v_mfma_f32_16x16x32_bf16 v[132:135], v[196:199], a[28:31], v[132:135]\n\t"
    "ds_read_b64_tr_b16 v[166:167], v241 offset:12288\n\t"
    "v_mfma_f32_16x16x32_bf16 v[136:139], v[200:203], a[16:19], v[136:139]\n\t"
    "ds_read_b64_tr_b16 v[168:169], v241 offset:256\n\t"
    "v_mfma_f32_16x16x32_bf16 v[136:139], v[204:207], a[24:27], v[136:139]\n\t"
    "ds_read_b64_tr_b16 v[170:171], v241 offset:4352\n\t"
    "v_mfma_f32_16x16x32_bf16 v[140:143], v[200:203], a[20:23], v[140:143]\n\t"
    "ds_read_b64_tr_b16 v[172:173], v241 offset:8448\n\t"
    "buffer_load_dwordx4 v247, %[srdW], s45 offen offset:1024 lds\n\t"
    "v_mfma_f32_16x16x32_bf16 v[140:143], v[204:207], a[28:31], v[140:143]\n\t"
    "ds_read_b64_tr_b16 v[174:175], v241 offset:12544\n\t"
    "s_waitcnt lgkmcnt(8)\n\t"
    "v_mfma_f32_16x16x32_bf16 v[144:147], v[208:211], a[16:19], v[144:147]\n\t"
    "ds_read_b64_tr_b16 v[176:177], v241 offset:512\n\t"
    "v_mfma_f32_16x16x32_bf16 v[144:147], v[212:215], a[24:27], v[144:147]\n\t"
    "ds_read_b64_tr_b16 v[178:179], v241 offset:4608\n\t"
    "v_mfma_f32_16x16x32_bf16 v[148:151], v[208:211], a[20:23], v[148:151]\n\t"
    "ds_read_b64_tr_b16 v[180:181], v241 offset:8704\n\t"
    "buffer_load_dwordx4 v250, %[srdW], s45 offen offset:2048 lds\n\t"
    "v_mfma_f32_16x16x32_bf16 v[148:151], v[212:215], a[28:31], v[148:151]\n\t"
    "ds_read_b64_tr_b16 v[182:183], v241 offset:12800\n\t"
    "v_mfma_f32_16x16x32_bf16 v[152:155], v[216:219], a[16:19], v[152:155]\n\t"
    "ds_read_b64_tr_b16 v[184:185], v241 offset:768\n\t"
    "v_mfma_f32_16x16x32_bf16 v[152:155], v[220:223], a[24:27], v[152:155]\n\t"
    "ds_read_b64_tr_b16 v[186:187], v241 offset:4864\n\t"
    "v_mfma_f32_16x16x32_bf16 v[156:159], v[216:219], a[20:23], v[156:159]\n\t"
    "ds_read_b64_tr_b16 v[188:189], v241 offset:8960\n\t"
    "buffer_load_dwordx4 v251, %[srdW], s45 offen offset:3072 lds\n\t"
    "v_mfma_f32_16x16x32_bf16 v[156:159], v[220:223], a[28:31], v[156:159]\n\t"
    "ds_read_b64_tr_b16 v[190:191], v241 offset:13056\n\t"
    "s_waitcnt vmcnt(18)\n\t"
    "s_waitcnt vmcnt(16)\n\t"
    "s_waitcnt lgkmcnt(8)\n\t"
    "v_mfma_f32_16x16x32_bf16 v[32:35], v[160:163], a[32:35], v[32:35]\n\t"
    "ds_read_b64_tr_b16 v[192:193], v241 offset:1024\n\t"
    "v_mfma_f32_16x16x32_bf16 v[32:35], v[164:167], a[40:43], v[32:35]\n\t"
    "ds_read_b64_tr_b16 v[194:195], v241 offset:5120\n\t"
    "s_add_u32 m0, s43, 36864\n\t"
    "s_mov_b32 s45, 2228224\n\t"
    "v_mfma_f32_16x16x32_bf16 v[36:39], v[160:163], a[36:39], v[36:39]\n\t"
    "ds_read_b64_tr_b16 v[196:197], v241 offset:9216\n\t"
    "buffer_load_dwordx4 %[wsrc0], %[srdW], s45 offen offset:0 lds\n\t"
    "v_mfma_f32_16x16x32_bf16 v[36:39], v[164:167], a[44:47], v[36:39]\n\t"
    "ds_read_b64_tr_b16 v[198:199], v241 offset:13312\n\t"
    "v_mfma_f32_16x16x32_bf16 v[40:43], v[168:171], a[32:35], v[40:43]\n\t"
    "ds_read_b64_tr_b16 v[200:201], v241 offset:1280\n\t"
    "v_mfma_f32_16x16x32_bf16 v[40:43], v[172:175], a[40:43], v[40:43]\n\t"
    "ds_read_b64_tr_b16 v[202:203], v241 offset:5376\n\t"
    "v_mfma_f32_16x16x32_bf16 v[44:47], v[168:171], a[36:39], v[44:47]\n\t"
    "ds_read_b64_tr_b16 v[204:205], v241 offset:9472\n\t"
    "buffer_load_dwordx4 v247, %[srdW], s45 offen offset:1024 lds\n\t"
    "v_mfma_f32_16x16x32_bf16 v[44:47], v[172:175], a[44:47], v[44:47]\n\t"
    "ds_read_b64_tr_b16 v[206:207], v241 offset:13568\n\t"
    "s_waitcnt lgkmcnt(8)\n\t"
    "v_mfma_f32_16x16x32_bf16 v[48:51], v[176:179], a[32:35], v[48:51]\n\t"
    "ds_read_b64_tr_b16 v[208:209], v241 offset:1536\n\t"
    "v_mfma_f32_16x16x32_bf16 v[48:51], v[180:183], a[40:43], v[48:51]\n\t"
    "ds_read_b64_tr_b16 v[210:211], v241 offset:5632\n\t"
    "v_mfma_f32_16x16x32_bf16 v[52:55], v[176:179], a[36:39], v[52:55]\n\t"
    "ds_read_b64_tr_b16 v[212:213], v241 offset:9728\n\t"
    "buffer_load_dwordx4 v250, %[srdW], s45 offen offset:2048 lds\n\t"
    "v_mfma_f32_16x16x32_bf16 v[52:55], v[180:183], a[44:47], v[52:55]\n\t"
    "ds_read_b64_tr_b16 v[214:215], v241 offset:13824\n\t"
    "v_mfma_f32_16x16x32_bf16 v[56:59], v[184:187], a[32:35], v[56:59]\n\t"
    "ds_read_b64_tr_b16 v[216:217], v241 offset:1792\n\t"
    "v_mfma_f32_16x16x32_bf16 v[56:59], v[188:191], a[40:43], v[56:59]\n\t"
    "ds_read_b64_tr_b16 v[218:219], v241 offset:5888\n\t"
    "v_mfma_f32_16x16x32_bf16 v[60:63], v[184:187], a[36:39], v[60:63]\n\t"
    "ds_read_b64_tr_b16 v[220:221], v241 offset:9984\n\t"
    "buffer_load_dwordx4 v251, %[srdW], s45 offen offset:3072 lds\n\t"
    "v_mfma_f32_16x16x32_bf16 v[60:63], v[188:191], a[44:47], v[60:63]\n\t"
    "ds_read_b64_tr_b16 v[222:223], v241 offset:14080\n\t"
    "s_waitcnt lgkmcnt(8)\n\t"
    "v_mfma_f32_16x16x32_bf16 v[64:67], v[192:195], a[32:35], v[64:67]\n\t"
    "ds_read_b64_tr_b16 v[160:161], v241 offset:16384\n\t"
    "v_mfma_f32_16x16x32_bf16 v[64:67], v[196:199], a[40:43], v[64:67]\n\t"
    "ds_read_b64_tr_b16 v[162:163], v241 offset:20480\n\t"
    "s_mov_b32 s46, 8192\n\t"
    "v_mfma_f32_16x16x32_bf16 v[68:71], v[192:195], a[36:39], v[68:71]\n\t"
    "ds_read_b64_tr_b16 v[164:165], v241 offset:24576\n\t"
    "buffer_load_dwordx4 a[64:67], %[hoff], %[srdH], s46 offen offset:0 sc1\n\t"
    "v_mfma_f32_16x16x32_bf16 v[68:71], v[196:199], a[44:47], v[68:71]\n\t"
    "ds_read_b64_tr_b16 v[166:167], v241 offset:28672\n\t"
    "v_mfma_f32_16x16x32_bf16 v[72:75], v[200:203], a[32:35], v[72:75]\n\t"
    "ds_read_b64_tr_b16 v[168:169], v241 offset:16640\n\t"
    "v_mfma_f32_16x16x32_bf16 v[72:75], v[204:207], a[40:43], v[72:75]\n\t"
    "ds_read_b64_tr_b16 v[170:171], v241 offset:20736\n\t"
    "s_mov_b32 s46, 26624\n\t"
    "v_mfma_f32_16x16x32_bf16 v[76:79], v[200:203], a[36:39], v[76:79]\n\t"
    "ds_read_b64_tr_b16 v[172:173], v241 offset:24832\n\t"
    "buffer_load_dwordx4 a[68:71], %[hoff], %[srdH], s46 offen offset:0 sc1\n\t"
    "v_mfma_f32_16x16x32_bf16 v[76:79], v[204:207], a[44:47], v[76:79]\n\t"
    "ds_read_b64_tr_b16 v[174:175], v241 offset:28928\n\t"
    "s_waitcnt lgkmcnt(8)\n\t"
    "v_mfma_f32_16x16x32_bf16 v[80:83], v[208:211], a[32:35], v[80:83]\n\t"
    "ds_read_b64_tr_b16 v[176:177], v241 offset:16896\n\t"
    "v_mfma_f32_16x16x32_bf16 v[80:83], v[212:215], a[40:43], v[80:83]\n\t"
    "ds_read_b64_tr_b16 v[178:179], v241 offset:20992\n\t"
    "s_mov_b32 s46, 8192\n\t"
    "v_mfma_f32_16x16x32_bf16 v[84:87], v[208:211], a[36:39], v[84:87]\n\t"
    "ds_read_b64_tr_b16 v[180:181], v241 offset:25088\n\t"
    "buffer_load_dwordx4 a[72:75], %[hoff], %[srdH], s46 offen offset:1024 sc1\n\t"
    "v_mfma_f32_16x16x32_bf16 v[84:87], v[212:215], a[44:47], v[84:87]\n\t"
    "ds_read_b64_tr_b16 v[182:183], v241 offset:29184\n\t"
    "v_mfma_f32_16x16x32_bf16 v[88:91], v[216:219], a[32:35], v[88:91]\n\t"
    "ds_read_b64_tr_b16 v[184:185], v241 offset:17152\n\t"
    "v_mfma_f32_16x16x32_bf16 v[88:91], v[220:223], a[40:43], v[88:91]\n\t"
    "ds_read_b64_tr_b16 v[186:187], v241 offset:21248\n\t"
    "s_mov_b32 s46, 26624\n\t"
    "v_mfma_f32_16x16x32_bf16 v[92:95], v[216:219], a[36:39], v[92:95]\n\t"
    "ds_read_b64_tr_b16 v[188:189], v241 offset:25344\n\t"
    "buffer_load_dwordx4 a[76:79], %[hoff], %[srdH], s46 offen offset:1024 sc1\n\t"
    "v_mfma_f32_16x16x32_bf16 v[92:95], v[220:223], a[44:47], v[92:95]\n\t"
    "ds_read_b64_tr_b16 v[190:191], v241 offset:29440\n\t"
    "s_waitcnt lgkmcnt(8)\n\t"
    "v_mfma_f32_16x16x32_bf16 v[96:99], v[160:163], a[32:35], v[96:99]\n\t"
    "ds_read_b64_tr_b16 v[192:193], v241 offset:17408\n\t"
    "v_mfma_f32_16x16x32_bf16 v[96:99], v[164:167], a[40:43], v[96:99]\n\t"
    "ds_read_b64_tr_b16 v[194:195], v241 offset:21504\n\t"
    "v_mfma_f32_16x16x32_bf16 v[100:103], v[160:163], a[36:39], v[100:103]\n\t"
    "ds_read_b64_tr_b16 v[196:197], v241 offset:25600\n\t"
    "v_mfma_f32_16x16x32_bf16 v[100:103], v[164:167], a[44:47], v[100:103]\n\t"
    "ds_read_b64_tr_b16 v[198:199], v241 offset:29696\n\t"
    "v_mfma_f32_16x16x32_bf16 v[104:107], v[168:171], a[32:35], v[104:107]\n\t"
    "ds_read_b64_tr_b16 v[200:201], v241 offset:17664\n\t"
    "v_mfma_f32_16x16x32_bf16 v[104:107], v[172:175], a[40:43], v[104:107]\n\t"
    "ds_read_b64_tr_b16 v[202:203], v241 offset:21760\n\t"
    "v_mfma_f32_16x16x32_bf16 v[108:111], v[168:171], a[36:39], v[108:111]\n\t"
    "ds_read_b64_tr_b16 v[204:205], v241 offset:25856\n\t"
    "v_mfma_f32_16x16x32_bf16 v[108:111], v[172:175], a[44:47], v[108:111]\n\t"
    "ds_read_b64_tr_b16 v[206:207], v241 offset:29952\n\t"
    "s_waitcnt lgkmcnt(8)\n\t"
    "v_mfma_f32_16x16x32_bf16 v[112:115], v[176:179], a[32:35], v[112:115]\n\t"
    "ds_read_b64_tr_b16 v[208:209], v241 offset:17920\n\t"
    "v_mfma_f32_16x16x32_bf16 v[112:115], v[180:183], a[40:43], v[112:115]\n\t"
    "ds_read_b64_tr_b16 v[210:211], v241 offset:22016\n\t"
    "v_mfma_f32_16x16x32_bf16 v[116:119], v[176:179], a[36:39], v[116:119]\n\t"
    "ds_read_b64_tr_b16 v[212:213], v241 offset:26112\n\t"
    "v_mfma_f32_16x16x32_bf16 v[116:119], v[180:183], a[44:47], v[116:119]\n\t"
    "ds_read_b64_tr_b16 v[214:215], v241 offset:30208\n\t"
    "v_mfma_f32_16x16x32_bf16 v[120:123], v[184:187], a[32:35], v[120:123]\n\t"
    "ds_read_b64_tr_b16 v[216:217], v241 offset:18176\n\t"
    "v_mfma_f32_16x16x32_bf16 v[120:123], v[188:191], a[40:43], v[120:123]\n\t"
    "ds_read_b64_tr_b16 v[218:219], v241 offset:22272\n\t"
    "v_mfma_f32_16x16x32_bf16 v[124:127], v[184:187], a[36:39], v[124:127]\n\t"
    "ds_read_b64_tr_b16 v[220:221], v241 offset:26368\n\t"
    "v_mfma_f32_16x16x32_bf16 v[124:127], v[188:191], a[44:47], v[124:127]\n\t"
    "ds_read_b64_tr_b16 v[222:223], v241 offset:30464\n\t"
    "s_waitcnt vmcnt(16)\n\t"
    "s_waitcnt lgkmcnt(0)\n\t"
    "s_barrier\n\t"
    "s_waitcnt lgkmcnt(8)\n\t"
    "v_mfma_f32_16x16x32_bf16 v[128:131], v[192:195], a[32:35], v[128:131]\n\t"
    "ds_read_b64_tr_b16 v[160:161], %[vb] offset:0\n\t"
    "v_mfma_f32_16x16x32_bf16 v[128:131], v[196:199], a[40:43], v[128:131]\n\t"
    "ds_read_b64_tr_b16 v[162:163], %[vb] offset:4096\n\t"
    "s_add_u32 m0, s43, 65536\n\t"
    "s_mov_b32 s45, 2621440\n\t"
    "v_mfma_f32_16x16x32_bf16 v[132:135], v[192:195], a[36:39], v[132:135]\n\t"
    "ds_read_b64_tr_b16 v[164:165], %[vb] offset:8192\n\t"
    "buffer_load_dwordx4 %[wsrc0], %[srdW], s45 offen offset:0 lds\n\t"
    "v_mfma_f32_16x16x32_bf16 v[132:135], v[196:199], a[44:47], v[132:135]\n\t"
    "ds_read_b64_tr_b16 v[166:167], %[vb] offset:12288\n\t"
    "v_mfma_f32_16x16x32_bf16 v[136:139], v[200:203], a[32:35], v[136:139]\n\t"
    "ds_read_b64_tr_b16 v[168:169], %[vb] offset:256\n\t"
    "v_mfma_f32_16x16x32_bf16 v[136:139], v[204:207], a[40:43], v[136:139]\n\t"
    "ds_read_b64_tr_b16 v[170:171], %[vb] offset:4352\n\t"
    "v_mfma_f32_16x16x32_bf16 v[140:143], v[200:203], a[36:39], v[140:143]\n\t"
    "ds_read_b64_tr_b16 v[172:173], %[vb] offset:8448\n\t"
    "buffer_load_dwordx4 v247, %[srdW], s45 offen offset:1024 lds\n\t"
    "v_mfma_f32_16x16x32_bf16 v[140:143], v[204:207], a[44:47], v[140:143]\n\t"
    "ds_read_b64_tr_b16 v[174:175], %[vb] offset:12544\n\t"
    "s_waitcnt lgkmcnt(8)\n\t"
    "v_mfma_f32_16x16x32_bf16 v[144:147], v[208:211], a[32:35], v[144:147]\n\t"
    "ds_read_b64_tr_b16 v[176:177], %[vb] offset:512\n\t"
    "v_mfma_f32_16x16x32_bf16 v[144:147], v[212:215], a[40:43], v[144:147]\n\t"
    "ds_read_b64_tr_b16 v[178:179], %[vb] offset:4608\n\t"
    "v_mfma_f32_16x16x32_bf16 v[148:151], v[208:211], a[36:39], v[148:151]\n\t"
    "ds_read_b64_tr_b16 v[180:181], %[vb] offset:8704\n\t"
    "buffer_load_dwordx4 v250, %[srdW], s45 offen offset:2048 lds\n\t"
    "v_mfma_f32_16x16x32_bf16 v[148:151], v[212:215], a[44:47], v[148:151]\n\t"
    "ds_read_b64_tr_b16 v[182:183], %[vb] offset:12800\n\t"
    "v_mfma_f32_16x16x32_bf16 v[152:155], v[216:219], a[32:35], v[152:155]\n\t"
    "ds_read_b64_tr_b16 v[184:185], %[vb] offset:768\n\t"
    "v_mfma_f32_16x16x32_bf16 v[152:155], v[220:223], a[40:43], v[152:155]\n\t"
    "ds_read_b64_tr_b16 v[186:187], %[vb] offset:4864\n\t"
    "v_mfma_f32_16x16x32_bf16 v[156:159], v[216:219], a[36:39], v[156:159]\n\t"
    "ds_read_b64_tr_b16 v[188:189], %[vb] offset:8960\n\t"
    "buffer_load_dwordx4 v251, %[srdW], s45 offen offset:3072 lds\n\t"
    "v_mfma_f32_16x16x32_bf16 v[156:159], v[220:223], a[44:47], v[156:159]\n\t"
    "ds_read_b64_tr_b16 v[190:191], %[vb] offset:13056\n\t"
    "s_waitcnt vmcnt(18)\n\t"
    "s_waitcnt vmcnt(16)\n\t"
    "s_waitcnt lgkmcnt(8)\n\t"
    "v_mfma_f32_16x16x32_bf16 v[32:35], v[160:163], a[48:51], v[32:35]\n\t"
    "ds_read_b64_tr_b16 v[192:193], %[vb] offset:1024\n\t"
    "v_mfma_f32_16x16x32_bf16 v[32:35], v[164:167], a[56:59], v[32:35]\n\t"
    "ds_read_b64_tr_b16 v[194:195], %[vb] offset:5120\n\t"
    "s_add_u32 m0, s43, 69632\n\t"
    "s_mov_b32 s45, 2752512\n\t"
    "v_mfma_f32_16x16x32_bf16 v[36:39], v[160:163], a[52:55], v[36:39]\n\t"
    "ds_read_b64_tr_b16 v[196:197], %[vb] offset:9216\n\t"
    "buffer_load_dwordx4 %[wsrc0], %[srdW], s45 offen offset:0 lds\n\t"
    "v_mfma_f32_16x16x32_bf16 v[36:39], v[164:167], a[60:63], v[36:39]\n\t"
    "ds_read_b64_tr_b16 v[198:199], %[vb] offset:13312\n\t"
    "v_mfma_f32_16x16x32_bf16 v[40:43], v[168:171], a[48:51], v[40:43]\n\t"
    "ds_read_b64_tr_b16 v[200:201], %[vb] offset:1280\n\t"
    "v_mfma_f32_16x16x32_bf16 v[40:43], v[172:175], a[56:59], v[40:43]\n\t"
    "ds_read_b64_tr_b16 v[202:203], %[vb] offset:5376\n\t"
    "v_mfma_f32_16x16x32_bf16 v[44:47], v[168:171], a[52:55], v[44:47]\n\t"
    "ds_read_b64_tr_b16 v[204:205], %[vb] offset:9472\n\t"
    "buffer_load_dwordx4 v247, %[srdW], s45 offen offset:1024 lds\n\t"
    "v_mfma_f32_16x16x32_bf16 v[44:47], v[172:175], a[60:63], v[44:47]\n\t"
    "ds_read_b64_tr_b16 v[206:207], %[vb] offset:13568\n\t"
    "s_waitcnt lgkmcnt(8)\n\t"
    "v_mfma_f32_16x16x32_bf16 v[48:51], v[176:179], a[48:51], v[48:51]\n\t"
    "ds_read_b64_tr_b16 v[208:209], %[vb] offset:1536\n\t"
    "v_mfma_f32_16x16x32_bf16 v[48:51], v[180:183], a[56:59], v[48:51]\n\t"
    "ds_read_b64_tr_b16 v[210:211], %[vb] offset:5632\n\t"
    "v_mfma_f32_16x16x32_bf16 v[52:55], v[176:179], a[52:55], v[52:55]\n\t"
    "ds_read_b64_tr_b16 v[212:213], %[vb] offset:9728\n\t"
    "buffer_load_dwordx4 v250, %[srdW], s45 offen offset:2048 lds\n\t"
    "v_mfma_f32_16x16x32_bf16 v[52:55], v[180:183], a[60:63], v[52:55]\n\t"
    "ds_read_b64_tr_b16 v[214:215], %[vb] offset:13824\n\t"
    "v_mfma_f32_16x16x32_bf16 v[56:59], v[184:187], a[48:51], v[56:59]\n\t"
    "ds_read_b64_tr_b16 v[216:217], %[vb] offset:1792\n\t"
    "v_mfma_f32_16x16x32_bf16 v[56:59], v[188:191], a[56:59], v[56:59]\n\t"
    "ds_read_b64_tr_b16 v[218:219], %[vb] offset:5888\n\t"
    "v_mfma_f32_16x16x32_bf16 v[60:63], v[184:187], a[52:55], v[60:63]\n\t"
    "ds_read_b64_tr_b16 v[220:221], %[vb] offset:9984\n\t"
    "buffer_load_dwordx4 v251, %[srdW], s45 offen offset:3072 lds\n\t"
    "v_mfma_f32_16x16x32_bf16 v[60:63], v[188:191], a[60:63], v[60:63]\n\t"
    "ds_read_b64_tr_b16 v[222:223], %[vb] offset:14080\n\t"
    "s_waitcnt lgkmcnt(8)\n\t"
    "v_mfma_f32_16x16x32_bf16 v[64:67], v[192:195], a[48:51], v[64:67]\n\t"
    "ds_read_b64_tr_b16 v[160:161], %[vb] offset:16384\n\t"
    "v_mfma_f32_16x16x32_bf16 v[64:67], v[196:199], a[56:59], v[64:67]\n\t"
    "ds_read_b64_tr_b16 v[162:163], %[vb] offset:20480\n\t"
    "s_mov_b32 s46, 8192\n\t"
    "v_mfma_f32_16x16x32_bf16 v[68:71], v[192:195], a[52:55], v[68:71]\n\t"
    "ds_read_b64_tr_b16 v[164:165], %[vb] offset:24576\n\t"
    "buffer_load_dwordx4 a[80:83], %[hoff], %[srdH], s46 offen offset:2048 sc1\n\t"
    "v_mfma_f32_16x16x32_bf16 v[68:71], v[196:199], a[60:63], v[68:71]\n\t"
    "ds_read_b64_tr_b16 v[166:167], %[vb] offset:28672\n\t"
    "v_mfma_f32_16x16x32_bf16 v[72:75], v[200:203], a[48:51], v[72:75]\n\t"
    "ds_read_b64_tr_b16 v[168:169], %[vb] offset:16640\n\t"
    "v_mfma_f32_16x16x32_bf16 v[72:75], v[204:207], a[56:59], v[72:75]\n\t"
    "ds_read_b64_tr_b16 v[170:171], %[vb] offset:20736\n\t"
    "s_mov_b32 s46, 26624\n\t"
    "v_mfma_f32_16x16x32_bf16 v[76:79], v[200:203], a[52:55], v[76:79]\n\t"
    "ds_read_b64_tr_b16 v[172:173], %[vb] offset:24832\n\t"
    "buffer_load_dwordx4 a[84:87], %[hoff], %[srdH], s46 offen offset:2048 sc1\n\t"
    "v_mfma_f32_16x16x32_bf16 v[76:79], v[204:207], a[60:63], v[76:79]\n\t"
    "ds_read_b64_tr_b16 v[174:175], %[vb] offset:28928\n\t"
    "s_waitcnt lgkmcnt(8)\n\t"
    "v_mfma_f32_16x16x32_bf16 v[80:83], v[208:211], a[48:51], v[80:83]\n\t"
    "ds_read_b64_tr_b16 v[176:177], %[vb] offset:16896\n\t"
    "v_mfma_f32_16x16x32_bf16 v[80:83], v[212:215], a[56:59], v[80:83]\n\t"
    "ds_read_b64_tr_b16 v[178:179], %[vb] offset:20992\n\t"
    "s_mov_b32 s46, 8192\n\t"
    "v_mfma_f32_16x16x32_bf16 v[84:87], v[208:211], a[52:55], v[84:87]\n\t"
    "ds_read_b64_tr_b16 v[180:181], %[vb] offset:25088\n\t"
    "buffer_load_dwordx4 a[88:91], %[hoff], %[srdH], s46 offen offset:3072 sc1\n\t"
    "v_mfma_f32_16x16x32_bf16 v[84:87], v[212:215], a[60:63], v[84:87]\n\t"
    "ds_read_b64_tr_b16 v[182:183], %[vb] offset:29184\n\t"
    "v_mfma_f32_16x16x32_bf16 v[88:91], v[216:219], a[48:51], v[88:91]\n\t"
    "ds_read_b64_tr_b16 v[184:185], %[vb] offset:17152\n\t"
    "v_mfma_f32_16x16x32_bf16 v[88:91], v[220:223], a[56:59], v[88:91]\n\t"
    "ds_read_b64_tr_b16 v[186:187], %[vb] offset:21248\n\t"
    "s_mov_b32 s46, 26624\n\t"
    "v_mfma_f32_16x16x32_bf16 v[92:95], v[216:219], a[52:55], v[92:95]\n\t"
    "ds_read_b64_tr_b16 v[188:189], %[vb] offset:25344\n\t"
    "buffer_load_dwordx4 a[92:95], %[hoff], %[srdH], s46 offen offset:3072 sc1\n\t"
    "v_mfma_f32_16x16x32_bf16 v[92:95], v[220:223], a[60:63], v[92:95]\n\t"
    "ds_read_b64_tr_b16 v[190:191], %[vb] offset:29440\n\t"
    "s_waitcnt lgkmcnt(8)\n\t"
    "v_mfma_f32_16x16x32_bf16 v[96:99], v[160:163], a[48:51], v[96:99]\n\t"
    "ds_read_b64_tr_b16 v[192:193], %[vb] offset:17408\n\t"
    "v_mfma_f32_16x16x32_bf16 v[96:99], v[164:167], a[56:59], v[96:99]\n\t"
    "ds_read_b64_tr_b16 v[194:195], %[vb] offset:21504\n\t"
    "v_mfma_f32_16x16x32_bf16 v[100:103], v[160:163], a[52:55], v[100:103]\n\t"
    "ds_read_b64_tr_b16 v[196:197], %[vb] offset:25600\n\t"
    "v_mfma_f32_16x16x32_bf16 v[100:103], v[164:167], a[60:63], v[100:103]\n\t"
    "ds_read_b64_tr_b16 v[198:199], %[vb] offset:29696\n\t"
    "v_mfma_f32_16x16x32_bf16 v[104:107], v[168:171], a[48:51], v[104:107]\n\t"
    "ds_read_b64_tr_b16 v[200:201], %[vb] offset:17664\n\t"
    "v_mfma_f32_16x16x32_bf16 v[104:107], v[172:175], a[56:59], v[104:107]\n\t"
    "ds_read_b64_tr_b16 v[202:203], %[vb] offset:21760\n\t"
    "v_mfma_f32_16x16x32_bf16 v[108:111], v[168:171], a[52:55], v[108:111]\n\t"
    "ds_read_b64_tr_b16 v[204:205], %[vb] offset:25856\n\t"
    "v_mfma_f32_16x16x32_bf16 v[108:111], v[172:175], a[60:63], v[108:111]\n\t"
    "ds_read_b64_tr_b16 v[206:207], %[vb] offset:29952\n\t"
    "s_waitcnt lgkmcnt(8)\n\t"
    "v_mfma_f32_16x16x32_bf16 v[112:115], v[176:179], a[48:51], v[112:115]\n\t"
    "ds_read_b64_tr_b16 v[208:209], %[vb] offset:17920\n\t"
    "v_mfma_f32_16x16x32_bf16 v[112:115], v[180:183], a[56:59], v[112:115]\n\t"
    "ds_read_b64_tr_b16 v[210:211], %[vb] offset:22016\n\t"
    "v_mfma_f32_16x16x32_bf16 v[116:119], v[176:179], a[52:55], v[116:119]\n\t"
    "ds_read_b64_tr_b16 v[212:213], %[vb] offset:26112\n\t"
    "v_mfma_f32_16x16x32_bf16 v[116:119], v[180:183], a[60:63], v[116:119]\n\t"
    "ds_read_b64_tr_b16 v[214:215], %[vb] offset:30208\n\t"
    "v_mfma_f32_16x16x32_bf16 v[120:123], v[184:187], a[48:51], v[120:123]\n\t"
    "ds_read_b64_tr_b16 v[216:217], %[vb] offset:18176\n\t"
    "v_mfma_f32_16x16x32_bf16 v[120:123], v[188:191], a[56:59], v[120:123]\n\t"
    "ds_read_b64_tr_b16 v[218:219], %[vb] offset:22272\n\t"
    "v_mfma_f32_16x16x32_bf16 v[124:127], v[184:187], a[52:55], v[124:127]\n\t"
    "ds_read_b64_tr_b16 v[220:221], %[vb] offset:26368\n\t"
    "v_mfma_f32_16x16x32_bf16 v[124:127], v[188:191], a[60:63], v[124:127]\n\t"
    "ds_read_b64_tr_b16 v[222:223], %[vb] offset:30464\n\t"
    "s_waitcnt vmcnt(16)\n\t"
    "s_waitcnt lgkmcnt(0)\n\t"
    "s_barrier\n\t"
    "s_waitcnt lgkmcnt(8)\n\t"
    "v_mfma_f32_16x16x32_bf16 v[128:131], v[192:195], a[48:51], v[128:131]\n\t"
    "ds_read_b64_tr_b16 v[160:161], %[vb] offset:32768\n\t"
    "v_mfma_f32_16x16x32_bf16 v[128:131], v[196:199], a[56:59], v[128:131]\n\t"
    "ds_read_b64_tr_b16 v[162:163], %[vb] offset:36864\n\t"
    "s_add_u32 m0, s43, 0\n\t"
    "s_mov_b32 s45, 3145728\n\t"
    "v_mfma_f32_16x16x32_bf16 v[132:135], v[192:195], a[52:55], v[132:135]\n\t"
    "ds_read_b64_tr_b16 v[164:165], %[vb] offset:40960\n\t"
    "buffer_load_dwordx4 %[wsrc0], %[srdW], s45 offen offset:0 lds\n\t"
    "v_mfma_f32_16x16x32_bf16 v[132:135], v[196:199], a[60:63], v[132:135]\n\t"
    "ds_read_b64_tr_b16 v[166:167], %[vb] offset:45056\n\t"
    "v_mfma_f32_16x16x32_bf16 v[136:139], v[200:203], a[48:51], v[136:139]\n\t"
    "ds_read_b64_tr_b16 v[168:169], %[vb] offset:33024\n\t"
    "v_mfma_f32_16x16x32_bf16 v[136:139], v[204:207], a[56:59], v[136:139]\n\t"
    "ds_read_b64_tr_b16 v[170:171], %[vb] offset:37120\n\t"
    "v_mfma_f32_16x16x32_bf16 v[140:143], v[200:203], a[52:55], v[140:143]\n\t"
    "ds_read_b64_tr_b16 v[172:173], %[vb] offset:41216\n\t"
    "buffer_load_dwordx4 v247, %[srdW], s45 offen offset:1024 lds\n\t"
    "v_mfma_f32_16x16x32_bf16 v[140:143], v[204:207], a[60:63], v[140:143]\n\t"
    "ds_read_b64_tr_b16 v[174:175], %[vb] offset:45312\n\t"
    "s_waitcnt lgkmcnt(8)\n\t"
    "v_mfma_f32_16x16x32_bf16 v[144:147], v[208:211], a[48:51], v[144:147]\n\t"
    "ds_read_b64_tr_b16 v[176:177], %[vb] offset:33280\n\t"
    "v_mfma_f32_16x16x32_bf16 v[144:147], v[212:215], a[56:59], v[144:147]\n\t"
    "ds_read_b64_tr_b16 v[178:179], %[vb] offset:37376\n\t"
    "v_mfma_f32_16x16x32_bf16 v[148:151], v[208:211], a[52:55], v[148:151]\n\t"
    "ds_read_b64_tr_b16 v[180:181], %[vb] offset:41472\n\t"
    "buffer_load_dwordx4 v250, %[srdW], s45 offen offset:2048 lds\n\t"
    "v_mfma_f32_16x16x32_bf16 v[148:151], v[212:215], a[60:63], v[148:151]\n\t"
    "ds_read_b64_tr_b16 v[182:183], %[vb] offset:45568\n\t"
    "v_mfma_f32_16x16x32_bf16 v[152:155], v[216:219], a[48:51], v[152:155]\n\t"
    "ds_read_b64_tr_b16 v[184:185], %[vb] offset:33536\n\t"
    "v_mfma_f32_16x16x32_bf16 v[152:155], v[220:223], a[56:59], v[152:155]\n\t"
    "ds_read_b64_tr_b16 v[186:187], %[vb] offset:37632\n\t"
    "v_mfma_f32_16x16x32_bf16 v[156:159], v[216:219], a[52:55], v[156:159]\n\t"
    "ds_read_b64_tr_b16 v[188:189], %[vb] offset:41728\n\t"
    "buffer_load_dwordx4 v251, %[srdW], s45 offen offset:3072 lds\n\t"
    "v_mfma_f32_16x16x32_bf16 v[156:159], v[220:223], a[60:63], v[156:159]\n\t"
    "ds_read_b64_tr_b16 v[190:191], %[vb] offset:45824\n\t"
    "s_waitcnt vmcnt(18)\n\t"
    "s_waitcnt vmcnt(16)\n\t"
    "s_waitcnt lgkmcnt(8)\n\t"
    "v_mfma_f32_16x16x32_bf16 v[32:35], v[160:163], a[64:67], v[32:35]\n\t"
    "ds_read_b64_tr_b16 v[192:193], %[vb] offset:33792\n\t"
    "v_mfma_f32_16x16x32_bf16 v[32:35], v[164:167], a[72:75], v[32:35]\n\t"
    "ds_read_b64_tr_b16 v[194:195], %[vb] offset:37888\n\t"
    "s_add_u32 m0, s43, 4096\n\t"
    "s_mov_b32 s45, 3276800\n\t"
    "v_mfma_f32_16x16x32_bf16 v[36:39], v[160:163], a[68:71], v[36:39]\n\t"
    "ds_read_b64_tr_b16 v[196:197], %[vb] offset:41984\n\t"
    "buffer_load_dwordx4 %[wsrc0], %[srdW], s45 offen offset:0 lds\n\t"
    "v_mfma_f32_16x16x32_bf16 v[36:39], v[164:167], a[76:79], v[36:39]\n\t"
    "ds_read_b64_tr_b16 v[198:199], %[vb] offset:46080\n\t"
    "v_mfma_f32_16x16x32_bf16 v[40:43], v[168:171], a[64:67], v[40:43]\n\t"
    "ds_read_b64_tr_b16 v[200:201], %[vb] offset:34048\n\t"
    "v_mfma_f32_16x16x32_bf16 v[40:43], v[172:175], a[72:75], v[40:43]\n\t"
    "ds_read_b64_tr_b16 v[202:203], %[vb] offset:38144\n\t"
    "v_mfma_f32_16x16x32_bf16 v[44:47], v[168:171], a[68:71], v[44:47]\n\t"
    "ds_read_b64_tr_b16 v[204:205], %[vb] offset:42240\n\t"
    "buffer_load_dwordx4 v247, %[srdW], s45 offen offset:1024 lds\n\t"
    "v_mfma_f32_16x16x32_bf16 v[44:47], v[172:175], a[76:79], v[44:47]\n\t"
    "ds_read_b64_tr_b16 v[206:207], %[vb] offset:46336\n\t"
    "s_waitcnt lgkmcnt(8)\n\t"
    "v_mfma_f32_16x16x32_bf16 v[48:51], v[176:179], a[64:67], v[48:51]\n\t"
    "ds_read_b64_tr_b16 v[208:209], %[vb] offset:34304\n\t"
    "v_mfma_f32_16x16x32_bf16 v[48:51], v[180:183], a[72:75], v[48:51]\n\t"
    "ds_read_b64_tr_b16 v[210:211], %[vb] offset:38400\n\t"
    "v_mfma_f32_16x16x32_bf16 v[52:55], v[176:179], a[68:71], v[52:55]\n\t"
    "ds_read_b64_tr_b16 v[212:213], %[vb] offset:42496\n\t"
    "buffer_load_dwordx4 v250, %[srdW], s45 offen offset:2048 lds\n\t"
    "v_mfma_f32_16x16x32_bf16 v[52:55], v[180:183], a[76:79], v[52:55]\n\t"
    "ds_read_b64_tr_b16 v[214:215], %[vb] offset:46592\n\t"
    "v_mfma_f32_16x16x32_bf16 v[56:59], v[184:187], a[64:67], v[56:59]\n\t"
    "ds_read_b64_tr_b16 v[216:217], %[vb] offset:34560\n\t"
    "v_mfma_f32_16x16x32_bf16 v[56:59], v[188:191], a[72:75], v[56:59]\n\t"
    "ds_read_b64_tr_b16 v[218:219], %[vb] offset:38656\n\t"
    "v_mfma_f32_16x16x32_bf16 v[60:63], v[184:187], a[68:71], v[60:63]\n\t"
    "ds_read_b64_tr_b16 v[220:221], %[vb] offset:42752\n\t"
    "buffer_load_dwordx4 v251, %[srdW], s45 offen offset:3072 lds\n\t"
    "v_mfma_f32_16x16x32_bf16 v[60:63], v[188:191], a[76:79], v[60:63]\n\t"
    "ds_read_b64_tr_b16 v[222:223], %[vb] offset:46848\n\t"
    "s_waitcnt lgkmcnt(8)\n\t"
    "v_mfma_f32_16x16x32_bf16 v[64:67], v[192:195], a[64:67], v[64:67]\n\t"
    "ds_read_b64_tr_b16 v[160:161], %[vb] offset:49152\n\t"
    "v_mfma_f32_16x16x32_bf16 v[64:67], v[196:199], a[72:75], v[64:67]\n\t"
    "ds_read_b64_tr_b16 v[162:163], %[vb] offset:53248\n\t"
    "s_mov_b32 s46, 12288\n\t"
    "v_mfma_f32_16x16x32_bf16 v[68:71], v[192:195], a[68:71], v[68:71]\n\t"
    "ds_read_b64_tr_b16 v[164:165], %[vb] offset:57344\n\t"
    "buffer_load_dwordx4 a[96:99], %[hoff], %[srdH], s46 offen offset:0 sc1\n\t"
    "v_mfma_f32_16x16x32_bf16 v[68:71], v[196:199], a[76:79], v[68:71]\n\t"
    "ds_read_b64_tr_b16 v[166:167], %[vb] offset:61440\n\t"
    "v_mfma_f32_16x16x32_bf16 v[72:75], v[200:203], a[64:67], v[72:75]\n\t"
    "ds_read_b64_tr_b16 v[168:169], %[vb] offset:49408\n\t"
    "v_mfma_f32_16x16x32_bf16 v[72:75], v[204:207], a[72:75], v[72:75]\n\t"
    "ds_read_b64_tr_b16 v[170:171], %[vb] offset:53504\n\t"
    "s_mov_b32 s46, 30720\n\t"
    "v_mfma_f32_16x16x32_bf16 v[76:79], v[200:203], a[68:71], v[76:79]\n\t"
    "ds_read_b64_tr_b16 v[172:173], %[vb] offset:57600\n\t"
    "buffer_load_dwordx4 a[100:103], %[hoff], %[srdH], s46 offen offset:0 sc1\n\t"
    "v_mfma_f32_16x16x32_bf16 v[76:79], v[204:207], a[76:79], v[76:79]\n\t"
    "ds_read_b64_tr_b16 v[174:175], %[vb] offset:61696\n\t"
    "s_waitcnt lgkmcnt(8)\n\t"
    "v_mfma_f32_16x16x32_bf16 v[80:83], v[208:211], a[64:67], v[80:83]\n\t"
    "ds_read_b64_tr_b16 v[176:177], %[vb] offset:49664\n\t"
    "v_mfma_f32_16x16x32_bf16 v[80:83], v[212:215], a[72:75], v[80:83]\n\t"
    "ds_read_b64_tr_b16 v[178:179], %[vb] offset:53760\n\t"
    "s_mov_b32 s46, 12288\n\t"
    "v_mfma_f32_16x16x32_bf16 v[84:87], v[208:211], a[68:71], v[84:87]\n\t"
    "ds_read_b64_tr_b16 v[180:181], %[vb] offset:57856\n\t"
    "buffer_load_dwordx4 a[104:107], %[hoff], %[srdH], s46 offen offset:1024 sc1\n\t"
    "v_mfma_f32_16x16x32_bf16 v[84:87], v[212:215], a[76:79], v[84:87]\n\t"
    "ds_read_b64_tr_b16 v[182:183], %[vb] offset:61952\n\t"
    "v_mfma_f32_16x16x32_bf16 v[88:91], v[216:219], a[64:67], v[88:91]\n\t"
    "ds_read_b64_tr_b16 v[184:185], %[vb] offset:49920\n\t"
    "v_mfma_f32_16x16x32_bf16 v[88:91], v[220:223], a[72:75], v[88:91]\n\t"
    "ds_read_b64_tr_b16 v[186:187], %[vb] offset:54016\n\t"
    "s_mov_b32 s46, 30720\n\t"
    "v_mfma_f32_16x16x32_bf16 v[92:95], v[216:219], a[68:71], v[92:95]\n\t"
    "ds_read_b64_tr_b16 v[188:189], %[vb] offset:58112\n\t"
    "buffer_load_dwordx4 a[108:111], %[hoff], %[srdH], s46 offen offset:1024 sc1\n\t"
    "v_mfma_f32_16x16x32_bf16 v[92:95], v[220:223], a[76:79], v[92:95]\n\t"
    "ds_read_b64_tr_b16 v[190:191], %[vb] offset:62208\n\t"
    "s_waitcnt lgkmcnt(8)\n\t"
    "v_mfma_f32_16x16x32_bf16 v[96:99], v[160:163], a[64:67], v[96:99]\n\t"
    "ds_read_b64_tr_b16 v[192:193], %[vb] offset:50176\n\t"
    "v_mfma_f32_16x16x32_bf16 v[96:99], v[164:167], a[72:75], v[96:99]\n\t"
    "ds_read_b64_tr_b16 v[194:195], %[vb] offset:54272\n\t"
    "v_mfma_f32_16x16x32_bf16 v[100:103], v[160:163], a[68:71], v[100:103]\n\t"
    "ds_read_b64_tr_b16 v[196:197], %[vb] offset:58368\n\t"
    "v_mfma_f32_16x16x32_bf16 v[100:103], v[164:167], a[76:79], v[100:103]\n\t"
    "ds_read_b64_tr_b16 v[198:199], %[vb] offset:62464\n\t"
    "v_mfma_f32_16x16x32_bf16 v[104:107], v[168:171], a[64:67], v[104:107]\n\t"
    "ds_read_b64_tr_b16 v[200:201], %[vb] offset:50432\n\t"
    "v_mfma_f32_16x16x32_bf16 v[104:107], v[172:175], a[72:75], v[104:107]\n\t"
    "ds_read_b64_tr_b16 v[202:203], %[vb] offset:54528\n\t"
    "v_mfma_f32_16x16x32_bf16 v[108:111], v[168:171], a[68:71], v[108:111]\n\t"
    "ds_read_b64_tr_b16 v[204:205], %[vb] offset:58624\n\t"
    "v_mfma_f32_16x16x32_bf16 v[108:111], v[172:175], a[76:79], v[108:111]\n\t"
    "ds_read_b64_tr_b16 v[206:207], %[vb] offset:62720\n\t"
    "s_waitcnt lgkmcnt(8)\n\t"
    "v_mfma_f32_16x16x32_bf16 v[112:115], v[176:179], a[64:67], v[112:115]\n\t"
    "ds_read_b64_tr_b16 v[208:209], %[vb] offset:50688\n\t"
    "v_mfma_f32_16x16x32_bf16 v[112:115], v[180:183], a[72:75], v[112:115]\n\t"
    "ds_read_b64_tr_b16 v[210:211], %[vb] offset:54784\n\t"
    "v_mfma_f32_16x16x32_bf16 v[116:119], v[176:179], a[68:71], v[116:119]\n\t"
    "ds_read_b64_tr_b16 v[212:213], %[vb] offset:58880\n\t"
    "v_mfma_f32_16x16x32_bf16 v[116:119], v[180:183], a[76:79], v[116:119]\n\t"
    "ds_read_b64_tr_b16 v[214:215], %[vb] offset:62976\n\t"
    "v_mfma_f32_16x16x32_bf16 v[120:123], v[184:187], a[64:67], v[120:123]\n\t"
    "ds_read_b64_tr_b16 v[216:217], %[vb] offset:50944\n\t"
    "v_mfma_f32_16x16x32_bf16 v[120:123], v[188:191], a[72:75], v[120:123]\n\t"
    "ds_read_b64_tr_b16 v[218:219], %[vb] offset:55040\n\t"
    "v_mfma_f32_16x16x32_bf16 v[124:127], v[184:187], a[68:71], v[124:127]\n\t"
    "ds_read_b64_tr_b16 v[220:221], %[vb] offset:59136\n\t"
    "v_mfma_f32_16x16x32_bf16 v[124:127], v[188:191], a[76:79], v[124:127]\n\t"
    "ds_read_b64_tr_b16 v[222:223], %[vb] offset:63232\n\t"
    "s_waitcnt vmcnt(16)\n\t"
    "s_waitcnt lgkmcnt(0)\n\t"
    "s_barrier\n\t"
    "s_waitcnt lgkmcnt(8)\n\t"
    "v_mfma_f32_16x16x32_bf16 v[128:131], v[192:195], a[64:67], v[128:131]\n\t"
    "ds_read_b64_tr_b16 v[160:161], v241 offset:0\n\t"
    "v_mfma_f32_16x16x32_bf16 v[128:131], v[196:199], a[72:75], v[128:131]\n\t"
    "ds_read_b64_tr_b16 v[162:163], v241 offset:4096\n\t"
    "s_add_u32 m0, s43, 32768\n\t"
    "s_mov_b32 s45, 3670016\n\t"
    "v_mfma_f32_16x16x32_bf16 v[132:135], v[192:195], a[68:71], v[132:135]\n\t"
    "ds_read_b64_tr_b16 v[164:165], v241 offset:8192\n\t"
    "buffer_load_dwordx4 %[wsrc0], %[srdW], s45 offen offset:0 lds\n\t"
    "v_mfma_f32_16x16x32_bf16 v[132:135], v[196:199], a[76:79], v[132:135]\n\t"
    "ds_read_b64_tr_b16 v[166:167], v241 offset:12288\n\t"
    "v_mfma_f32_16x16x32_bf16 v[136:139], v[200:203], a[64:67], v[136:139]\n\t"
    "ds_read_b64_tr_b16 v[168:169], v241 offset:256\n\t"
    "v_mfma_f32_16x16x32_bf16 v[136:139], v[204:207], a[72:75], v[136:139]\n\t"
    "ds_read_b64_tr_b16 v[170:171], v241 offset:4352\n\t"
    "v_mfma_f32_16x16x32_bf16 v[140:143], v[200:203], a[68:71], v[140:143]\n\t"
    "ds_read_b64_tr_b16 v[172:173], v241 offset:8448\n\t"
    "buffer_load_dwordx4 v247, %[srdW], s45 offen offset:1024 lds\n\t"
    "v_mfma_f32_16x16x32_bf16 v[140:143], v[204:207], a[76:79], v[140:143]\n\t"
    "ds_read_b64_tr_b16 v[174:175], v241 offset:12544\n\t"
    "s_waitcnt lgkmcnt(8)\n\t"
    "v_mfma_f32_16x16x32_bf16 v[144:147], v[208:211], a[64:67], v[144:147]\n\t"
    "ds_read_b64_tr_b16 v[176:177], v241 offset:512\n\t"
    "v_mfma_f32_16x16x32_bf16 v[144:147], v[212:215], a[72:75], v[144:147]\n\t"
    "ds_read_b64_tr_b16 v[178:179], v241 offset:4608\n\t"
    "v_mfma_f32_16x16x32_bf16 v[148:151], v[208:211], a[68:71], v[148:151]\n\t"
    "ds_read_b64_tr_b16 v[180:181], v241 offset:8704\n\t"
    "buffer_load_dwordx4 v250, %[srdW], s45 offen offset:2048 lds\n\t"
    "v_mfma_f32_16x16x32_bf16 v[148:151], v[212:215], a[76:79], v[148:151]\n\t"
    "ds_read_b64_tr_b16 v[182:183], v241 offset:12800\n\t"
    "v_mfma_f32_16x16x32_bf16 v[152:155], v[216:219], a[64:67], v[152:155]\n\t"
    "ds_read_b64_tr_b16 v[184:185], v241 offset:768\n\t"
    "v_mfma_f32_16x16x32_bf16 v[152:155], v[220:223], a[72:75], v[152:155]\n\t"
    "ds_read_b64_tr_b16 v[186:187], v241 offset:4864\n\t"
    "v_mfma_f32_16x16x32_bf16 v[156:159], v[216:219], a[68:71], v[156:159]\n\t"
    "ds_read_b64_tr_b16 v[188:189], v241 offset:8960\n\t"
    "buffer_load_dwordx4 v251, %[srdW], s45 offen offset:3072 lds\n\t"
    "v_mfma_f32_16x16x32_bf16 v[156:159], v[220:223], a[76:79], v[156:159]\n\t"
    "ds_read_b64_tr_b16 v[190:191], v241 offset:13056\n\t"
    "s_waitcnt vmcnt(18)\n\t"
    "s_waitcnt vmcnt(16)\n\t"
    "s_waitcnt lgkmcnt(8)\n\t"
    "v_mfma_f32_16x16x32_bf16 v[32:35], v[160:163], a[80:83], v[32:35]\n\t"
    "ds_read_b64_tr_b16 v[192:193], v241 offset:1024\n\t"
    "v_mfma_f32_16x16x32_bf16 v[32:35], v[164:167], a[88:91], v[32:35]\n\t"
    "ds_read_b64_tr_b16 v[194:195], v241 offset:5120\n\t"
    "s_add_u32 m0, s43, 36864\n\t"
    "s_mov_b32 s45, 3801088\n\t"
    "v_mfma_f32_16x16x32_bf16 v[36:39], v[160:163], a[84:87], v[36:39]\n\t"
    "ds_read_b64_tr_b16 v[196:197], v241 offset:9216\n\t"
    "buffer_load_dwordx4 %[wsrc0], %[srdW], s45 offen offset:0 lds\n\t"
    "v_mfma_f32_16x16x32_bf16 v[36:39], v[164:167], a[92:95], v[36:39]\n\t"
    "ds_read_b64_tr_b16 v[198:199], v241 offset:13312\n\t"
    "v_mfma_f32_16x16x32_bf16 v[40:43], v[168:171], a[80:83], v[40:43]\n\t"
    "ds_read_b64_tr_b16 v[200:201], v241 offset:1280\n\t"
    "v_mfma_f32_16x16x32_bf16 v[40:43], v[172:175], a[88:91], v[40:43]\n\t"
    "ds_read_b64_tr_b16 v[202:203], v241 offset:5376\n\t"
    "v_mfma_f32_16x16x32_bf16 v[44:47], v[168:171], a[84:87], v[44:47]\n\t"
    "ds_read_b64_tr_b16 v[204:205], v241 offset:9472\n\t"
    "buffer_load_dwordx4 v247, %[srdW], s45 offen offset:1024 lds\n\t"
    "v_mfma_f32_16x16x32_bf16 v[44:47], v[172:175], a[92:95], v[44:47]\n\t"
    "ds_read_b64_tr_b16 v[206:207], v241 offset:13568\n\t"
    "s_waitcnt lgkmcnt(8)\n\t"
    "v_mfma_f32_16x16x32_bf16 v[48:51], v[176:179], a[80:83], v[48:51]\n\t"
    "ds_read_b64_tr_b16 v[208:209], v241 offset:1536\n\t"
    "v_mfma_f32_16x16x32_bf16 v[48:51], v[180:183], a[88:91], v[48:51]\n\t"
    "ds_read_b64_tr_b16 v[210:211], v241 offset:5632\n\t"
    "v_mfma_f32_16x16x32_bf16 v[52:55], v[176:179], a[84:87], v[52:55]\n\t"
    "ds_read_b64_tr_b16 v[212:213], v241 offset:9728\n\t"
    "buffer_load_dwordx4 v250, %[srdW], s45 offen offset:2048 lds\n\t"
    "v_mfma_f32_16x16x32_bf16 v[52:55], v[180:183], a[92:95], v[52:55]\n\t"
    "ds_read_b64_tr_b16 v[214:215], v241 offset:13824\n\t"
    "v_mfma_f32_16x16x32_bf16 v[56:59], v[184:187], a[80:83], v[56:59]\n\t"
    "ds_read_b64_tr_b16 v[216:217], v241 offset:1792\n\t"
    "v_mfma_f32_16x16x32_bf16 v[56:59], v[188:191], a[88:91], v[56:59]\n\t"
    "ds_read_b64_tr_b16 v[218:219], v241 offset:5888\n\t"
    "v_mfma_f32_16x16x32_bf16 v[60:63], v[184:187], a[84:87], v[60:63]\n\t"
    "ds_read_b64_tr_b16 v[220:221], v241 offset:9984\n\t"
    "buffer_load_dwordx4 v251, %[srdW], s45 offen offset:3072 lds\n\t"
    "v_mfma_f32_16x16x32_bf16 v[60:63], v[188:191], a[92:95], v[60:63]\n\t"
    "ds_read_b64_tr_b16 v[222:223], v241 offset:14080\n\t"
    "s_waitcnt lgkmcnt(8)\n\t"
    "v_mfma_f32_16x16x32_bf16 v[64:67], v[192:195], a[80:83], v[64:67]\n\t"
    "ds_read_b64_tr_b16 v[160:161], v241 offset:16384\n\t"
    "v_mfma_f32_16x16x32_bf16 v[64:67], v[196:199], a[88:91], v[64:67]\n\t"
    "ds_read_b64_tr_b16 v[162:163], v241 offset:20480\n\t"
    "s_mov_b32 s46, 12288\n\t"
    "v_mfma_f32_16x16x32_bf16 v[68:71], v[192:195], a[84:87], v[68:71]\n\t"
    "ds_read_b64_tr_b16 v[164:165], v241 offset:24576\n\t"
    "buffer_load_dwordx4 a[112:115], %[hoff], %[srdH], s46 offen offset:2048 sc1\n\t"
    "v_mfma_f32_16x16x32_bf16 v[68:71], v[196:199], a[92:95], v[68:71]\n\t"
    "ds_read_b64_tr_b16 v[166:167], v241 offset:28672\n\t"
    "v_mfma_f32_16x16x32_bf16 v[72:75], v[200:203], a[80:83], v[72:75]\n\t"
    "ds_read_b64_tr_b16 v[168:169], v241 offset:16640\n\t"
    "v_mfma_f32_16x16x32_bf16 v[72:75], v[204:207], a[88:91], v[72:75]\n\t"
    "ds_read_b64_tr_b16 v[170:171], v241 offset:20736\n\t"
    "s_mov_b32 s46, 30720\n\t"
    "v_mfma_f32_16x16x32_bf16 v[76:79], v[200:203], a[84:87], v[76:79]\n\t"
    "ds_read_b64_tr_b16 v[172:173], v241 offset:24832\n\t"
    "buffer_load_dwordx4 a[116:119], %[hoff], %[srdH], s46 offen offset:2048 sc1\n\t"
    "v_mfma_f32_16x16x32_bf16 v[76:79], v[204:207], a[92:95], v[76:79]\n\t"
    "ds_read_b64_tr_b16 v[174:175], v241 offset:28928\n\t"
    "s_waitcnt lgkmcnt(8)\n\t"
    "v_mfma_f32_16x16x32_bf16 v[80:83], v[208:211], a[80:83], v[80:83]\n\t"
    "ds_read_b64_tr_b16 v[176:177], v241 offset:16896\n\t"
    "v_mfma_f32_16x16x32_bf16 v[80:83], v[212:215], a[88:91], v[80:83]\n\t"
    "ds_read_b64_tr_b16 v[178:179], v241 offset:20992\n\t"
    "s_mov_b32 s46, 12288\n\t"
    "v_mfma_f32_16x16x32_bf16 v[84:87], v[208:211], a[84:87], v[84:87]\n\t"
    "ds_read_b64_tr_b16 v[180:181], v241 offset:25088\n\t"
    "buffer_load_dwordx4 a[120:123], %[hoff], %[srdH], s46 offen offset:3072 sc1\n\t"
    "v_mfma_f32_16x16x32_bf16 v[84:87], v[212:215], a[92:95], v[84:87]\n\t"
    "ds_read_b64_tr_b16 v[182:183], v241 offset:29184\n\t"
    "v_mfma_f32_16x16x32_bf16 v[88:91], v[216:219], a[80:83], v[88:91]\n\t"
    "ds_read_b64_tr_b16 v[184:185], v241 offset:17152\n\t"
    "v_mfma_f32_16x16x32_bf16 v[88:91], v[220:223], a[88:91], v[88:91]\n\t"
    "ds_read_b64_tr_b16 v[186:187], v241 offset:21248\n\t"
    "s_mov_b32 s46, 30720\n\t"
    "v_mfma_f32_16x16x32_bf16 v[92:95], v[216:219], a[84:87], v[92:95]\n\t"
    "ds_read_b64_tr_b16 v[188:189], v241 offset:25344\n\t"
    "buffer_load_dwordx4 a[124:127], %[hoff], %[srdH], s46 offen offset:3072 sc1\n\t"
    "v_mfma_f32_16x16x32_bf16 v[92:95], v[220:223], a[92:95], v[92:95]\n\t"
    "ds_read_b64_tr_b16 v[190:191], v241 offset:29440\n\t"
    "s_waitcnt lgkmcnt(8)\n\t"
    "v_mfma_f32_16x16x32_bf16 v[96:99], v[160:163], a[80:83], v[96:99]\n\t"
    "ds_read_b64_tr_b16 v[192:193], v241 offset:17408\n\t"
    "v_mfma_f32_16x16x32_bf16 v[96:99], v[164:167], a[88:91], v[96:99]\n\t"
    "ds_read_b64_tr_b16 v[194:195], v241 offset:21504\n\t"
    "v_mfma_f32_16x16x32_bf16 v[100:103], v[160:163], a[84:87], v[100:103]\n\t"
    "ds_read_b64_tr_b16 v[196:197], v241 offset:25600\n\t"
    "v_mfma_f32_16x16x32_bf16 v[100:103], v[164:167], a[92:95], v[100:103]\n\t"
    "ds_read_b64_tr_b16 v[198:199], v241 offset:29696\n\t"
    "v_mfma_f32_16x16x32_bf16 v[104:107], v[168:171], a[80:83], v[104:107]\n\t"
    "ds_read_b64_tr_b16 v[200:201], v241 offset:17664\n\t"
    "v_mfma_f32_16x16x32_bf16 v[104:107], v[172:175], a[88:91], v[104:107]\n\t"
    "ds_read_b64_tr_b16 v[202:203], v241 offset:21760\n\t"
    "v_mfma_f32_16x16x32_bf16 v[108:111], v[168:171], a[84:87], v[108:111]\n\t"
    "ds_read_b64_tr_b16 v[204:205], v241 offset:25856\n\t"
    "v_mfma_f32_16x16x32_bf16 v[108:111], v[172:175], a[92:95], v[108:111]\n\t"
    "ds_read_b64_tr_b16 v[206:207], v241 offset:29952\n\t"
    "s_waitcnt lgkmcnt(8)\n\t"
    "v_mfma_f32_16x16x32_bf16 v[112:115], v[176:179], a[80:83], v[112:115]\n\t"
    "ds_read_b64_tr_b16 v[208:209], v241 offset:17920\n\t"
    "v_mfma_f32_16x16x32_bf16 v[112:115], v[180:183], a[88:91], v[112:115]\n\t"
    "ds_read_b64_tr_b16 v[210:211], v241 offset:22016\n\t"
    "v_mfma_f32_16x16x32_bf16 v[116:119], v[176:179], a[84:87], v[116:119]\n\t"
    "ds_read_b64_tr_b16 v[212:213], v241 offset:26112\n\t"
    "v_mfma_f32_16x16x32_bf16 v[116:119], v[180:183], a[92:95], v[116:119]\n\t"
    "ds_read_b64_tr_b16 v[214:215], v241 offset:30208\n\t"
    "v_mfma_f32_16x16x32_bf16 v[120:123], v[184:187], a[80:83], v[120:123]\n\t"
    "ds_read_b64_tr_b16 v[216:217], v241 offset:18176\n\t"
    "v_mfma_f32_16x16x32_bf16 v[120:123], v[188:191], a[88:91], v[120:123]\n\t"
    "ds_read_b64_tr_b16 v[218:219], v241 offset:22272\n\t"
    "v_mfma_f32_16x16x32_bf16 v[124:127], v[184:187], a[84:87], v[124:127]\n\t"
    "ds_read_b64_tr_b16 v[220:221], v241 offset:26368\n\t"
    "v_mfma_f32_16x16x32_bf16 v[124:127], v[188:191], a[92:95], v[124:127]\n\t"
    "ds_read_b64_tr_b16 v[222:223], v241 offset:30464\n\t"
    "s_waitcnt vmcnt(16)\n\t"
    "s_waitcnt lgkmcnt(0)\n\t"
    "s_barrier\n\t"
    "s_waitcnt lgkmcnt(8)\n\t"
    "v_mfma_f32_16x16x32_bf16 v[128:131], v[192:195], a[80:83], v[128:131]\n\t"
    "ds_read_b64_tr_b16 v[160:161], %[vb] offset:0\n\t"
    "v_mfma_f32_16x16x32_bf16 v[128:131], v[196:199], a[88:91], v[128:131]\n\t"
    "ds_read_b64_tr_b16 v[162:163], %[vb] offset:4096\n\t"
    "s_add_u32 m0, s43, 65536\n\t"
    "s_mov_b32 s45, 4194304\n\t"
    "v_mfma_f32_16x16x32_bf16 v[132:135], v[192:195], a[84:87], v[132:135]\n\t"
    "ds_read_b64_tr_b16 v[164:165], %[vb] offset:8192\n\t"
    "buffer_load_dwordx4 %[wsrc0], %[srdW], s45 offen offset:0 lds\n\t"
    "v_mfma_f32_16x16x32_bf16 v[132:135], v[196:199], a[92:95], v[132:135]\n\t"
    "ds_read_b64_tr_b16 v[166:167], %[vb] offset:12288\n\t"
    "v_mfma_f32_16x16x32_bf16 v[136:139], v[200:203], a[80:83], v[136:139]\n\t"
    "ds_read_b64_tr_b16 v[168:169], %[vb] offset:256\n\t"
    "v_mfma_f32_16x16x32_bf16 v[136:139], v[204:207], a[88:91], v[136:139]\n\t"
    "ds_read_b64_tr_b16 v[170:171], %[vb] offset:4352\n\t"
    "v_mfma_f32_16x16x32_bf16 v[140:143], v[200:203], a[84:87], v[140:143]\n\t"
    "ds_read_b64_tr_b16 v[172:173], %[vb] offset:8448\n\t"
    "buffer_load_dwordx4 v247, %[srdW], s45 offen offset:1024 lds\n\t"
    "v_mfma_f32_16x16x32_bf16 v[140:143], v[204:207], a[92:95], v[140:143]\n\t"
    "ds_read_b64_tr_b16 v[174:175], %[vb] offset:12544\n\t"
    "s_waitcnt lgkmcnt(8)\n\t"
    "v_mfma_f32_16x16x32_bf16 v[144:147], v[208:211], a[80:83], v[144:147]\n\t"
    "ds_read_b64_tr_b16 v[176:177], %[vb] offset:512\n\t"
    "v_mfma_f32_16x16x32_bf16 v[144:147], v[212:215], a[88:91], v[144:147]\n\t"
    "ds_read_b64_tr_b16 v[178:179], %[vb] offset:4608\n\t"
    "v_mfma_f32_16x16x32_bf16 v[148:151], v[208:211], a[84:87], v[148:151]\n\t"
    "ds_read_b64_tr_b16 v[180:181], %[vb] offset:8704\n\t"
    "buffer_load_dwordx4 v250, %[srdW], s45 offen offset:2048 lds\n\t"
    "v_mfma_f32_16x16x32_bf16 v[148:151], v[212:215], a[92:95], v[148:151]\n\t"
    "ds_read_b64_tr_b16 v[182:183], %[vb] offset:12800\n\t"
    "v_mfma_f32_16x16x32_bf16 v[152:155], v[216:219], a[80:83], v[152:155]\n\t"
    "ds_read_b64_tr_b16 v[184:185], %[vb] offset:768\n\t"
    "v_mfma_f32_16x16x32_bf16 v[152:155], v[220:223], a[88:91], v[152:155]\n\t"
    "ds_read_b64_tr_b16 v[186:187], %[vb] offset:4864\n\t"
    "v_mfma_f32_16x16x32_bf16 v[156:159], v[216:219], a[84:87], v[156:159]\n\t"
    "ds_read_b64_tr_b16 v[188:189], %[vb] offset:8960\n\t"
    "buffer_load_dwordx4 v251, %[srdW], s45 offen offset:3072 lds\n\t"
    "v_mfma_f32_16x16x32_bf16 v[156:159], v[220:223], a[92:95], v[156:159]\n\t"
    "ds_read_b64_tr_b16 v[190:191], %[vb] offset:13056\n\t"
    "s_waitcnt vmcnt(18)\n\t"
    "s_waitcnt vmcnt(16)\n\t"
    "s_waitcnt lgkmcnt(8)\n\t"
    "v_mfma_f32_16x16x32_bf16 v[32:35], v[160:163], a[96:99], v[32:35]\n\t"
    "ds_read_b64_tr_b16 v[192:193], %[vb] offset:1024\n\t"
    "v_mfma_f32_16x16x32_bf16 v[32:35], v[164:167], a[104:107], v[32:35]\n\t"
    "ds_read_b64_tr_b16 v[194:195], %[vb] offset:5120\n\t"
    "s_add_u32 m0, s43, 69632\n\t"
    "s_mov_b32 s45, 4325376\n\t"
    "v_mfma_f32_16x16x32_bf16 v[36:39], v[160:163], a[100:103], v[36:39]\n\t"
    "ds_read_b64_tr_b16 v[196:197], %[vb] offset:9216\n\t"
    "buffer_load_dwordx4 %[wsrc0], %[srdW], s45 offen offset:0 lds\n\t"
    "v_mfma_f32_16x16x32_bf16 v[36:39], v[164:167], a[108:111], v[36:39]\n\t"
    "ds_read_b64_tr_b16 v[198:199], %[vb] offset:13312\n\t"
    "v_mfma_f32_16x16x32_bf16 v[40:43], v[168:171], a[96:99], v[40:43]\n\t"
    "ds_read_b64_tr_b16 v[200:201], %[vb] offset:1280\n\t"
    "v_mfma_f32_16x16x32_bf16 v[40:43], v[172:175], a[104:107], v[40:43]\n\t"
    "ds_read_b64_tr_b16 v[202:203], %[vb] offset:5376\n\t"
    "v_mfma_f32_16x16x32_bf16 v[44:47], v[168:171], a[100:103], v[44:47]\n\t"
    "ds_read_b64_tr_b16 v[204:205], %[vb] offset:9472\n\t"
    "buffer_load_dwordx4 v247, %[srdW], s45 offen offset:1024 lds\n\t"
    "v_mfma_f32_16x16x32_bf16 v[44:47], v[172:175], a[108:111], v[44:47]\n\t"
    "ds_read_b64_tr_b16 v[206:207], %[vb] offset:13568\n\t"
    "s_waitcnt lgkmcnt(8)\n\t"
    "v_mfma_f32_16x16x32_bf16 v[48:51], v[176:179], a[96:99], v[48:51]\n\t"
    "ds_read_b64_tr_b16 v[208:209], %[vb] offset:1536\n\t"
    "v_mfma_f32_16x16x32_bf16 v[48:51], v[180:183], a[104:107], v[48:51]\n\t"
    "ds_read_b64_tr_b16 v[210:211], %[vb] offset:5632\n\t"
    "v_mfma_f32_16x16x32_bf16 v[52:55], v[176:179], a[100:103], v[52:55]\n\t"
    "ds_read_b64_tr_b16 v[212:213], %[vb] offset:9728\n\t"
    "buffer_load_dwordx4 v250, %[srdW], s45 offen offset:2048 lds\n\t"
    "v_mfma_f32_16x16x32_bf16 v[52:55], v[180:183], a[108:111], v[52:55]\n\t"
    "ds_read_b64_tr_b16 v[214:215], %[vb] offset:13824\n\t"
    "v_mfma_f32_16x16x32_bf16 v[56:59], v[184:187], a[96:99], v[56:59]\n\t"
    "ds_read_b64_tr_b16 v[216:217], %[vb] offset:1792\n\t"
    "v_mfma_f32_16x16x32_bf16 v[56:59], v[188:191], a[104:107], v[56:59]\n\t"
    "ds_read_b64_tr_b16 v[218:219], %[vb] offset:5888\n\t"
    "v_mfma_f32_16x16x32_bf16 v[60:63], v[184:187], a[100:103], v[60:63]\n\t"
    "ds_read_b64_tr_b16 v[220:221], %[vb] offset:9984\n\t"
    "buffer_load_dwordx4 v251, %[srdW], s45 offen offset:3072 lds\n\t"
    "v_mfma_f32_16x16x32_bf16 v[60:63], v[188:191], a[108:111], v[60:63]\n\t"
    "ds_read_b64_tr_b16 v[222:223], %[vb] offset:14080\n\t"
    "s_waitcnt lgkmcnt(8)\n\t"
    "v_mfma_f32_16x16x32_bf16 v[64:67], v[192:195], a[96:99], v[64:67]\n\t"
    "ds_read_b64_tr_b16 v[160:161], %[vb] offset:16384\n\t"
    "v_mfma_f32_16x16x32_bf16 v[64:67], v[196:199], a[104:107], v[64:67]\n\t"
    "ds_read_b64_tr_b16 v[162:163], %[vb] offset:20480\n\t"
    "s_mov_b32 s46, 16384\n\t"
    "v_mfma_f32_16x16x32_bf16 v[68:71], v[192:195], a[100:103], v[68:71]\n\t"
    "ds_read_b64_tr_b16 v[164:165], %[vb] offset:24576\n\t"
    "buffer_load_dwordx4 a[128:131], %[hoff], %[srdH], s46 offen offset:0 sc1\n\t"
    "v_mfma_f32_16x16x32_bf16 v[68:71], v[196:199], a[108:111], v[68:71]\n\t"
    "ds_read_b64_tr_b16 v[166:167], %[vb] offset:28672\n\t"
    "v_mfma_f32_16x16x32_bf16 v[72:75], v[200:203], a[96:99], v[72:75]\n\t"
    "ds_read_b64_tr_b16 v[168:169], %[vb] offset:16640\n\t"
    "v_mfma_f32_16x16x32_bf16 v[72:75], v[204:207], a[104:107], v[72:75]\n\t"
    "ds_read_b64_tr_b16 v[170:171], %[vb] offset:20736\n\t"
    "s_mov_b32 s46, 34816\n\t"
    "v_mfma_f32_16x16x32_bf16 v[76:79], v[200:203], a[100:103], v[76:79]\n\t"
    "ds_read_b64_tr_b16 v[172:173], %[vb] offset:24832\n\t"
    "buffer_load_dwordx4 a[132:135], %[hoff], %[srdH], s46 offen offset:0 sc1\n\t"
    "v_mfma_f32_16x16x32_bf16 v[76:79], v[204:207], a[108:111], v[76:79]\n\t"
    "ds_read_b64_tr_b16 v[174:175], %[vb] offset:28928\n\t"
    "s_waitcnt lgkmcnt(8)\n\t"
    "v_mfma_f32_16x16x32_bf16 v[80:83], v[208:211], a[96:99], v[80:83]\n\t"
    "ds_read_b64_tr_b16 v[176:177], %[vb] offset:16896\n\t"
    "v_mfma_f32_16x16x32_bf16 v[80:83], v[212:215], a[104:107], v[80:83]\n\t"
    "ds_read_b64_tr_b16 v[178:179], %[vb] offset:20992\n\t"
    "s_mov_b32 s46, 16384\n\t"
    "v_mfma_f32_16x16x32_bf16 v[84:87], v[208:211], a[100:103], v[84:87]\n\t"
    "ds_read_b64_tr_b16 v[180:181], %[vb] offset:25088\n\t"
    "buffer_load_dwordx4 a[136:139], %[hoff], %[srdH], s46 offen offset:1024 sc1\n\t"
    "v_mfma_f32_16x16x32_bf16 v[84:87], v[212:215], a[108:111], v[84:87]\n\t"
    "ds_read_b64_tr_b16 v[182:183], %[vb] offset:29184\n\t"
    "v_mfma_f32_16x16x32_bf16 v[88:91], v[216:219], a[96:99], v[88:91]\n\t"
    "ds_read_b64_tr_b16 v[184:185], %[vb] offset:17152\n\t"
    "v_mfma_f32_16x16x32_bf16 v[88:91], v[220:223], a[104:107], v[88:91]\n\t"
    "ds_read_b64_tr_b16 v[186:187], %[vb] offset:21248\n\t"
    "s_mov_b32 s46, 34816\n\t"
    "v_mfma_f32_16x16x32_bf16 v[92:95], v[216:219], a[100:103], v[92:95]\n\t"
    "ds_read_b64_tr_b16 v[188:189], %[vb] offset:25344\n\t"
    "buffer_load_dwordx4 a[140:143], %[hoff], %[srdH], s46 offen offset:1024 sc1\n\t"
    "v_mfma_f32_16x16x32_bf16 v[92:95], v[220:223], a[108:111], v[92:95]\n\t"
    "ds_read_b64_tr_b16 v[190:191], %[vb] offset:29440\n\t"
    "s_waitcnt lgkmcnt(8)\n\t"
    "v_mfma_f32_16x16x32_bf16 v[96:99], v[160:163], a[96:99], v[96:99]\n\t"
    "ds_read_b64_tr_b16 v[192:193], %[vb] offset:17408\n\t"
    "v_mfma_f32_16x16x32_bf16 v[96:99], v[164:167], a[104:107], v[96:99]\n\t"
    "ds_read_b64_tr_b16 v[194:195], %[vb] offset:21504\n\t"
    "v_mfma_f32_16x16x32_bf16 v[100:103], v[160:163], a[100:103], v[100:103]\n\t"
    "ds_read_b64_tr_b16 v[196:197], %[vb] offset:25600\n\t"
    "v_mfma_f32_16x16x32_bf16 v[100:103], v[164:167], a[108:111], v[100:103]\n\t"
    "ds_read_b64_tr_b16 v[198:199], %[vb] offset:29696\n\t"
    "v_mfma_f32_16x16x32_bf16 v[104:107], v[168:171], a[96:99], v[104:107]\n\t"
    "ds_read_b64_tr_b16 v[200:201], %[vb] offset:17664\n\t"
    "v_mfma_f32_16x16x32_bf16 v[104:107], v[172:175], a[104:107], v[104:107]\n\t"
    "ds_read_b64_tr_b16 v[202:203], %[vb] offset:21760\n\t"
    "v_mfma_f32_16x16x32_bf16 v[108:111], v[168:171], a[100:103], v[108:111]\n\t"
    "ds_read_b64_tr_b16 v[204:205], %[vb] offset:25856\n\t"
    "v_mfma_f32_16x16x32_bf16 v[108:111], v[172:175], a[108:111], v[108:111]\n\t"
    "ds_read_b64_tr_b16 v[206:207], %[vb] offset:29952\n\t"
    "s_waitcnt lgkmcnt(8)\n\t"
    "v_mfma_f32_16x16x32_bf16 v[112:115], v[176:179], a[96:99], v[112:115]\n\t"
    "ds_read_b64_tr_b16 v[208:209], %[vb] offset:17920\n\t"
    "v_mfma_f32_16x16x32_bf16 v[112:115], v[180:183], a[104:107], v[112:115]\n\t"
    "ds_read_b64_tr_b16 v[210:211], %[vb] offset:22016\n\t"
    "v_mfma_f32_16x16x32_bf16 v[116:119], v[176:179], a[100:103], v[116:119]\n\t"
    "ds_read_b64_tr_b16 v[212:213], %[vb] offset:26112\n\t"
    "v_mfma_f32_16x16x32_bf16 v[116:119], v[180:183], a[108:111], v[116:119]\n\t"
    "ds_read_b64_tr_b16 v[214:215], %[vb] offset:30208\n\t"
    "v_mfma_f32_16x16x32_bf16 v[120:123], v[184:187], a[96:99], v[120:123]\n\t"
    "ds_read_b64_tr_b16 v[216:217], %[vb] offset:18176\n\t"
    "v_mfma_f32_16x16x32_bf16 v[120:123], v[188:191], a[104:107], v[120:123]\n\t"
    "ds_read_b64_tr_b16 v[218:219], %[vb] offset:22272\n\t"
    "v_mfma_f32_16x16x32_bf16 v[124:127], v[184:187], a[100:103], v[124:127]\n\t"
    "ds_read_b64_tr_b16 v[220:221], %[vb] offset:26368\n\t"
    "v_mfma_f32_16x16x32_bf16 v[124:127], v[188:191], a[108:111], v[124:127]\n\t"
    "ds_read_b64_tr_b16 v[222:223], %[vb] offset:30464\n\t"
    "s_waitcnt vmcnt(16)\n\t"
    "s_waitcnt lgkmcnt(0)\n\t"
    "s_barrier\n\t"
    "s_waitcnt lgkmcnt(8)\n\t"
    "v_mfma_f32_16x16x32_bf16 v[128:131], v[192:195], a[96:99], v[128:131]\n\t"
    "ds_read_b64_tr_b16 v[160:161], %[vb] offset:32768\n\t"
    "v_mfma_f32_16x16x32_bf16 v[128:131], v[196:199], a[104:107], v[128:131]\n\t"
    "ds_read_b64_tr_b16 v[162:163], %[vb] offset:36864\n\t"
    "v_mfma_f32_16x16x32_bf16 v[132:135], v[192:195], a[100:103], v[132:135]\n\t"
    "ds_read_b64_tr_b16 v[164:165], %[vb] offset:40960\n\t"
    "v_mfma_f32_16x16x32_bf16 v[132:135], v[196:199], a[108:111], v[132:135]\n\t"
    "ds_read_b64_tr_b16 v[166:167], %[vb] offset:45056\n\t"
    "v_mfma_f32_16x16x32_bf16 v[136:139], v[200:203], a[96:99], v[136:139]\n\t"
    "ds_read_b64_tr_b16 v[168:169], %[vb] offset:33024\n\t"
    "v_mfma_f32_16x16x32_bf16 v[136:139], v[204:207], a[104:107], v[136:139]\n\t"
    "ds_read_b64_tr_b16 v[170:171], %[vb] offset:37120\n\t"
    "v_mfma_f32_16x16x32_bf16 v[140:143], v[200:203], a[100:103], v[140:143]\n\t"
    "ds_read_b64_tr_b16 v[172:173], %[vb] offset:41216\n\t"
    "v_mfma_f32_16x16x32_bf16 v[140:143], v[204:207], a[108:111], v[140:143]\n\t"
    "ds_read_b64_tr_b16 v[174:175], %[vb] offset:45312\n\t"
    "s_waitcnt lgkmcnt(8)\n\t"
    "v_mfma_f32_16x16x32_bf16 v[144:147], v[208:211], a[96:99], v[144:147]\n\t"
    "ds_read_b64_tr_b16 v[176:177], %[vb] offset:33280\n\t"
    "v_mfma_f32_16x16x32_bf16 v[144:147], v[212:215], a[104:107], v[144:147]\n\t"
    "ds_read_b64_tr_b16 v[178:179], %[vb] offset:37376\n\t"
    "v_mfma_f32_16x16x32_bf16 v[148:151], v[208:211], a[100:103], v[148:151]\n\t"
    "ds_read_b64_tr_b16 v[180:181], %[vb] offset:41472\n\t"
    "v_mfma_f32_16x16x32_bf16 v[148:151], v[212:215], a[108:111], v[148:151]\n\t"
    "ds_read_b64_tr_b16 v[182:183], %[vb] offset:45568\n\t"
    "v_mfma_f32_16x16x32_bf16 v[152:155], v[216:219], a[96:99], v[152:155]\n\t"
    "ds_read_b64_tr_b16 v[184:185], %[vb] offset:33536\n\t"
    "v_mfma_f32_16x16x32_bf16 v[152:155], v[220:223], a[104:107], v[152:155]\n\t"
    "ds_read_b64_tr_b16 v[186:187], %[vb] offset:37632\n\t"
    "v_mfma_f32_16x16x32_bf16 v[156:159], v[216:219], a[100:103], v[156:159]\n\t"
    "ds_read_b64_tr_b16 v[188:189], %[vb] offset:41728\n\t"
    "v_mfma_f32_16x16x32_bf16 v[156:159], v[220:223], a[108:111], v[156:159]\n\t"
    "ds_read_b64_tr_b16 v[190:191], %[vb] offset:45824\n\t"
    "s_waitcnt vmcnt(14)\n\t"
    "s_waitcnt vmcnt(12)\n\t"
    "s_waitcnt lgkmcnt(8)\n\t"
    "v_mfma_f32_16x16x32_bf16 v[32:35], v[160:163], a[112:115], v[32:35]\n\t"
    "ds_read_b64_tr_b16 v[192:193], %[vb] offset:33792\n\t"
    "v_mfma_f32_16x16x32_bf16 v[32:35], v[164:167], a[120:123], v[32:35]\n\t"
    "ds_read_b64_tr_b16 v[194:195], %[vb] offset:37888\n\t"
    "v_mfma_f32_16x16x32_bf16 v[36:39], v[160:163], a[116:119], v[36:39]\n\t"
    "ds_read_b64_tr_b16 v[196:197], %[vb] offset:41984\n\t"
    "v_mfma_f32_16x16x32_bf16 v[36:39], v[164:167], a[124:127], v[36:39]\n\t"
    "ds_read_b64_tr_b16 v[198:199], %[vb] offset:46080\n\t"
    "v_mfma_f32_16x16x32_bf16 v[40:43], v[168:171], a[112:115], v[40:43]\n\t"
    "ds_read_b64_tr_b16 v[200:201], %[vb] offset:34048\n\t"
    "v_mfma_f32_16x16x32_bf16 v[40:43], v[172:175], a[120:123], v[40:43]\n\t"
    "ds_read_b64_tr_b16 v[202:203], %[vb] offset:38144\n\t"
    "v_mfma_f32_16x16x32_bf16 v[44:47], v[168:171], a[116:119], v[44:47]\n\t"
    "ds_read_b64_tr_b16 v[204:205], %[vb] offset:42240\n\t"
    "v_mfma_f32_16x16x32_bf16 v[44:47], v[172:175], a[124:127], v[44:47]\n\t"
    "ds_read_b64_tr_b16 v[206:207], %[vb] offset:46336\n\t"
    "s_waitcnt lgkmcnt(8)\n\t"
    "v_mfma_f32_16x16x32_bf16 v[48:51], v[176:179], a[112:115], v[48:51]\n\t"
    "ds_read_b64_tr_b16 v[208:209], %[vb] offset:34304\n\t"
    "v_mfma_f32_16x16x32_bf16 v[48:51], v[180:183], a[120:123], v[48:51]\n\t"
    "ds_read_b64_tr_b16 v[210:211], %[vb] offset:38400\n\t"
    "v_mfma_f32_16x16x32_bf16 v[52:55], v[176:179], a[116:119], v[52:55]\n\t"
    "ds_read_b64_tr_b16 v[212:213], %[vb] offset:42496\n\t"
    "v_mfma_f32_16x16x32_bf16 v[52:55], v[180:183], a[124:127], v[52:55]\n\t"
    "ds_read_b64_tr_b16 v[214:215], %[vb] offset:46592\n\t"
    "v_mfma_f32_16x16x32_bf16 v[56:59], v[184:187], a[112:115], v[56:59]\n\t"
    "ds_read_b64_tr_b16 v[216:217], %[vb] offset:34560\n\t"
    "v_mfma_f32_16x16x32_bf16 v[56:59], v[188:191], a[120:123], v[56:59]\n\t"
    "ds_read_b64_tr_b16 v[218:219], %[vb] offset:38656\n\t"
    "v_mfma_f32_16x16x32_bf16 v[60:63], v[184:187], a[116:119], v[60:63]\n\t"
    "ds_read_b64_tr_b16 v[220:221], %[vb] offset:42752\n\t"
    "v_mfma_f32_16x16x32_bf16 v[60:63], v[188:191], a[124:127], v[60:63]\n\t"
    "ds_read_b64_tr_b16 v[222:223], %[vb] offset:46848\n\t"
    "s_waitcnt lgkmcnt(8)\n\t"
    "v_mfma_f32_16x16x32_bf16 v[64:67], v[192:195], a[112:115], v[64:67]\n\t"
    "ds_read_b64_tr_b16 v[160:161], %[vb] offset:49152\n\t"
    "v_mfma_f32_16x16x32_bf16 v[64:67], v[196:199], a[120:123], v[64:67]\n\t"
    "ds_read_b64_tr_b16 v[162:163], %[vb] offset:53248\n\t"
    "v_mfma_f32_16x16x32_bf16 v[68:71], v[192:195], a[116:119], v[68:71]\n\t"
    "ds_read_b64_tr_b16 v[164:165], %[vb] offset:57344\n\t"
    "v_mfma_f32_16x16x32_bf16 v[68:71], v[196:199], a[124:127], v[68:71]\n\t"
    "ds_read_b64_tr_b16 v[166:167], %[vb] offset:61440\n\t"
    "v_mfma_f32_16x16x32_bf16 v[72:75], v[200:203], a[112:115], v[72:75]\n\t"
    "ds_read_b64_tr_b16 v[168:169], %[vb] offset:49408\n\t"
    "v_mfma_f32_16x16x32_bf16 v[72:75], v[204:207], a[120:123], v[72:75]\n\t"
    "ds_read_b64_tr_b16 v[170:171], %[vb] offset:53504\n\t"
    "v_mfma_f32_16x16x32_bf16 v[76:79], v[200:203], a[116:119], v[76:79]\n\t"
    "ds_read_b64_tr_b16 v[172:173], %[vb] offset:57600\n\t"
    "v_mfma_f32_16x16x32_bf16 v[76:79], v[204:207], a[124:127], v[76:79]\n\t"
    "ds_read_b64_tr_b16 v[174:175], %[vb] offset:61696\n\t"
    "s_waitcnt lgkmcnt(8)\n\t"
    "v_mfma_f32_16x16x32_bf16 v[80:83], v[208:211], a[112:115], v[80:83]\n\t"
    "ds_read_b64_tr_b16 v[176:177], %[vb] offset:49664\n\t"
    "v_mfma_f32_16x16x32_bf16 v[80:83], v[212:215], a[120:123], v[80:83]\n\t"
    "ds_read_b64_tr_b16 v[178:179], %[vb] offset:53760\n\t"
    "v_mfma_f32_16x16x32_bf16 v[84:87], v[208:211], a[116:119], v[84:87]\n\t"
    "ds_read_b64_tr_b16 v[180:181], %[vb] offset:57856\n\t"
    "v_mfma_f32_16x16x32_bf16 v[84:87], v[212:215], a[124:127], v[84:87]\n\t"
    "ds_read_b64_tr_b16 v[182:183], %[vb] offset:61952\n\t"
    "v_mfma_f32_16x16x32_bf16 v[88:91], v[216:219], a[112:115], v[88:91]\n\t"
    "ds_read_b64_tr_b16 v[184:185], %[vb] offset:49920\n\t"
    "v_mfma_f32_16x16x32_bf16 v[88:91], v[220:223], a[120:123], v[88:91]\n\t"
    "ds_read_b64_tr_b16 v[186:187], %[vb] offset:54016\n\t"
    "v_mfma_f32_16x16x32_bf16 v[92:95], v[216:219], a[116:119], v[92:95]\n\t"
    "ds_read_b64_tr_b16 v[188:189], %[vb] offset:58112\n\t"
    "v_mfma_f32_16x16x32_bf16 v[92:95], v[220:223], a[124:127], v[92:95]\n\t"
    "ds_read_b64_tr_b16 v[190:191], %[vb] offset:62208\n\t"
    "s_waitcnt lgkmcnt(8)\n\t"
    "v_mfma_f32_16x16x32_bf16 v[96:99], v[160:163], a[112:115], v[96:99]\n\t"
    "ds_read_b64_tr_b16 v[192:193], %[vb] offset:50176\n\t"
    "v_mfma_f32_16x16x32_bf16 v[96:99], v[164:167], a[120:123], v[96:99]\n\t"
    "ds_read_b64_tr_b16 v[194:195], %[vb] offset:54272\n\t"
    "v_mfma_f32_16x16x32_bf16 v[100:103], v[160:163], a[116:119], v[100:103]\n\t"
    "ds_read_b64_tr_b16 v[196:197], %[vb] offset:58368\n\t"
    "v_mfma_f32_16x16x32_bf16 v[100:103], v[164:167], a[124:127], v[100:103]\n\t"
    "ds_read_b64_tr_b16 v[198:199], %[vb] offset:62464\n\t"
    "v_mfma_f32_16x16x32_bf16 v[104:107], v[168:171], a[112:115], v[104:107]\n\t"
    "ds_read_b64_tr_b16 v[200:201], %[vb] offset:50432\n\t"
    "v_mfma_f32_16x16x32_bf16 v[104:107], v[172:175], a[120:123], v[104:107]\n\t"
    "ds_read_b64_tr_b16 v[202:203], %[vb] offset:54528\n\t"
    "v_mfma_f32_16x16x32_bf16 v[108:111], v[168:171], a[116:119], v[108:111]\n\t"
    "ds_read_b64_tr_b16 v[204:205], %[vb] offset:58624\n\t"
    "v_mfma_f32_16x16x32_bf16 v[108:111], v[172:175], a[124:127], v[108:111]\n\t"
    "ds_read_b64_tr_b16 v[206:207], %[vb] offset:62720\n\t"
    "s_waitcnt lgkmcnt(8)\n\t"
    "v_mfma_f32_16x16x32_bf16 v[112:115], v[176:179], a[112:115], v[112:115]\n\t"
    "ds_read_b64_tr_b16 v[208:209], %[vb] offset:50688\n\t"
    "v_mfma_f32_16x16x32_bf16 v[112:115], v[180:183], a[120:123], v[112:115]\n\t"
    "ds_read_b64_tr_b16 v[210:211], %[vb] offset:54784\n\t"
    "v_mfma_f32_16x16x32_bf16 v[116:119], v[176:179], a[116:119], v[116:119]\n\t"
    "ds_read_b64_tr_b16 v[212:213], %[vb] offset:58880\n\t"
    "v_mfma_f32_16x16x32_bf16 v[116:119], v[180:183], a[124:127], v[116:119]\n\t"
    "ds_read_b64_tr_b16 v[214:215], %[vb] offset:62976\n\t"
    "v_mfma_f32_16x16x32_bf16 v[120:123], v[184:187], a[112:115], v[120:123]\n\t"
    "ds_read_b64_tr_b16 v[216:217], %[vb] offset:50944\n\t"
    "v_mfma_f32_16x16x32_bf16 v[120:123], v[188:191], a[120:123], v[120:123]\n\t"
    "ds_read_b64_tr_b16 v[218:219], %[vb] offset:55040\n\t"
    "v_mfma_f32_16x16x32_bf16 v[124:127], v[184:187], a[116:119], v[124:127]\n\t"
    "ds_read_b64_tr_b16 v[220:221], %[vb] offset:59136\n\t"
    "v_mfma_f32_16x16x32_bf16 v[124:127], v[188:191], a[124:127], v[124:127]\n\t"
    "ds_read_b64_tr_b16 v[222:223], %[vb] offset:63232\n\t"
    "s_waitcnt vmcnt(4)\n\t"
    "s_waitcnt lgkmcnt(0)\n\t"
    "s_barrier\n\t"
    "s_waitcnt lgkmcnt(8)\n\t"
    "v_mfma_f32_16x16x32_bf16 v[128:131], v[192:195], a[112:115], v[128:131]\n\t"
    "ds_read_b64_tr_b16 v[160:161], v241 offset:0\n\t"
    "v_mfma_f32_16x16x32_bf16 v[128:131], v[196:199], a[120:123], v[128:131]\n\t"
    "ds_read_b64_tr_b16 v[162:163], v241 offset:4096\n\t"
    "v_mfma_f32_16x16x32_bf16 v[132:135], v[192:195], a[116:119], v[132:135]\n\t"
    "ds_read_b64_tr_b16 v[164:165], v241 offset:8192\n\t"
    "v_mfma_f32_16x16x32_bf16 v[132:135], v[196:199], a[124:127], v[132:135]\n\t"
    "ds_read_b64_tr_b16 v[166:167], v241 offset:12288\n\t"
    "v_mfma_f32_16x16x32_bf16 v[136:139], v[200:203], a[112:115], v[136:139]\n\t"
    "ds_read_b64_tr_b16 v[168:169], v241 offset:256\n\t"
    "v_mfma_f32_16x16x32_bf16 v[136:139], v[204:207], a[120:123], v[136:139]\n\t"
    "ds_read_b64_tr_b16 v[170:171], v241 offset:4352\n\t"
    "v_mfma_f32_16x16x32_bf16 v[140:143], v[200:203], a[116:119], v[140:143]\n\t"
    "ds_read_b64_tr_b16 v[172:173], v241 offset:8448\n\t"
    "v_mfma_f32_16x16x32_bf16 v[140:143], v[204:207], a[124:127], v[140:143]\n\t"
    "ds_read_b64_tr_b16 v[174:175], v241 offset:12544\n\t"
    "s_waitcnt lgkmcnt(8)\n\t"
    "v_mfma_f32_16x16x32_bf16 v[144:147], v[208:211], a[112:115], v[144:147]\n\t"
    "ds_read_b64_tr_b16 v[176:177], v241 offset:512\n\t"
    "v_mfma_f32_16x16x32_bf16 v[144:147], v[212:215], a[120:123], v[144:147]\n\t"
    "ds_read_b64_tr_b16 v[178:179], v241 offset:4608\n\t"
    "v_mfma_f32_16x16x32_bf16 v[148:151], v[208:211], a[116:119], v[148:151]\n\t"
    "ds_read_b64_tr_b16 v[180:181], v241 offset:8704\n\t"
    "v_mfma_f32_16x16x32_bf16 v[148:151], v[212:215], a[124:127], v[148:151]\n\t"
    "ds_read_b64_tr_b16 v[182:183], v241 offset:12800\n\t"
    "v_mfma_f32_16x16x32_bf16 v[152:155], v[216:219], a[112:115], v[152:155]\n\t"
    "ds_read_b64_tr_b16 v[184:185], v241 offset:768\n\t"
    "v_mfma_f32_16x16x32_bf16 v[152:155], v[220:223], a[120:123], v[152:155]\n\t"
    "ds_read_b64_tr_b16 v[186:187], v241 offset:4864\n\t"
    "v_mfma_f32_16x16x32_bf16 v[156:159], v[216:219], a[116:119], v[156:159]\n\t"
    "ds_read_b64_tr_b16 v[188:189], v241 offset:8960\n\t"
    "v_mfma_f32_16x16x32_bf16 v[156:159], v[220:223], a[124:127], v[156:159]\n\t"
    "ds_read_b64_tr_b16 v[190:191], v241 offset:13056\n\t"
    "s_waitcnt vmcnt(2)\n\t"
    "s_waitcnt vmcnt(0)\n\t"
    "s_waitcnt lgkmcnt(8)\n\t"
    "v_mfma_f32_16x16x32_bf16 v[32:35], v[160:163], a[128:131], v[32:35]\n\t"
    "ds_read_b64_tr_b16 v[192:193], v241 offset:1024\n\t"
    "v_mfma_f32_16x16x32_bf16 v[32:35], v[164:167], a[136:139], v[32:35]\n\t"
    "ds_read_b64_tr_b16 v[194:195], v241 offset:5120\n\t"
    "v_mfma_f32_16x16x32_bf16 v[36:39], v[160:163], a[132:135], v[36:39]\n\t"
    "ds_read_b64_tr_b16 v[196:197], v241 offset:9216\n\t"
    "v_mfma_f32_16x16x32_bf16 v[36:39], v[164:167], a[140:143], v[36:39]\n\t"
    "ds_read_b64_tr_b16 v[198:199], v241 offset:13312\n\t"
    "v_mfma_f32_16x16x32_bf16 v[40:43], v[168:171], a[128:131], v[40:43]\n\t"
    "ds_read_b64_tr_b16 v[200:201], v241 offset:1280\n\t"
    "v_mfma_f32_16x16x32_bf16 v[40:43], v[172:175], a[136:139], v[40:43]\n\t"
    "ds_read_b64_tr_b16 v[202:203], v241 offset:5376\n\t"
    "v_mfma_f32_16x16x32_bf16 v[44:47], v[168:171], a[132:135], v[44:47]\n\t"
    "ds_read_b64_tr_b16 v[204:205], v241 offset:9472\n\t"
    "v_mfma_f32_16x16x32_bf16 v[44:47], v[172:175], a[140:143], v[44:47]\n\t"
    "ds_read_b64_tr_b16 v[206:207], v241 offset:13568\n\t"
    "s_waitcnt lgkmcnt(8)\n\t"
    "v_mfma_f32_16x16x32_bf16 v[48:51], v[176:179], a[128:131], v[48:51]\n\t"
    "ds_read_b64_tr_b16 v[208:209], v241 offset:1536\n\t"
    "v_mfma_f32_16x16x32_bf16 v[48:51], v[180:183], a[136:139], v[48:51]\n\t"
    "ds_read_b64_tr_b16 v[210:211], v241 offset:5632\n\t"
    "v_mfma_f32_16x16x32_bf16 v[52:55], v[176:179], a[132:135], v[52:55]\n\t"
    "ds_read_b64_tr_b16 v[212:213], v241 offset:9728\n\t"
    "v_mfma_f32_16x16x32_bf16 v[52:55], v[180:183], a[140:143], v[52:55]\n\t"
    "ds_read_b64_tr_b16 v[214:215], v241 offset:13824\n\t"
    "v_mfma_f32_16x16x32_bf16 v[56:59], v[184:187], a[128:131], v[56:59]\n\t"
    "ds_read_b64_tr_b16 v[216:217], v241 offset:1792\n\t"
    "v_mfma_f32_16x16x32_bf16 v[56:59], v[188:191], a[136:139], v[56:59]\n\t"
    "ds_read_b64_tr_b16 v[218:219], v241 offset:5888\n\t"
    "v_mfma_f32_16x16x32_bf16 v[60:63], v[184:187], a[132:135], v[60:63]\n\t"
    "ds_read_b64_tr_b16 v[220:221], v241 offset:9984\n\t"
    "v_mfma_f32_16x16x32_bf16 v[60:63], v[188:191], a[140:143], v[60:63]\n\t"
    "ds_read_b64_tr_b16 v[222:223], v241 offset:14080\n\t"
    "s_waitcnt lgkmcnt(8)\n\t"
    "v_mfma_f32_16x16x32_bf16 v[64:67], v[192:195], a[128:131], v[64:67]\n\t"
    "ds_read_b64_tr_b16 v[160:161], v241 offset:16384\n\t"
    "v_mfma_f32_16x16x32_bf16 v[64:67], v[196:199], a[136:139], v[64:67]\n\t"
    "ds_read_b64_tr_b16 v[162:163], v241 offset:20480\n\t"
    "v_mfma_f32_16x16x32_bf16 v[68:71], v[192:195], a[132:135], v[68:71]\n\t"
    "ds_read_b64_tr_b16 v[164:165], v241 offset:24576\n\t"
    "v_mfma_f32_16x16x32_bf16 v[68:71], v[196:199], a[140:143], v[68:71]\n\t"
    "ds_read_b64_tr_b16 v[166:167], v241 offset:28672\n\t"
    "v_mfma_f32_16x16x32_bf16 v[72:75], v[200:203], a[128:131], v[72:75]\n\t"
    "ds_read_b64_tr_b16 v[168:169], v241 offset:16640\n\t"
    "v_mfma_f32_16x16x32_bf16 v[72:75], v[204:207], a[136:139], v[72:75]\n\t"
    "ds_read_b64_tr_b16 v[170:171], v241 offset:20736\n\t"
    "v_mfma_f32_16x16x32_bf16 v[76:79], v[200:203], a[132:135], v[76:79]\n\t"
    "ds_read_b64_tr_b16 v[172:173], v241 offset:24832\n\t"
    "v_mfma_f32_16x16x32_bf16 v[76:79], v[204:207], a[140:143], v[76:79]\n\t"
    "ds_read_b64_tr_b16 v[174:175], v241 offset:28928\n\t"
    "s_waitcnt lgkmcnt(8)\n\t"
    "v_mfma_f32_16x16x32_bf16 v[80:83], v[208:211], a[128:131], v[80:83]\n\t"
    "ds_read_b64_tr_b16 v[176:177], v241 offset:16896\n\t"
    "v_mfma_f32_16x16x32_bf16 v[80:83], v[212:215], a[136:139], v[80:83]\n\t"
    "ds_read_b64_tr_b16 v[178:179], v241 offset:20992\n\t"
    "v_mfma_f32_16x16x32_bf16 v[84:87], v[208:211], a[132:135], v[84:87]\n\t"
    "ds_read_b64_tr_b16 v[180:181], v241 offset:25088\n\t"
    "v_mfma_f32_16x16x32_bf16 v[84:87], v[212:215], a[140:143], v[84:87]\n\t"
    "ds_read_b64_tr_b16 v[182:183], v241 offset:29184\n\t"
    "v_mfma_f32_16x16x32_bf16 v[88:91], v[216:219], a[128:131], v[88:91]\n\t"
    "ds_read_b64_tr_b16 v[184:185], v241 offset:17152\n\t"
    "v_mfma_f32_16x16x32_bf16 v[88:91], v[220:223], a[136:139], v[88:91]\n\t"
    "ds_read_b64_tr_b16 v[186:187], v241 offset:21248\n\t"
    "v_mfma_f32_16x16x32_bf16 v[92:95], v[216:219], a[132:135], v[92:95]\n\t"
    "ds_read_b64_tr_b16 v[188:189], v241 offset:25344\n\t"
    "v_mfma_f32_16x16x32_bf16 v[92:95], v[220:223], a[140:143], v[92:95]\n\t"
    "ds_read_b64_tr_b16 v[190:191], v241 offset:29440\n\t"
    "s_waitcnt lgkmcnt(8)\n\t"
    "v_mfma_f32_16x16x32_bf16 v[96:99], v[160:163], a[128:131], v[96:99]\n\t"
    "ds_read_b64_tr_b16 v[192:193], v241 offset:17408\n\t"
    "v_mfma_f32_16x16x32_bf16 v[96:99], v[164:167], a[136:139], v[96:99]\n\t"
    "ds_read_b64_tr_b16 v[194:195], v241 offset:21504\n\t"
    "v_mfma_f32_16x16x32_bf16 v[100:103], v[160:163], a[132:135], v[100:103]\n\t"
    "ds_read_b64_tr_b16 v[196:197], v241 offset:25600\n\t"
    "v_mfma_f32_16x16x32_bf16 v[100:103], v[164:167], a[140:143], v[100:103]\n\t"
    "ds_read_b64_tr_b16 v[198:199], v241 offset:29696\n\t"
    "v_mfma_f32_16x16x32_bf16 v[104:107], v[168:171], a[128:131], v[104:107]\n\t"
    "ds_read_b64_tr_b16 v[200:201], v241 offset:17664\n\t"
    "v_mfma_f32_16x16x32_bf16 v[104:107], v[172:175], a[136:139], v[104:107]\n\t"
    "ds_read_b64_tr_b16 v[202:203], v241 offset:21760\n\t"
    "v_mfma_f32_16x16x32_bf16 v[108:111], v[168:171], a[132:135], v[108:111]\n\t"
    "ds_read_b64_tr_b16 v[204:205], v241 offset:25856\n\t"
    "v_mfma_f32_16x16x32_bf16 v[108:111], v[172:175], a[140:143], v[108:111]\n\t"
    "ds_read_b64_tr_b16 v[206:207], v241 offset:29952\n\t"
    "s_waitcnt lgkmcnt(8)\n\t"
    "v_mfma_f32_16x16x32_bf16 v[112:115], v[176:179], a[128:131], v[112:115]\n\t"
    "ds_read_b64_tr_b16 v[208:209], v241 offset:17920\n\t"
    "v_mfma_f32_16x16x32_bf16 v[112:115], v[180:183], a[136:139], v[112:115]\n\t"
    "ds_read_b64_tr_b16 v[210:211], v241 offset:22016\n\t"
    "v_mfma_f32_16x16x32_bf16 v[116:119], v[176:179], a[132:135], v[116:119]\n\t"
    "ds_read_b64_tr_b16 v[212:213], v241 offset:26112\n\t"
    "v_mfma_f32_16x16x32_bf16 v[116:119], v[180:183], a[140:143], v[116:119]\n\t"
    "ds_read_b64_tr_b16 v[214:215], v241 offset:30208\n\t"
    "v_mfma_f32_16x16x32_bf16 v[120:123], v[184:187], a[128:131], v[120:123]\n\t"
    "ds_read_b64_tr_b16 v[216:217], v241 offset:18176\n\t"
    "v_mfma_f32_16x16x32_bf16 v[120:123], v[188:191], a[136:139], v[120:123]\n\t"
    "ds_read_b64_tr_b16 v[218:219], v241 offset:22272\n\t"
    "v_mfma_f32_16x16x32_bf16 v[124:127], v[184:187], a[132:135], v[124:127]\n\t"
    "ds_read_b64_tr_b16 v[220:221], v241 offset:26368\n\t"
    "v_mfma_f32_16x16x32_bf16 v[124:127], v[188:191], a[140:143], v[124:127]\n\t"
    "ds_read_b64_tr_b16 v[222:223], v241 offset:30464\n\t"
    "s_waitcnt lgkmcnt(8)\n\t"
    "v_mfma_f32_16x16x32_bf16 v[128:131], v[192:195], a[128:131], v[128:131]\n\t"
    "v_mfma_f32_16x16x32_bf16 v[128:131], v[196:199], a[136:139], v[128:131]\n\t"
    "v_mfma_f32_16x16x32_bf16 v[132:135], v[192:195], a[132:135], v[132:135]\n\t"
    "v_mfma_f32_16x16x32_bf16 v[132:135], v[196:199], a[140:143], v[132:135]\n\t"
    "v_mfma_f32_16x16x32_bf16 v[136:139], v[200:203], a[128:131], v[136:139]\n\t"
    "v_mfma_f32_16x16x32_bf16 v[136:139], v[204:207], a[136:139], v[136:139]\n\t"
    "v_mfma_f32_16x16x32_bf16 v[140:143], v[200:203], a[132:135], v[140:143]\n\t"
    "v_mfma_f32_16x16x32_bf16 v[140:143], v[204:207], a[140:143], v[140:143]\n\t"
    "s_waitcnt lgkmcnt(0)\n\t"
    "v_mfma_f32_16x16x32_bf16 v[144:147], v[208:211], a[128:131], v[144:147]\n\t"
    "v_mfma_f32_16x16x32_bf16 v[144:147], v[212:215], a[136:139], v[144:147]\n\t"
    "v_mfma_f32_16x16x32_bf16 v[148:151], v[208:211], a[132:135], v[148:151]\n\t"
    "v_mfma_f32_16x16x32_bf16 v[148:151], v[212:215], a[140:143], v[148:151]\n\t"
    "v_mfma_f32_16x16x32_bf16 v[152:155], v[216:219], a[128:131], v[152:155]\n\t"
    "v_mfma_f32_16x16x32_bf16 v[152:155], v[220:223], a[136:139], v[152:155]\n\t"
    "v_mfma_f32_16x16x32_bf16 v[156:159], v[216:219], a[132:135], v[156:159]\n\t"
    "v_mfma_f32_16x16x32_bf16 v[156:159], v[220:223], a[140:143], v[156:159]\n\t"
    "s_nop 7\n\t"
    "v_mov_b32 v208, 0\n\t"
    "v_mov_b32 v209, 0\n\t"
    "v_mov_b32 v210, 0\n\t"
    "v_mov_b32 v211, 0\n\t"
    "v_mov_b32 v212, 0\n\t"
    "v_mov_b32 v213, 0\n\t"
    "v_mov_b32 v214, 0\n\t"
    "v_mov_b32 v215, 0\n\t"
    "v_mov_b32 v216, 0\n\t"
    "v_mov_b32 v217, 0\n\t"
    "v_mov_b32 v218, 0\n\t"
    "v_mov_b32 v219, 0\n\t"
    "v_mov_b32 v220, 0\n\t"
    "v_mov_b32 v221, 0\n\t"
    "v_mov_b32 v222, 0\n\t"
    "v_mov_b32 v223, 0\n\t"
    "v_cvt_pk_bf16_f32 v160, v32, v33\n\t"
    "v_cvt_pk_bf16_f32 v161, v34, v35\n\t"
    "v_mfma_f32_16x16x32_bf16 a[0:3], v[252:255], v[252:255], 0\n\t"
    "v_cvt_pk_bf16_f32 v162, v40, v41\n\t"
    "v_cvt_pk_bf16_f32 v163, v42, v43\n\t"
    "v_cvt_pk_bf16_f32 v164, v48, v49\n\t"
    "v_cvt_pk_bf16_f32 v165, v50, v51\n\t"
    "v_mfma_f32_16x16x32_bf16 a[4:7], v[252:255], v[252:255], 0\n\t"
    "v_cvt_pk_bf16_f32 v166, v56, v57\n\t"
    "v_cvt_pk_bf16_f32 v167, v58, v59\n\t"
    "v_cvt_pk_bf16_f32 v168, v64, v65\n\t"
    "v_cvt_pk_bf16_f32 v169, v66, v67\n\t"
    "v_mfma_f32_16x16x32_bf16 a[8:11], v[252:255], v[252:255], 0\n\t"
    "v_cvt_pk_bf16_f32 v170, v72, v73\n\t"
    "v_cvt_pk_bf16_f32 v171, v74, v75\n\t"
    "v_cvt_pk_bf16_f32 v172, v80, v81\n\t"
    "v_cvt_pk_bf16_f32 v173, v82, v83\n\t"
    "v_mfma_f32_16x16x32_bf16 a[12:15], v[252:255], v[252:255], 0\n\t"
    "v_cvt_pk_bf16_f32 v174, v88, v89\n\t"
    "v_cvt_pk_bf16_f32 v175, v90, v91\n\t"
    "v_mfma_f32_16x16x32_bf16 a[16:19], v[252:255], v[252:255], 0\n\t"
    "v_permlane32_swap_b32 v160, v162\n\t"
    "v_permlane32_swap_b32 v161, v163\n\t"
    "v_permlane32_swap_b32 v164, v166\n\t"
    "v_permlane32_swap_b32 v165, v167\n\t"
    "v_permlane32_swap_b32 v168, v170\n\t"
    "v_permlane32_swap_b32 v169, v171\n\t"
    "v_permlane32_swap_b32 v172, v174\n\t"
    "v_permlane32_swap_b32 v173, v175\n\t"
    "v_mfma_f32_16x16x32_bf16 a[20:23], v[252:255], v[252:255], 0\n\t"
    "v_permlane16_swap_b32 v160, v162\n\t"
    "v_permlane16_swap_b32 v161, v163\n\t"
    "v_permlane16_swap_b32 v164, v166\n\t"
    "v_permlane16_swap_b32 v165, v167\n\t"
    "v_permlane16_swap_b32 v168, v170\n\t"
    "v_permlane16_swap_b32 v169, v171\n\t"
    "v_permlane16_swap_b32 v172, v174\n\t"
    "v_permlane16_swap_b32 v173, v175\n\t"
    "v_mfma_f32_16x16x32_bf16 a[24:27], v[252:255], v[252:255], 0\n\t"
    "v_accvgpr_write_b32 a128, v160\n\t"
    "v_accvgpr_write_b32 a129, v161\n\t"
    "v_accvgpr_write_b32 a130, v162\n\t"
    "v_accvgpr_write_b32 a131, v163\n\t"
    "v_accvgpr_write_b32 a132, v164\n\t"
    "v_accvgpr_write_b32 a133, v165\n\t"
    "v_accvgpr_write_b32 a134, v166\n\t"
    "v_accvgpr_write_b32 a135, v167\n\t"
    "v_mfma_f32_16x16x32_bf16 a[28:31], v[252:255], v[252:255], 0\n\t"
    "v_accvgpr_write_b32 a136, v168\n\t"
    "v_accvgpr_write_b32 a137, v169\n\t"
    "v_accvgpr_write_b32 a138, v170\n\t"
    "v_accvgpr_write_b32 a139, v171\n\t"
    "v_accvgpr_write_b32 a140, v172\n\t"
    "v_accvgpr_write_b32 a141, v173\n\t"
    "v_accvgpr_write_b32 a142, v174\n\t"
    "v_accvgpr_write_b32 a143, v175\n\t"
    "v_cvt_pk_bf16_f32 v160, v36, v37\n\t"
    "v_cvt_pk_bf16_f32 v161, v38, v39\n\t"
    "v_mfma_f32_16x16x32_bf16 a[32:35], v[252:255], v[252:255], 0\n\t"
    "v_cvt_pk_bf16_f32 v162, v44, v45\n\t"
    "v_cvt_pk_bf16_f32 v163, v46, v47\n\t"
    "v_cvt_pk_bf16_f32 v164, v52, v53\n\t"
    "v_cvt_pk_bf16_f32 v165, v54, v55\n\t"
    "v_mfma_f32_16x16x32_bf16 a[36:39], v[252:255], v[252:255], 0\n\t"
    "v_cvt_pk_bf16_f32 v166, v60, v61\n\t"
    "v_cvt_pk_bf16_f32 v167, v62, v63\n\t"
    "v_cvt_pk_bf16_f32 v168, v68, v69\n\t"
    "v_cvt_pk_bf16_f32 v169, v70, v71\n\t"
    "v_mfma_f32_16x16x32_bf16 a[40:43], v[252:255], v[252:255], 0\n\t"
    "v_cvt_pk_bf16_f32 v170, v76, v77\n\t"
    "v_cvt_pk_bf16_f32 v171, v78, v79\n\t"
    "v_cvt_pk_bf16_f32 v172, v84, v85\n\t"
    "v_cvt_pk_bf16_f32 v173, v86, v87\n\t"
    "v_mfma_f32_16x16x32_bf16 a[44:47], v[252:255], v[252:255], 0\n\t"
    "v_cvt_pk_bf16_f32 v174, v92, v93\n\t"
    "v_cvt_pk_bf16_f32 v175, v94, v95\n\t"
    "v_mfma_f32_16x16x32_bf16 a[48:51], v[252:255], v[252:255], 0\n\t"
    "v_permlane32_swap_b32 v160, v162\n\t"
    "v_permlane32_swap_b32 v161, v163\n\t"
    "v_permlane32_swap_b32 v164, v166\n\t"
    "v_permlane32_swap_b32 v165, v167\n\t"
    "v_permlane32_swap_b32 v168, v170\n\t"
    "v_permlane32_swap_b32 v169, v171\n\t"
    "v_permlane32_swap_b32 v172, v174\n\t"
    "v_permlane32_swap_b32 v173, v175\n\t"
    "v_mfma_f32_16x16x32_bf16 a[52:55], v[252:255], v[252:255], 0\n\t"
    "v_permlane16_swap_b32 v160, v162\n\t"
    "v_permlane16_swap_b32 v161, v163\n\t"
    "v_permlane16_swap_b32 v164, v166\n\t"
    "v_permlane16_swap_b32 v165, v167\n\t"
    "v_permlane16_swap_b32 v168, v170\n\t"
    "v_permlane16_swap_b32 v169, v171\n\t"
    "v_permlane16_swap_b32 v172, v174\n\t"
    "v_permlane16_swap_b32 v173, v175\n\t"
    "v_mfma_f32_16x16x32_bf16 a[56:59], v[252:255], v[252:255], 0\n\t"
    "v_accvgpr_write_b32 a144, v160\n\t"
    "v_accvgpr_write_b32 a145, v161\n\t"
    "v_accvgpr_write_b32 a146, v162\n\t"
    "v_accvgpr_write_b32 a147, v163\n\t"
    "v_accvgpr_write_b32 a148, v164\n\t"
    "v_accvgpr_write_b32 a149, v165\n\t"
    "v_accvgpr_write_b32 a150, v166\n\t"
    "v_accvgpr_write_b32 a151, v167\n\t"
    "v_mfma_f32_16x16x32_bf16 a[60:63], v[252:255], v[252:255], 0\n\t"
    "v_accvgpr_write_b32 a152, v168\n\t"
    "v_accvgpr_write_b32 a153, v169\n\t"
    "v_accvgpr_write_b32 a154, v170\n\t"
    "v_accvgpr_write_b32 a155, v171\n\t"
    "v_accvgpr_write_b32 a156, v172\n\t"
    "v_accvgpr_write_b32 a157, v173\n\t"
    "v_accvgpr_write_b32 a158, v174\n\t"
    "v_accvgpr_write_b32 a159, v175\n\t"
    "v_cvt_pk_bf16_f32 v96, 0, v96\n\t"
    "v_cvt_pk_bf16_f32 v97, 0, v97\n\t"
    "v_cvt_pk_bf16_f32 v98, 0, v98\n\t"
    "v_cvt_pk_bf16_f32 v99, 0, v99\n\t"
    "v_cvt_pk_bf16_f32 v104, 0, v104\n\t"
    "v_cvt_pk_bf16_f32 v105, 0, v105\n\t"
    "v_cvt_pk_bf16_f32 v106, 0, v106\n\t"
    "v_cvt_pk_bf16_f32 v107, 0, v107\n\t"
    "v_cvt_pk_bf16_f32 v112, 0, v112\n\t"
    "v_cvt_pk_bf16_f32 v113, 0, v113\n\t"
    "v_cvt_pk_bf16_f32 v114, 0, v114\n\t"
    "v_cvt_pk_bf16_f32 v115, 0, v115\n\t"
    "v_cvt_pk_bf16_f32 v120, 0, v120\n\t"
    "v_cvt_pk_bf16_f32 v121, 0, v121\n\t"
    "v_cvt_pk_bf16_f32 v122, 0, v122\n\t"
    "v_cvt_pk_bf16_f32 v123, 0, v123\n\t"
    "v_cvt_pk_bf16_f32 v128, 0, v128\n\t"
    "v_cvt_pk_bf16_f32 v129, 0, v129\n\t"
    "v_cvt_pk_bf16_f32 v130, 0, v130\n\t"
    "v_cvt_pk_bf16_f32 v131, 0, v131\n\t"
    "v_cvt_pk_bf16_f32 v136, 0, v136\n\t"
    "v_cvt_pk_bf16_f32 v137, 0, v137\n\t"
    "v_cvt_pk_bf16_f32 v138, 0, v138\n\t"
    "v_cvt_pk_bf16_f32 v139, 0, v139\n\t"
    "v_cvt_pk_bf16_f32 v144, 0, v144\n\t"
    "v_cvt_pk_bf16_f32 v145, 0, v145\n\t"
    "v_cvt_pk_bf16_f32 v146, 0, v146\n\t"
    "v_cvt_pk_bf16_f32 v147, 0, v147\n\t"
    "v_cvt_pk_bf16_f32 v152, 0, v152\n\t"
    "v_cvt_pk_bf16_f32 v153, 0, v153\n\t"
    "v_cvt_pk_bf16_f32 v154, 0, v154\n\t"
    "v_cvt_pk_bf16_f32 v155, 0, v155\n\t"
    "v_mul_f32 v96, s60, v96\n\t"
    "v_mul_f32 v97, s60, v97\n\t"
    "v_mul_f32 v98, s60, v98\n\t"
    "v_mul_f32 v99, s60, v99\n\t"
    "v_mul_f32 v104, s60, v104\n\t"
    "v_mul_f32 v105, s60, v105\n\t"
    "v_mul_f32 v106, s60, v106\n\t"
    "v_mul_f32 v107, s60, v107\n\t"
    "v_mul_f32 v112, s60, v112\n\t"
    "v_mul_f32 v113, s60, v113\n\t"
    "v_mul_f32 v114, s60, v114\n\t"
    "v_mul_f32 v115, s60, v115\n\t"
    "v_mul_f32 v120, s60, v120\n\t"
    "v_mul_f32 v121, s60, v121\n\t"
    "v_mul_f32 v122, s60, v122\n\t"
    "v_mul_f32 v123, s60, v123\n\t"
    "v_mul_f32 v128, s60, v128\n\t"
    "v_mul_f32 v129, s60, v129\n\t"
    "v_mul_f32 v130, s60, v130\n\t"
    "v_mul_f32 v131, s60, v131\n\t"
    "v_mul_f32 v136, s60, v136\n\t"
    "v_mul_f32 v137, s60, v137\n\t"
    "v_mul_f32 v138, s60, v138\n\t"
    "v_mul_f32 v139, s60, v139\n\t"
    "v_mul_f32 v144, s60, v144\n\t"
    "v_mul_f32 v145, s60, v145\n\t"
    "v_mul_f32 v146, s60, v146\n\t"
    "v_mul_f32 v147, s60, v147\n\t"
    "v_mul_f32 v152, s60, v152\n\t"
    "v_mul_f32 v153, s60, v153\n\t"
    "v_mul_f32 v154, s60, v154\n\t"
    "v_mul_f32 v155, s60, v155\n\t"
    "v_cvt_pk_bf16_f32 v160, v96, v97\n\t"
    "v_cvt_pk_bf16_f32 v161, v98, v99\n\t"
    "v_mfma_f32_16x16x32_bf16 a[64:67], v[252:255], v[252:255], 0\n\t"
    "v_cvt_pk_bf16_f32 v162, v104, v105\n\t"
    "v_cvt_pk_bf16_f32 v163, v106, v107\n\t"
    "v_cvt_pk_bf16_f32 v164, v112, v113\n\t"
    "v_cvt_pk_bf16_f32 v165, v114, v115\n\t"
    "v_mfma_f32_16x16x32_bf16 a[68:71], v[252:255], v[252:255], 0\n\t"
    "v_cvt_pk_bf16_f32 v166, v120, v121\n\t"
    "v_cvt_pk_bf16_f32 v167, v122, v123\n\t"
    "v_cvt_pk_bf16_f32 v168, v128, v129\n\t"
    "v_cvt_pk_bf16_f32 v169, v130, v131\n\t"
    "v_mfma_f32_16x16x32_bf16 a[72:75], v[252:255], v[252:255], 0\n\t"
    "v_cvt_pk_bf16_f32 v170, v136, v137\n\t"
    "v_cvt_pk_bf16_f32 v171, v138, v139\n\t"
    "v_cvt_pk_bf16_f32 v172, v144, v145\n\t"
    "v_cvt_pk_bf16_f32 v173, v146, v147\n\t"
    "v_mfma_f32_16x16x32_bf16 a[76:79], v[252:255], v[252:255], 0\n\t"
    "v_cvt_pk_bf16_f32 v174, v152, v153\n\t"
    "v_cvt_pk_bf16_f32 v175, v154, v155\n\t"
    "v_mfma_f32_16x16x32_bf16 a[80:83], v[252:255], v[252:255], 0\n\t"
    "v_permlane32_swap_b32 v160, v162\n\t"
    "v_permlane32_swap_b32 v161, v163\n\t"
    "v_permlane32_swap_b32 v164, v166\n\t"
    "v_permlane32_swap_b32 v165, v167\n\t"
    "v_permlane32_swap_b32 v168, v170\n\t"
    "v_permlane32_swap_b32 v169, v171\n\t"
    "v_permlane32_swap_b32 v172, v174\n\t"
    "v_permlane32_swap_b32 v173, v175\n\t"
    "v_mfma_f32_16x16x32_bf16 a[84:87], v[252:255], v[252:255], 0\n\t"
    "v_permlane16_swap_b32 v160, v162\n\t"
    "v_permlane16_swap_b32 v161, v163\n\t"
    "v_permlane16_swap_b32 v164, v166\n\t"
    "v_permlane16_swap_b32 v165, v167\n\t"
    "v_permlane16_swap_b32 v168, v170\n\t"
    "v_permlane16_swap_b32 v169, v171\n\t"
    "v_permlane16_swap_b32 v172, v174\n\t"
    "v_permlane16_swap_b32 v173, v175\n\t"
    "v_mfma_f32_16x16x32_bf16 a[88:91], v[252:255], v[252:255], 0\n\t"
    "v_accvgpr_write_b32 a160, v160\n\t"
    "v_accvgpr_write_b32 a161, v161\n\t"
    "v_accvgpr_write_b32 a162, v162\n\t"
    "v_accvgpr_write_b32 a163, v163\n\t"
    "v_accvgpr_write_b32 a164, v164\n\t"
    "v_accvgpr_write_b32 a165, v165\n\t"
    "v_accvgpr_write_b32 a166, v166\n\t"
    "v_accvgpr_write_b32 a167, v167\n\t"
    "v_mfma_f32_16x16x32_bf16 a[92:95], v[252:255], v[252:255], 0\n\t"
    "v_accvgpr_write_b32 a168, v168\n\t"
    "v_accvgpr_write_b32 a169, v169\n\t"
    "v_accvgpr_write_b32 a170, v170\n\t"
    "v_accvgpr_write_b32 a171, v171\n\t"
    "v_accvgpr_write_b32 a172, v172\n\t"
    "v_accvgpr_write_b32 a173, v173\n\t"
    "v_accvgpr_write_b32 a174, v174\n\t"
    "v_accvgpr_write_b32 a175, v175\n\t"
    "v_cvt_pk_bf16_f32 v100, 0, v100\n\t"
    "v_cvt_pk_bf16_f32 v101, 0, v101\n\t"
    "v_cvt_pk_bf16_f32 v102, 0, v102\n\t"
    "v_cvt_pk_bf16_f32 v103, 0, v103\n\t"
    "v_cvt_pk_bf16_f32 v108, 0, v108\n\t"
    "v_cvt_pk_bf16_f32 v109, 0, v109\n\t"
    "v_cvt_pk_bf16_f32 v110, 0, v110\n\t"
    "v_cvt_pk_bf16_f32 v111, 0, v111\n\t"
    "v_cvt_pk_bf16_f32 v116, 0, v116\n\t"
    "v_cvt_pk_bf16_f32 v117, 0, v117\n\t"
    "v_cvt_pk_bf16_f32 v118, 0, v118\n\t"
    "v_cvt_pk_bf16_f32 v119, 0, v119\n\t"
    "v_cvt_pk_bf16_f32 v124, 0, v124\n\t"
    "v_cvt_pk_bf16_f32 v125, 0, v125\n\t"
    "v_cvt_pk_bf16_f32 v126, 0, v126\n\t"
    "v_cvt_pk_bf16_f32 v127, 0, v127\n\t"
    "v_cvt_pk_bf16_f32 v132, 0, v132\n\t"
    "v_cvt_pk_bf16_f32 v133, 0, v133\n\t"
    "v_cvt_pk_bf16_f32 v134, 0, v134\n\t"
    "v_cvt_pk_bf16_f32 v135, 0, v135\n\t"
    "v_cvt_pk_bf16_f32 v140, 0, v140\n\t"
    "v_cvt_pk_bf16_f32 v141, 0, v141\n\t"
    "v_cvt_pk_bf16_f32 v142, 0, v142\n\t"
    "v_cvt_pk_bf16_f32 v143, 0, v143\n\t"
    "v_cvt_pk_bf16_f32 v148, 0, v148\n\t"
    "v_cvt_pk_bf16_f32 v149, 0, v149\n\t"
    "v_cvt_pk_bf16_f32 v150, 0, v150\n\t"
    "v_cvt_pk_bf16_f32 v151, 0, v151\n\t"
    "v_cvt_pk_bf16_f32 v156, 0, v156\n\t"
    "v_cvt_pk_bf16_f32 v157, 0, v157\n\t"
    "v_cvt_pk_bf16_f32 v158, 0, v158\n\t"
    "v_cvt_pk_bf16_f32 v159, 0, v159\n\t"
    "v_mul_f32 v100, s60, v100\n\t"
    "v_mul_f32 v101, s60, v101\n\t"
    "v_mul_f32 v102, s60, v102\n\t"
    "v_mul_f32 v103, s60, v103\n\t"
    "v_mul_f32 v108, s60, v108\n\t"
    "v_mul_f32 v109, s60, v109\n\t"
    "v_mul_f32 v110, s60, v110\n\t"
    "v_mul_f32 v111, s60, v111\n\t"
    "v_mul_f32 v116, s60, v116\n\t"
    "v_mul_f32 v117, s60, v117\n\t"
    "v_mul_f32 v118, s60, v118\n\t"
    "v_mul_f32 v119, s60, v119\n\t"
    "v_mul_f32 v124, s60, v124\n\t"
    "v_mul_f32 v125, s60, v125\n\t"
    "v_mul_f32 v126, s60, v126\n\t"
    "v_mul_f32 v127, s60, v127\n\t"
    "v_mul_f32 v132, s60, v132\n\t"
    "v_mul_f32 v133, s60, v133\n\t"
    "v_mul_f32 v134, s60, v134\n\t"
    "v_mul_f32 v135, s60, v135\n\t"
    "v_mul_f32 v140, s60, v140\n\t"
    "v_mul_f32 v141, s60, v141\n\t"
    "v_mul_f32 v142, s60, v142\n\t"
    "v_mul_f32 v143, s60, v143\n\t"
    "v_mul_f32 v148, s60, v148\n\t"
    "v_mul_f32 v149, s60, v149\n\t"
    "v_mul_f32 v150, s60, v150\n\t"
    "v_mul_f32 v151, s60, v151\n\t"
    "v_mul_f32 v156, s60, v156\n\t"
    "v_mul_f32 v157, s60, v157\n\t"
    "v_mul_f32 v158, s60, v158\n\t"
    "v_mul_f32 v159, s60, v159\n\t"
    "v_cvt_pk_bf16_f32 v160, v100, v101\n\t"
    "v_cvt_pk_bf16_f32 v161, v102, v103\n\t"
    "v_mfma_f32_16x16x32_bf16 a[96:99], v[252:255], v[252:255], 0\n\t"
    "v_cvt_pk_bf16_f32 v162, v108, v109\n\t"
    "v_cvt_pk_bf16_f32 v163, v110, v111\n\t"
    "v_cvt_pk_bf16_f32 v164, v116, v117\n\t"
    "v_cvt_pk_bf16_f32 v165, v118, v119\n\t"
    "v_mfma_f32_16x16x32_bf16 a[100:103], v[252:255], v[252:255], 0\n\t"
    "v_cvt_pk_bf16_f32 v166, v124, v125\n\t"
    "v_cvt_pk_bf16_f32 v167, v126, v127\n\t"
    "v_cvt_pk_bf16_f32 v168, v132, v133\n\t"
    "v_cvt_pk_bf16_f32 v169, v134, v135\n\t"
    "v_mfma_f32_16x16x32_bf16 a[104:107], v[252:255], v[252:255], 0\n\t"
    "v_cvt_pk_bf16_f32 v170, v140, v141\n\t"
    "v_cvt_pk_bf16_f32 v171, v142, v143\n\t"
    "v_cvt_pk_bf16_f32 v172, v148, v149\n\t"
    "v_cvt_pk_bf16_f32 v173, v150, v151\n\t"
    "v_mfma_f32_16x16x32_bf16 a[108:111], v[252:255], v[252:255], 0\n\t"
    "v_cvt_pk_bf16_f32 v174, v156, v157\n\t"
    "v_cvt_pk_bf16_f32 v175, v158, v159\n\t"
    "v_mfma_f32_16x16x32_bf16 a[112:115], v[252:255], v[252:255], 0\n\t"
    "v_permlane32_swap_b32 v160, v162\n\t"
    "v_permlane32_swap_b32 v161, v163\n\t"
    "v_permlane32_swap_b32 v164, v166\n\t"
    "v_permlane32_swap_b32 v165, v167\n\t"
    "v_permlane32_swap_b32 v168, v170\n\t"
    "v_permlane32_swap_b32 v169, v171\n\t"
    "v_permlane32_swap_b32 v172, v174\n\t"
    "v_permlane32_swap_b32 v173, v175\n\t"
    "v_mfma_f32_16x16x32_bf16 a[116:119], v[252:255], v[252:255], 0\n\t"
    "v_permlane16_swap_b32 v160, v162\n\t"
    "v_permlane16_swap_b32 v161, v163\n\t"
    "v_permlane16_swap_b32 v164, v166\n\t"
    "v_permlane16_swap_b32 v165, v167\n\t"
    "v_permlane16_swap_b32 v168, v170\n\t"
    "v_permlane16_swap_b32 v169, v171\n\t"
    "v_permlane16_swap_b32 v172, v174\n\t"
    "v_permlane16_swap_b32 v173, v175\n\t"
    "v_mfma_f32_16x16x32_bf16 a[120:123], v[252:255], v[252:255], 0\n\t"
    "v_accvgpr_write_b32 a176, v160\n\t"
    "v_accvgpr_write_b32 a177, v161\n\t"
    "v_accvgpr_write_b32 a178, v162\n\t"
    "v_accvgpr_write_b32 a179, v163\n\t"
    "v_accvgpr_write_b32 a180, v164\n\t"
    "v_accvgpr_write_b32 a181, v165\n\t"
    "v_accvgpr_write_b32 a182, v166\n\t"
    "v_accvgpr_write_b32 a183, v167\n\t"
    "v_mfma_f32_16x16x32_bf16 a[124:127], v[252:255], v[252:255], 0\n\t"
    "v_accvgpr_write_b32 a184, v168\n\t"
    "v_accvgpr_write_b32 a185, v169\n\t"
    "v_accvgpr_write_b32 a186, v170\n\t"
    "v_accvgpr_write_b32 a187, v171\n\t"
    "v_accvgpr_write_b32 a188, v172\n\t"
    "v_accvgpr_write_b32 a189, v173\n\t"
    "v_accvgpr_write_b32 a190, v174\n\t"
    "v_accvgpr_write_b32 a191, v175\n\t"
    "s_waitcnt vmcnt(0)\n\t"
    "s_barrier\n\t"
    "ds_read_b128 a[192:195], v228 offset:0\n\t"
    "ds_read_b128 a[196:199], v229 offset:0\n\t"
    "ds_read_b128 a[200:203], v230 offset:0\n\t"
    "ds_read_b128 a[204:207], v231 offset:0\n\t"
    "ds_read_b128 a[208:211], v228 offset:4096\n\t"
    "ds_read_b128 a[212:215], v229 offset:4096\n\t"
    "ds_read_b128 a[216:219], v230 offset:4096\n\t"
    "ds_read_b128 a[220:223], v231 offset:4096\n\t"
    "s_mov_b32 s40, 65536\n\t"
    "s_mov_b32 s41, 32768\n\t"
    "s_xor_b32 s42, s42, 0x8000\n\t"
    "s_waitcnt lgkmcnt(0)\n\t"
    "v_mfma_f32_16x16x32_bf16 v[32:35], a[192:195], a[128:131], 0\n\t"
    "v_mfma_f32_16x16x32_bf16 v[32:35], a[196:199], a[132:135], v[32:35]\n\t"
    "v_mfma_f32_16x16x32_bf16 v[32:35], a[200:203], a[136:139], v[32:35]\n\t"
    "v_mfma_f32_16x16x32_bf16 v[32:35], a[204:207], a[140:143], v[32:35]\n\t"
    "v_mfma_f32_16x16x32_bf16 v[36:39], a[192:195], a[144:147], 0\n\t"
    "v_mfma_f32_16x16x32_bf16 v[36:39], a[196:199], a[148:151], v[36:39]\n\t"
    "v_mfma_f32_16x16x32_bf16 v[36:39], a[200:203], a[152:155], v[36:39]\n\t"
    "v_mfma_f32_16x16x32_bf16 v[36:39], a[204:207], a[156:159], v[36:39]\n\t"
    "v_mfma_f32_16x16x32_bf16 v[40:43], a[192:195], a[160:163], 0\n\t"
    "v_mfma_f32_16x16x32_bf16 v[40:43], a[196:199], a[164:167], v[40:43]\n\t"
    "v_mfma_f32_16x16x32_bf16 v[40:43], a[200:203], a[168:171], v[40:43]\n\t"
    "v_mfma_f32_16x16x32_bf16 v[40:43], a[204:207], a[172:175], v[40:43]\n\t"
    "v_mfma_f32_16x16x32_bf16 v[44:47], a[192:195], a[176:179], 0\n\t"
    "v_mfma_f32_16x16x32_bf16 v[44:47], a[196:199], a[180:183], v[44:47]\n\t"
    "v_mfma_f32_16x16x32_bf16 v[44:47], a[200:203], a[184:187], v[44:47]\n\t"
    "v_mfma_f32_16x16x32_bf16 v[44:47], a[204:207], a[188:191], v[44:47]\n\t"
    "v_mfma_f32_16x16x32_bf16 v[48:51], a[208:211], a[128:131], 0\n\t"
    "v_mfma_f32_16x16x32_bf16 v[48:51], a[212:215], a[132:135], v[48:51]\n\t"
    "v_mfma_f32_16x16x32_bf16 v[48:51], a[216:219], a[136:139], v[48:51]\n\t"
    "v_mfma_f32_16x16x32_bf16 v[48:51], a[220:223], a[140:143], v[48:51]\n\t"
    "v_mfma_f32_16x16x32_bf16 v[52:55], a[208:211], a[144:147], 0\n\t"
    "v_mfma_f32_16x16x32_bf16 v[52:55], a[212:215], a[148:151], v[52:55]\n\t"
    "v_mfma_f32_16x16x32_bf16 v[52:55], a[216:219], a[152:155], v[52:55]\n\t"
    "v_mfma_f32_16x16x32_bf16 v[52:55], a[220:223], a[156:159], v[52:55]\n\t"
    "v_mfma_f32_16x16x32_bf16 v[56:59], a[208:211], a[160:163], 0\n\t"
    "v_mfma_f32_16x16x32_bf16 v[56:59], a[212:215], a[164:167], v[56:59]\n\t"
    "v_mfma_f32_16x16x32_bf16 v[56:59], a[216:219], a[168:171], v[56:59]\n\t"
    "v_mfma_f32_16x16x32_bf16 v[56:59], a[220:223], a[172:175], v[56:59]\n\t"
    "v_mfma_f32_16x16x32_bf16 v[60:63], a[208:211], a[176:179], 0\n\t"
    "v_mfma_f32_16x16x32_bf16 v[60:63], a[212:215], a[180:183], v[60:63]\n\t"
    "v_mfma_f32_16x16x32_bf16 v[60:63], a[216:219], a[184:187], v[60:63]\n\t"
    "v_mfma_f32_16x16x32_bf16 v[60:63], a[220:223], a[188:191], v[60:63]\n\t"
    "ds_read_b128 a[192:195], v228 offset:8192\n\t"
    "ds_read_b128 a[196:199], v229 offset:8192\n\t"
    "ds_read_b128 a[200:203], v230 offset:8192\n\t"
    "ds_read_b128 a[204:207], v231 offset:8192\n\t"
    "s_nop 7\n\t"
    "v_mov_b32 v144, v52\n\t"
    "v_mov_b32 v145, v53\n\t"
    "v_mov_b32 v146, v54\n\t"
    "v_mov_b32 v147, v55\n\t"
    "v_cvt_pk_bf16_f32 v32, 0, v32\n\t"
    "v_cvt_pk_bf16_f32 v33, 0, v33\n\t"
    "v_cvt_pk_bf16_f32 v34, 0, v34\n\t"
    "v_cvt_pk_bf16_f32 v35, 0, v35\n\t"
    "v_cvt_pk_bf16_f32 v36, 0, v36\n\t"
    "v_cvt_pk_bf16_f32 v37, 0, v37\n\t"
    "v_cvt_pk_bf16_f32 v38, 0, v38\n\t"
    "v_cvt_pk_bf16_f32 v39, 0, v39\n\t"
    "v_mul_f32 v32, s56, v32\n\t"
    "v_mul_f32 v33, s56, v33\n\t"
    "v_mul_f32 v34, s56, v34\n\t"
    "v_mul_f32 v35, s56, v35\n\t"
    "v_mul_f32 v36, s56, v36\n\t"
    "v_mul_f32 v37, s56, v37\n\t"
    "v_mul_f32 v38, s56, v38\n\t"
    "v_mul_f32 v39, s56, v39\n\t"
    "v_cvt_pk_bf16_f32 v32, 0, v32\n\t"
    "v_cvt_pk_bf16_f32 v33, 0, v33\n\t"
    "v_cvt_pk_bf16_f32 v34, 0, v34\n\t"
    "v_cvt_pk_bf16_f32 v35, 0, v35\n\t"
    "v_cvt_pk_bf16_f32 v36, 0, v36\n\t"
    "v_cvt_pk_bf16_f32 v37, 0, v37\n\t"
    "v_cvt_pk_bf16_f32 v38, 0, v38\n\t"
    "v_cvt_pk_bf16_f32 v39, 0, v39\n\t"
    "v_cvt_pk_bf16_f32 v48, 0, v48\n\t"
    "v_cvt_pk_bf16_f32 v49, 0, v49\n\t"
    "v_cvt_pk_bf16_f32 v50, 0, v50\n\t"
    "v_cvt_pk_bf16_f32 v51, 0, v51\n\t"
    "v_cvt_pk_bf16_f32 v52, 0, v52\n\t"
    "v_cvt_pk_bf16_f32 v53, 0, v53\n\t"
    "v_cvt_pk_bf16_f32 v54, 0, v54\n\t"
    "v_cvt_pk_bf16_f32 v55, 0, v55\n\t"
    "v_mul_f32 v48, s56, v48\n\t"
    "v_mul_f32 v49, s56, v49\n\t"
    "v_mul_f32 v50, s56, v50\n\t"
    "v_mul_f32 v51, s56, v51\n\t"
    "v_mul_f32 v52, s56, v52\n\t"
    "v_mul_f32 v53, s56, v53\n\t"
    "v_mul_f32 v54, s56, v54\n\t"
    "v_mul_f32 v55, s56, v55\n\t"
    "v_cvt_pk_bf16_f32 v48, 0, v48\n\t"
    "v_cvt_pk_bf16_f32 v49, 0, v49\n\t"
    "v_cvt_pk_bf16_f32 v50, 0, v50\n\t"
    "v_cvt_pk_bf16_f32 v51, 0, v51\n\t"
    "v_cvt_pk_bf16_f32 v52, 0, v52\n\t"
    "v_cvt_pk_bf16_f32 v53, 0, v53\n\t"
    "v_cvt_pk_bf16_f32 v54, 0, v54\n\t"
    "v_cvt_pk_bf16_f32 v55, 0, v55\n\t"
    "v_max3_f32 v128, v32, v33, v34\n\t"
    "v_max3_f32 v128, v128, v35, v48\n\t"
    "v_max3_f32 v128, v128, v49, v50\n\t"
    "v_max_f32 v128, v128, v51\n\t"
    "v_mov_b32 v129, v128\n\t"
    "v_max3_f32 v130, v36, v37, v38\n\t"
    "v_max3_f32 v130, v130, v39, v52\n\t"
    "v_max3_f32 v130, v130, v53, v54\n\t"
    "v_max_f32 v130, v130, v55\n\t"
    "v_mov_b32 v131, v130\n\t"
    "v_max3_f32 v132, v40, v41, v42\n\t"
    "v_max3_f32 v132, v132, v43, v56\n\t"
    "v_max3_f32 v132, v132, v57, v58\n\t"
    "v_max_f32 v132, v132, v59\n\t"
    "v_mov_b32 v133, v132\n\t"
    "v_max3_f32 v134, v44, v45, v46\n\t"
    "v_max3_f32 v134, v134, v47, v60\n\t"
    "v_max3_f32 v134, v134, v61, v62\n\t"
    "v_max_f32 v134, v134, v63\n\t"
    "v_mov_b32 v135, v134\n\t"
    "s_nop 1\n\t"
    "v_permlane32_swap_b32 v128, v129\n\t"
    "v_permlane32_swap_b32 v130, v131\n\t"
    "v_permlane32_swap_b32 v132, v133\n\t"
    "v_permlane32_swap_b32 v134, v135\n\t"
    "s_nop 1\n\t"
    "v_max_f32 v128, v128, v129\n\t"
    "v_mov_b32 v129, v128\n\t"
    "v_max_f32 v130, v130, v131\n\t"
    "v_mov_b32 v131, v130\n\t"
    "v_max_f32 v132, v132, v133\n\t"
    "v_mov_b32 v133, v132\n\t"
    "v_max_f32 v134, v134, v135\n\t"
    "v_mov_b32 v135, v134\n\t"
    "s_nop 1\n\t"
    "v_permlane16_swap_b32 v128, v129\n\t"
    "v_permlane16_swap_b32 v130, v131\n\t"
    "v_permlane16_swap_b32 v132, v133\n\t"
    "v_permlane16_swap_b32 v134, v135\n\t"
    "s_nop 1\n\t"
    "v_max_f32 v128, v128, v129\n\t"
    "v_mul_f32 v128, s58, v128\n\t"
    "v_sub_f32 v192, 0xc2c00000, v128\n\t"
    "v_max_f32 v130, v130, v131\n\t"
    "v_mul_f32 v130, s58, v130\n\t"
    "v_sub_f32 v193, 0xc2c00000, v130\n\t"
    "v_max_f32 v132, v132, v133\n\t"
    "v_sub_f32 v196, 0xc2c00000, v132\n\t"
    "v_sub_f32 v197, 0xc2c00000, v132\n\t"
    "v_sub_f32 v198, 0xc2c00000, v132\n\t"
    "v_sub_f32 v199, 0xc2c00000, v132\n\t"
    "v_add_f32 v40, v40, v196\n\t"
    "v_add_f32 v41, v41, v197\n\t"
    "v_add_f32 v42, v42, v198\n\t"
    "v_add_f32 v43, v43, v199\n\t"
    "v_add_f32 v56, v56, v196\n\t"
    "v_add_f32 v57, v57, v197\n\t"
    "v_add_f32 v58, v58, v198\n\t"
    "v_add_f32 v59, v59, v199\n\t"
    "v_max_f32 v134, v134, v135\n\t"
    "v_sub_f32 v200, 0xc2c00000, v134\n\t"
    "v_sub_f32 v201, 0xc2c00000, v134\n\t"
    "v_sub_f32 v202, 0xc2c00000, v134\n\t"
    "v_sub_f32 v203, 0xc2c00000, v134\n\t"
    "v_add_f32 v44, v44, v200\n\t"
    "v_add_f32 v45, v45, v201\n\t"
    "v_add_f32 v46, v46, v202\n\t"
    "v_add_f32 v47, v47, v203\n\t"
    "v_add_f32 v60, v60, v200\n\t"
    "v_add_f32 v61, v61, v201\n\t"
    "v_add_f32 v62, v62, v202\n\t"
    "v_add_f32 v63, v63, v203\n\t"
    "v_fma_f32 v32, v32, s58, v192\n\t"
    "v_fma_f32 v33, v33, s58, v192\n\t"
    "v_fma_f32 v34, v34, s58, v192\n\t"
    "v_fma_f32 v35, v35, s58, v192\n\t"
    "v_fma_f32 v36, v36, s58, v193\n\t"
    "v_fma_f32 v37, v37, s58, v193\n\t"
    "v_fma_f32 v38, v38, s58, v193\n\t"
    "v_fma_f32 v39, v39, s58, v193\n\t"
    "v_fma_f32 v48, v48, s58, v192\n\t"
    "v_fma_f32 v49, v49, s58, v192\n\t"
    "v_fma_f32 v50, v50, s58, v192\n\t"
    "v_fma_f32 v51, v51, s58, v192\n\t"
    "v_mov_b32 v52, v144\n\t"
    "v_mov_b32 v53, v145\n\t"
    "v_mov_b32 v54, v146\n\t"
    "v_mov_b32 v55, v147\n\t"
    "v_exp_f32 v32, v32\n\t"
    "v_exp_f32 v33, v33\n\t"
    "v_exp_f32 v34, v34\n\t"
    "v_exp_f32 v35, v35\n\t"
    "v_exp_f32 v36, v36\n\t"
    "v_exp_f32 v37, v37\n\t"
    "v_exp_f32 v38, v38\n\t"
    "v_exp_f32 v39, v39\n\t"
    "v_exp_f32 v40, v40\n\t"
    "v_exp_f32 v41, v41\n\t"
    "v_exp_f32 v42, v42\n\t"
    "v_exp_f32 v43, v43\n\t"
    "v_exp_f32 v44, v44\n\t"
    "v_exp_f32 v45, v45\n\t"
    "v_exp_f32 v46, v46\n\t"
    "v_exp_f32 v47, v47\n\t"
    "v_exp_f32 v48, v48\n\t"
    "v_exp_f32 v49, v49\n\t"
    "v_exp_f32 v50, v50\n\t"
    "v_exp_f32 v51, v51\n\t"
    "v_exp_f32 v56, v56\n\t"
    "v_exp_f32 v57, v57\n\t"
    "v_exp_f32 v58, v58\n\t"
    "v_exp_f32 v59, v59\n\t"
    "s_nop 0\n\t"
    "v_cvt_pk_bf16_f32 v64, v32, v33\n\t"
    "v_cvt_pk_bf16_f32 v65, v34, v35\n\t"
    "v_cvt_pk_bf16_f32 v80, v36, v37\n\t"
    "v_cvt_pk_bf16_f32 v81, v38, v39\n\t"
    "v_cvt_pk_bf16_f32 v96, v40, v41\n\t"
    "v_cvt_pk_bf16_f32 v97, v42, v43\n\t"
    "v_cvt_pk_bf16_f32 v112, v44, v45\n\t"
    "v_cvt_pk_bf16_f32 v113, v46, v47\n\t"
    "v_cvt_pk_bf16_f32 v66, v48, v49\n\t"
    "v_cvt_pk_bf16_f32 v67, v50, v51\n\t"
    "v_cvt_pk_bf16_f32 v98, v56, v57\n\t"
    "v_cvt_pk_bf16_f32 v99, v58, v59\n\t"
    "s_mov_b32 s44, 8\n\t"
    "s_waitcnt lgkmcnt(0)\n\t"
    "v_mfma_f32_16x16x32_bf16 v[32:35], a[192:195], a[128:131], 0\n\t"
    "v_cvt_pk_bf16_f32 v52, 0, v52\n\t"
    "v_cvt_pk_bf16_f32 v53, 0, v53\n\t"
    "v_cvt_pk_bf16_f32 v54, 0, v54\n\t"
    "v_mfma_f32_16x16x32_bf16 v[32:35], a[196:199], a[132:135], v[32:35]\n\t"
    "ds_read_b128 a[208:211], v228 offset:12288\n\t"
    "v_cvt_pk_bf16_f32 v55, 0, v55\n\t"
    "v_mul_f32 v52, s56, v52\n\t"
    "v_mfma_f32_16x16x32_bf16 v[32:35], a[200:203], a[136:139], v[32:35]\n\t"
    "v_mul_f32 v53, s56, v53\n\t"
    "v_mul_f32 v54, s56, v54\n\t"
    "v_mul_f32 v55, s56, v55\n\t"
    "v_mfma_f32_16x16x32_bf16 v[32:35], a[204:207], a[140:143], v[32:35]\n\t"
    "s_add_u32 m0, s42, 0\n\t"
    "s_mov_b32 s45, 32768\n\t"
    "buffer_load_dwordx4 %[ksrc0], %[srdK], s45 offen offset:0 lds\n\t"
    "v_mfma_f32_16x16x32_bf16 v[36:39], a[192:195], a[144:147], 0\n\t"
    "v_cvt_pk_bf16_f32 v52, 0, v52\n\t"
    "v_cvt_pk_bf16_f32 v53, 0, v53\n\t"
    "v_cvt_pk_bf16_f32 v54, 0, v54\n\t"
    "v_mfma_f32_16x16x32_bf16 v[36:39], a[196:199], a[148:151], v[36:39]\n\t"
    "ds_read_b128 a[212:215], v229 offset:12288\n\t"
    "v_cvt_pk_bf16_f32 v55, 0, v55\n\t"
    "v_fma_f32 v52, v52, s58, v193\n\t"
    "v_mfma_f32_16x16x32_bf16 v[36:39], a[200:203], a[152:155], v[36:39]\n\t"
    "v_fma_f32 v53, v53, s58, v193\n\t"
    "v_fma_f32 v54, v54, s58, v193\n\t"
    "v_fma_f32 v55, v55, s58, v193\n\t"
    "v_mfma_f32_16x16x32_bf16 v[36:39], a[204:207], a[156:159], v[36:39]\n\t"
    "buffer_load_dwordx4 v243, %[srdK], s45 offen offset:2048 lds\n\t"
    "v_exp_f32 v52, v52\n\t"
    "v_mfma_f32_16x16x32_bf16 v[40:43], a[192:195], a[160:163], v[196:199]\n\t"
    "v_exp_f32 v53, v53\n\t"
    "v_mfma_f32_16x16x32_bf16 v[40:43], a[196:199], a[164:167], v[40:43]\n\t"
    "ds_read_b128 a[216:219], v230 offset:12288\n\t"
    "v_exp_f32 v54, v54\n\t"
    "v_mfma_f32_16x16x32_bf16 v[40:43], a[200:203], a[168:171], v[40:43]\n\t"
    "v_exp_f32 v55, v55\n\t"
    "v_exp_f32 v60, v60\n\t"
    "v_mfma_f32_16x16x32_bf16 v[40:43], a[204:207], a[172:175], v[40:43]\n\t"
    "buffer_load_dwordx4 v242, %[srdK], s45 offen offset:1024 lds\n\t"
    "v_mfma_f32_16x16x32_bf16 v[44:47], a[192:195], a[176:179], v[200:203]\n\t"
    "v_exp_f32 v61, v61\n\t"
    "v_exp_f32 v62, v62\n\t"
    "v_mfma_f32_16x16x32_bf16 v[44:47], a[196:199], a[180:183], v[44:47]\n\t"
    "ds_read_b128 a[220:223], v231 offset:12288\n\t"
    "v_exp_f32 v63, v63\n\t"
    "v_mfma_f32_16x16x32_bf16 v[44:47], a[200:203], a[184:187], v[44:47]\n\t"
    "v_cvt_pk_bf16_f32 v82, v52, v53\n\t"
    "v_cvt_pk_bf16_f32 v83, v54, v55\n\t"
    "v_cvt_pk_bf16_f32 v114, v60, v61\n\t"
    "v_mfma_f32_16x16x32_bf16 v[44:47], a[204:207], a[188:191], v[44:47]\n\t"
    "buffer_load_dwordx4 v244, %[srdK], s45 offen offset:3072 lds\n\t"
    "v_cvt_pk_bf16_f32 v115, v62, v63\n\t"
    "s_add_u32 m0, s42, 4096\n\t"
    "s_mov_b32 s45, 36864\n\t"
    "buffer_load_dwordx4 %[ksrc0], %[srdK], s45 offen offset:0 lds\n\t"
    "buffer_load_dwordx4 v243, %[srdK], s45 offen offset:2048 lds\n\t"
    "buffer_load_dwordx4 v242, %[srdK], s45 offen offset:1024 lds\n\t"
    "buffer_load_dwordx4 v244, %[srdK], s45 offen offset:3072 lds\n\t"
    "v_cvt_pk_bf16_f32 v32, 0, v32\n\t"
    "v_cvt_pk_bf16_f32 v33, 0, v33\n\t"
    "v_cvt_pk_bf16_f32 v34, 0, v34\n\t"
    "v_cvt_pk_bf16_f32 v35, 0, v35\n\t"
    "v_mul_f32 v32, s56, v32\n\t"
    "v_mul_f32 v33, s56, v33\n\t"
    "v_mul_f32 v34, s56, v34\n\t"
    "v_mul_f32 v35, s56, v35\n\t"
    "v_cvt_pk_bf16_f32 v32, 0, v32\n\t"
    "v_cvt_pk_bf16_f32 v33, 0, v33\n\t"
    "v_cvt_pk_bf16_f32 v34, 0, v34\n\t"
    "v_cvt_pk_bf16_f32 v35, 0, v35\n\t"
    "v_fma_f32 v32, v32, s58, v192\n\t"
    "v_fma_f32 v33, v33, s58, v192\n\t"
    "v_fma_f32 v34, v34, s58, v192\n\t"
    "v_fma_f32 v35, v35, s58, v192\n\t"
    "v_exp_f32 v32, v32\n\t"
    "v_exp_f32 v33, v33\n\t"
    "v_exp_f32 v34, v34\n\t"
    "v_exp_f32 v35, v35\n\t"
    "v_exp_f32 v40, v40\n\t"
    "v_exp_f32 v41, v41\n\t"
    "v_exp_f32 v42, v42\n\t"
    "v_exp_f32 v43, v43\n\t"
    "v_cvt_pk_bf16_f32 v68, v32, v33\n\t"
    "v_cvt_pk_bf16_f32 v69, v34, v35\n\t"
    "v_cvt_pk_bf16_f32 v100, v40, v41\n\t"
    "v_cvt_pk_bf16_f32 v101, v42, v43\n\t"
    "s_waitcnt lgkmcnt(0)\n\t"
    "v_mfma_f32_16x16x32_bf16 v[48:51], a[208:211], a[128:131], 0\n\t"
    "v_cvt_pk_bf16_f32 v36, 0, v36\n\t"
    "v_cvt_pk_bf16_f32 v37, 0, v37\n\t"
    "v_cvt_pk_bf16_f32 v38, 0, v38\n\t"
    "v_mfma_f32_16x16x32_bf16 v[48:51], a[212:215], a[132:135], v[48:51]\n\t"
    "ds_read_b128 a[192:195], v228 offset:16384\n\t"
    "v_cvt_pk_bf16_f32 v39, 0, v39\n\t"
    "v_mul_f32 v36, s56, v36\n\t"
    "v_mfma_f32_16x16x32_bf16 v[48:51], a[216:219], a[136:139], v[48:51]\n\t"
    "v_mul_f32 v37, s56, v37\n\t"
    "v_mul_f32 v38, s56, v38\n\t"
    "v_mul_f32 v39, s56, v39\n\t"
    "v_mfma_f32_16x16x32_bf16 v[48:51], a[220:223], a[140:143], v[48:51]\n\t"
    "s_add_u32 m0, s43, 0\n\t"
    "s_mov_b32 s46, 0\n\t"
    "buffer_load_dwordx4 %[vsrc0], %[srdV], s46 offen offset:0 lds\n\t"
    "v_mfma_f32_16x16x32_bf16 v[52:55], a[208:211], a[144:147], 0\n\t"
    "v_cvt_pk_bf16_f32 v36, 0, v36\n\t"
    "v_cvt_pk_bf16_f32 v37, 0, v37\n\t"
    "v_cvt_pk_bf16_f32 v38, 0, v38\n\t"
    "v_mfma_f32_16x16x32_bf16 v[52:55], a[212:215], a[148:151], v[52:55]\n\t"
    "ds_read_b128 a[196:199], v229 offset:16384\n\t"
    "v_cvt_pk_bf16_f32 v39, 0, v39\n\t"
    "v_fma_f32 v36, v36, s58, v193\n\t"
    "v_mfma_f32_16x16x32_bf16 v[52:55], a[216:219], a[152:155], v[52:55]\n\t"
    "v_fma_f32 v37, v37, s58, v193\n\t"
    "v_fma_f32 v38, v38, s58, v193\n\t"
    "v_fma_f32 v39, v39, s58, v193\n\t"
    "v_mfma_f32_16x16x32_bf16 v[52:55], a[220:223], a[156:159], v[52:55]\n\t"
    "buffer_load_dwordx4 %[vsrc0], %[srdV], s46 offen offset:2048 lds\n\t"
    "v_exp_f32 v36, v36\n\t"
    "v_mfma_f32_16x16x32_bf16 v[56:59], a[208:211], a[160:163], v[196:199]\n\t"
    "v_exp_f32 v37, v37\n\t"
    "v_mfma_f32_16x16x32_bf16 v[56:59], a[212:215], a[164:167], v[56:59]\n\t"
    "ds_read_b128 a[200:203], v230 offset:16384\n\t"
    "v_exp_f32 v38, v38\n\t"
    "v_mfma_f32_16x16x32_bf16 v[56:59], a[216:219], a[168:171], v[56:59]\n\t"
    "v_exp_f32 v39, v39\n\t"
    "v_exp_f32 v44, v44\n\t"
    "v_mfma_f32_16x16x32_bf16 v[56:59], a[220:223], a[172:175], v[56:59]\n\t"
    "buffer_load_dwordx4 v245, %[srdV], s46 offen offset:1024 lds\n\t"
    "v_mfma_f32_16x16x32_bf16 v[60:63], a[208:211], a[176:179], v[200:203]\n\t"
    "v_exp_f32 v45, v45\n\t"
    "v_exp_f32 v46, v46\n\t"
    "v_mfma_f32_16x16x32_bf16 v[60:63], a[212:215], a[180:183], v[60:63]\n\t"
    "ds_read_b128 a[204:207], v231 offset:16384\n\t"
    "v_exp_f32 v47, v47\n\t"
    "v_mfma_f32_16x16x32_bf16 v[60:63], a[216:219], a[184:187], v[60:63]\n\t"
    "v_cvt_pk_bf16_f32 v84, v36, v37\n\t"
    "v_cvt_pk_bf16_f32 v85, v38, v39\n\t"
    "v_cvt_pk_bf16_f32 v116, v44, v45\n\t"
    "v_mfma_f32_16x16x32_bf16 v[60:63], a[220:223], a[188:191], v[60:63]\n\t"
    "buffer_load_dwordx4 v245, %[srdV], s46 offen offset:3072 lds\n\t"
    "v_cvt_pk_bf16_f32 v117, v46, v47\n\t"
    "s_add_u32 m0, s43, 4096\n\t"
    "s_mov_b32 s46, 4096\n\t"
    "buffer_load_dwordx4 %[vsrc0], %[srdV], s46 offen offset:0 lds\n\t"
    "buffer_load_dwordx4 %[vsrc0], %[srdV], s46 offen offset:2048 lds\n\t"
    "buffer_load_dwordx4 v245, %[srdV], s46 offen offset:1024 lds\n\t"
    "buffer_load_dwordx4 v245, %[srdV], s46 offen offset:3072 lds\n\t"
    "v_cvt_pk_bf16_f32 v48, 0, v48\n\t"
    "v_cvt_pk_bf16_f32 v49, 0, v49\n\t"
    "v_cvt_pk_bf16_f32 v50, 0, v50\n\t"
    "v_cvt_pk_bf16_f32 v51, 0, v51\n\t"
    "v_mul_f32 v48, s56, v48\n\t"
    "v_mul_f32 v49, s56, v49\n\t"
    "v_mul_f32 v50, s56, v50\n\t"
    "v_mul_f32 v51, s56, v51\n\t"
    "v_cvt_pk_bf16_f32 v48, 0, v48\n\t"
    "v_cvt_pk_bf16_f32 v49, 0, v49\n\t"
    "v_cvt_pk_bf16_f32 v50, 0, v50\n\t"
    "v_cvt_pk_bf16_f32 v51, 0, v51\n\t"
    "v_fma_f32 v48, v48, s58, v192\n\t"
    "v_fma_f32 v49, v49, s58, v192\n\t"
    "v_fma_f32 v50, v50, s58, v192\n\t"
    "v_fma_f32 v51, v51, s58, v192\n\t"
    "v_exp_f32 v48, v48\n\t"
    "v_exp_f32 v49, v49\n\t"
    "v_exp_f32 v50, v50\n\t"
    "v_exp_f32 v51, v51\n\t"
    "v_exp_f32 v56, v56\n\t"
    "v_exp_f32 v57, v57\n\t"
    "v_exp_f32 v58, v58\n\t"
    "v_exp_f32 v59, v59\n\t"
    "v_cvt_pk_bf16_f32 v70, v48, v49\n\t"
    "v_cvt_pk_bf16_f32 v71, v50, v51\n\t"
    "v_cvt_pk_bf16_f32 v102, v56, v57\n\t"
    "v_cvt_pk_bf16_f32 v103, v58, v59\n\t"
    "s_waitcnt lgkmcnt(0)\n\t"
    "v_mfma_f32_16x16x32_bf16 v[32:35], a[192:195], a[128:131], 0\n\t"
    "v_cvt_pk_bf16_f32 v52, 0, v52\n\t"
    "v_cvt_pk_bf16_f32 v53, 0, v53\n\t"
    "v_cvt_pk_bf16_f32 v54, 0, v54\n\t"
    "v_mfma_f32_16x16x32_bf16 v[32:35], a[196:199], a[132:135], v[32:35]\n\t"
    "ds_read_b128 a[208:211], v228 offset:20480\n\t"
    "v_cvt_pk_bf16_f32 v55, 0, v55\n\t"
    "v_mfma_f32_16x16x32_bf16 v[32:35], a[200:203], a[136:139], v[32:35]\n\t"
    "v_mul_f32 v52, s56, v52\n\t"
    "v_mul_f32 v53, s56, v53\n\t"
    "v_mul_f32 v54, s56, v54\n\t"
    "v_mfma_f32_16x16x32_bf16 v[32:35], a[204:207], a[140:143], v[32:35]\n\t"
    "v_mul_f32 v55, s56, v55\n\t"
    "v_cvt_pk_bf16_f32 v52, 0, v52\n\t"
    "v_mfma_f32_16x16x32_bf16 v[36:39], a[192:195], a[144:147], 0\n\t"
    "v_cvt_pk_bf16_f32 v53, 0, v53\n\t"
    "v_cvt_pk_bf16_f32 v54, 0, v54\n\t"
    "v_cvt_pk_bf16_f32 v55, 0, v55\n\t"
    "v_mfma_f32_16x16x32_bf16 v[36:39], a[196:199], a[148:151], v[36:39]\n\t"
    "ds_read_b128 a[212:215], v229 offset:20480\n\t"
    "v_fma_f32 v52, v52, s58, v193\n\t"
    "v_mfma_f32_16x16x32_bf16 v[36:39], a[200:203], a[152:155], v[36:39]\n\t"
    "v_fma_f32 v53, v53, s58, v193\n\t"
    "v_fma_f32 v54, v54, s58, v193\n\t"
    "v_fma_f32 v55, v55, s58, v193\n\t"
    "v_mfma_f32_16x16x32_bf16 v[36:39], a[204:207], a[156:159], v[36:39]\n\t"
    "v_exp_f32 v52, v52\n\t"
    "v_mfma_f32_16x16x32_bf16 v[40:43], a[192:195], a[160:163], v[196:199]\n\t"
    "v_exp_f32 v53, v53\n\t"
    "v_mfma_f32_16x16x32_bf16 v[40:43], a[196:199], a[164:167], v[40:43]\n\t"
    "ds_read_b128 a[216:219], v230 offset:20480\n\t"
    "v_exp_f32 v54, v54\n\t"
    "v_mfma_f32_16x16x32_bf16 v[40:43], a[200:203], a[168:171], v[40:43]\n\t"
    "v_exp_f32 v55, v55\n\t"
    "v_mfma_f32_16x16x32_bf16 v[40:43], a[204:207], a[172:175], v[40:43]\n\t"
    "v_exp_f32 v60, v60\n\t"
    "v_exp_f32 v61, v61\n\t"
    "v_mfma_f32_16x16x32_bf16 v[44:47], a[192:195], a[176:179], v[200:203]\n\t"
    "v_exp_f32 v62, v62\n\t"
    "v_mfma_f32_16x16x32_bf16 v[44:47], a[196:199], a[180:183], v[44:47]\n\t"
    "ds_read_b128 a[220:223], v231 offset:20480\n\t"
    "v_exp_f32 v63, v63\n\t"
    "v_mfma_f32_16x16x32_bf16 v[44:47], a[200:203], a[184:187], v[44:47]\n\t"
    "v_cvt_pk_bf16_f32 v86, v52, v53\n\t"
    "v_cvt_pk_bf16_f32 v87, v54, v55\n\t"
    "v_mfma_f32_16x16x32_bf16 v[44:47], a[204:207], a[188:191], v[44:47]\n\t"
    "v_cvt_pk_bf16_f32 v118, v60, v61\n\t"
    "v_cvt_pk_bf16_f32 v119, v62, v63\n\t"
    "v_cvt_pk_bf16_f32 v32, 0, v32\n\t"
    "v_cvt_pk_bf16_f32 v33, 0, v33\n\t"
    "v_cvt_pk_bf16_f32 v34, 0, v34\n\t"
    "v_cvt_pk_bf16_f32 v35, 0, v35\n\t"
    "v_mul_f32 v32, s56, v32\n\t"
    "v_mul_f32 v33, s56, v33\n\t"
    "v_mul_f32 v34, s56, v34\n\t"
    "v_mul_f32 v35, s56, v35\n\t"
    "v_cvt_pk_bf16_f32 v32, 0, v32\n\t"
    "v_cvt_pk_bf16_f32 v33, 0, v33\n\t"
    "v_cvt_pk_bf16_f32 v34, 0, v34\n\t"
    "v_cvt_pk_bf16_f32 v35, 0, v35\n\t"
    "v_fma_f32 v32, v32, s58, v192\n\t"
    "v_fma_f32 v33, v33, s58, v192\n\t"
    "v_fma_f32 v34, v34, s58, v192\n\t"
    "v_fma_f32 v35, v35, s58, v192\n\t"
    "v_exp_f32 v32, v32\n\t"
    "v_exp_f32 v33, v33\n\t"
    "v_exp_f32 v34, v34\n\t"
    "v_exp_f32 v35, v35\n\t"
    "v_exp_f32 v40, v40\n\t"
    "v_exp_f32 v41, v41\n\t"
    "v_exp_f32 v42, v42\n\t"
    "v_exp_f32 v43, v43\n\t"
    "v_cvt_pk_bf16_f32 v72, v32, v33\n\t"
    "v_cvt_pk_bf16_f32 v73, v34, v35\n\t"
    "v_cvt_pk_bf16_f32 v104, v40, v41\n\t"
    "v_cvt_pk_bf16_f32 v105, v42, v43\n\t"
    "s_waitcnt lgkmcnt(0)\n\t"
    "v_mfma_f32_16x16x32_bf16 v[48:51], a[208:211], a[128:131], 0\n\t"
    "v_cvt_pk_bf16_f32 v36, 0, v36\n\t"
    "v_cvt_pk_bf16_f32 v37, 0, v37\n\t"
    "v_cvt_pk_bf16_f32 v38, 0, v38\n\t"
    "v_mfma_f32_16x16x32_bf16 v[48:51], a[212:215], a[132:135], v[48:51]\n\t"
    "ds_read_b128 a[192:195], v228 offset:24576\n\t"
    "v_cvt_pk_bf16_f32 v39, 0, v39\n\t"
    "v_mfma_f32_16x16x32_bf16 v[48:51], a[216:219], a[136:139], v[48:51]\n\t"
    "v_mul_f32 v36, s56, v36\n\t"
    "v_mul_f32 v37, s56, v37\n\t"
    "v_mul_f32 v38, s56, v38\n\t"
    "v_mfma_f32_16x16x32_bf16 v[48:51], a[220:223], a[140:143], v[48:51]\n\t"
    "v_mul_f32 v39, s56, v39\n\t"
    "v_cvt_pk_bf16_f32 v36, 0, v36\n\t"
    "v_mfma_f32_16x16x32_bf16 v[52:55], a[208:211], a[144:147], 0\n\t"
    "v_cvt_pk_bf16_f32 v37, 0, v37\n\t"
    "v_cvt_pk_bf16_f32 v38, 0, v38\n\t"
    "v_cvt_pk_bf16_f32 v39, 0, v39\n\t"
    "v_mfma_f32_16x16x32_bf16 v[52:55], a[212:215], a[148:151], v[52:55]\n\t"
    "ds_read_b128 a[196:199], v229 offset:24576\n\t"
    "v_fma_f32 v36, v36, s58, v193\n\t"
    "v_mfma_f32_16x16x32_bf16 v[52:55], a[216:219], a[152:155], v[52:55]\n\t"
    "v_fma_f32 v37, v37, s58, v193\n\t"
    "v_fma_f32 v38, v38, s58, v193\n\t"
    "v_fma_f32 v39, v39, s58, v193\n\t"
    "v_mfma_f32_16x16x32_bf16 v[52:55], a[220:223], a[156:159], v[52:55]\n\t"
    "v_exp_f32 v36, v36\n\t"
    "v_mfma_f32_16x16x32_bf16 v[56:59], a[208:211], a[160:163], v[196:199]\n\t"
    "v_exp_f32 v37, v37\n\t"
    "v_mfma_f32_16x16x32_bf16 v[56:59], a[212:215], a[164:167], v[56:59]\n\t"
    "ds_read_b128 a[200:203], v230 offset:24576\n\t"
    "v_exp_f32 v38, v38\n\t"
    "v_mfma_f32_16x16x32_bf16 v[56:59], a[216:219], a[168:171], v[56:59]\n\t"
    "v_exp_f32 v39, v39\n\t"
    "v_mfma_f32_16x16x32_bf16 v[56:59], a[220:223], a[172:175], v[56:59]\n\t"
    "v_exp_f32 v44, v44\n\t"
    "v_exp_f32 v45, v45\n\t"
    "v_mfma_f32_16x16x32_bf16 v[60:63], a[208:211], a[176:179], v[200:203]\n\t"
    "v_exp_f32 v46, v46\n\t"
    "v_mfma_f32_16x16x32_bf16 v[60:63], a[212:215], a[180:183], v[60:63]\n\t"
    "ds_read_b128 a[204:207], v231 offset:24576\n\t"
    "v_exp_f32 v47, v47\n\t"
    "v_mfma_f32_16x16x32_bf16 v[60:63], a[216:219], a[184:187], v[60:63]\n\t"
    "v_cvt_pk_bf16_f32 v88, v36, v37\n\t"
    "v_cvt_pk_bf16_f32 v89, v38, v39\n\t"
    "v_mfma_f32_16x16x32_bf16 v[60:63], a[220:223], a[188:191], v[60:63]\n\t"
    "v_cvt_pk_bf16_f32 v120, v44, v45\n\t"
    "v_cvt_pk_bf16_f32 v121, v46, v47\n\t"
    "v_cvt_pk_bf16_f32 v48, 0, v48\n\t"
    "v_cvt_pk_bf16_f32 v49, 0, v49\n\t"
    "v_cvt_pk_bf16_f32 v50, 0, v50\n\t"
    "v_cvt_pk_bf16_f32 v51, 0, v51\n\t"
    "v_mul_f32 v48, s56, v48\n\t"
    "v_mul_f32 v49, s56, v49\n\t"
    "v_mul_f32 v50, s56, v50\n\t"
    "v_mul_f32 v51, s56, v51\n\t"
    "v_cvt_pk_bf16_f32 v48, 0, v48\n\t"
    "v_cvt_pk_bf16_f32 v49, 0, v49\n\t"
    "v_cvt_pk_bf16_f32 v50, 0, v50\n\t"
    "v_cvt_pk_bf16_f32 v51, 0, v51\n\t"
    "v_fma_f32 v48, v48, s58, v192\n\t"
    "v_fma_f32 v49, v49, s58, v192\n\t"
    "v_fma_f32 v50, v50, s58, v192\n\t"
    "v_fma_f32 v51, v51, s58, v192\n\t"
    "v_exp_f32 v48, v48\n\t"
    "v_exp_f32 v49, v49\n\t"
    "v_exp_f32 v50, v50\n\t"
    "v_exp_f32 v51, v51\n\t"
    "v_exp_f32 v56, v56\n\t"
    "v_exp_f32 v57, v57\n\t"
    "v_exp_f32 v58, v58\n\t"
    "v_exp_f32 v59, v59\n\t"
    "v_cvt_pk_bf16_f32 v74, v48, v49\n\t"
    "v_cvt_pk_bf16_f32 v75, v50, v51\n\t"
    "v_cvt_pk_bf16_f32 v106, v56, v57\n\t"
    "v_cvt_pk_bf16_f32 v107, v58, v59\n\t"
    "s_waitcnt lgkmcnt(0)\n\t"
    "v_mfma_f32_16x16x32_bf16 v[32:35], a[192:195], a[128:131], 0\n\t"
    "v_cvt_pk_bf16_f32 v52, 0, v52\n\t"
    "v_cvt_pk_bf16_f32 v53, 0, v53\n\t"
    "v_cvt_pk_bf16_f32 v54, 0, v54\n\t"
    "v_mfma_f32_16x16x32_bf16 v[32:35], a[196:199], a[132:135], v[32:35]\n\t"
    "ds_read_b128 a[208:211], v228 offset:28672\n\t"
    "v_cvt_pk_bf16_f32 v55, 0, v55\n\t"
    "v_mfma_f32_16x16x32_bf16 v[32:35], a[200:203], a[136:139], v[32:35]\n\t"
    "v_mul_f32 v52, s56, v52\n\t"
    "v_mul_f32 v53, s56, v53\n\t"
    "v_mul_f32 v54, s56, v54\n\t"
    "v_mfma_f32_16x16x32_bf16 v[32:35], a[204:207], a[140:143], v[32:35]\n\t"
    "v_mul_f32 v55, s56, v55\n\t"
    "v_cvt_pk_bf16_f32 v52, 0, v52\n\t"
    "v_mfma_f32_16x16x32_bf16 v[36:39], a[192:195], a[144:147], 0\n\t"
    "v_cvt_pk_bf16_f32 v53, 0, v53\n\t"
    "v_cvt_pk_bf16_f32 v54, 0, v54\n\t"
    "v_cvt_pk_bf16_f32 v55, 0, v55\n\t"
    "v_mfma_f32_16x16x32_bf16 v[36:39], a[196:199], a[148:151], v[36:39]\n\t"
    "ds_read_b128 a[212:215], v229 offset:28672\n\t"
    "v_fma_f32 v52, v52, s58, v193\n\t"
    "v_mfma_f32_16x16x32_bf16 v[36:39], a[200:203], a[152:155], v[36:39]\n\t"
    "v_fma_f32 v53, v53, s58, v193\n\t"
    "v_fma_f32 v54, v54, s58, v193\n\t"
    "v_fma_f32 v55, v55, s58, v193\n\t"
    "v_mfma_f32_16x16x32_bf16 v[36:39], a[204:207], a[156:159], v[36:39]\n\t"
    "v_exp_f32 v52, v52\n\t"
    "v_mfma_f32_16x16x32_bf16 v[40:43], a[192:195], a[160:163], v[196:199]\n\t"
    "v_exp_f32 v53, v53\n\t"
    "v_mfma_f32_16x16x32_bf16 v[40:43], a[196:199], a[164:167], v[40:43]\n\t"
    "ds_read_b128 a[216:219], v230 offset:28672\n\t"
    "v_exp_f32 v54, v54\n\t"
    "v_mfma_f32_16x16x32_bf16 v[40:43], a[200:203], a[168:171], v[40:43]\n\t"
    "v_exp_f32 v55, v55\n\t"
    "v_mfma_f32_16x16x32_bf16 v[40:43], a[204:207], a[172:175], v[40:43]\n\t"
    "v_exp_f32 v60, v60\n\t"
    "v_exp_f32 v61, v61\n\t"
    "v_mfma_f32_16x16x32_bf16 v[44:47], a[192:195], a[176:179], v[200:203]\n\t"
    "v_exp_f32 v62, v62\n\t"
    "v_mfma_f32_16x16x32_bf16 v[44:47], a[196:199], a[180:183], v[44:47]\n\t"
    "ds_read_b128 a[220:223], v231 offset:28672\n\t"
    "v_exp_f32 v63, v63\n\t"
    "v_mfma_f32_16x16x32_bf16 v[44:47], a[200:203], a[184:187], v[44:47]\n\t"
    "v_cvt_pk_bf16_f32 v90, v52, v53\n\t"
    "v_cvt_pk_bf16_f32 v91, v54, v55\n\t"
    "v_mfma_f32_16x16x32_bf16 v[44:47], a[204:207], a[188:191], v[44:47]\n\t"
    "v_cvt_pk_bf16_f32 v122, v60, v61\n\t"
    "v_cvt_pk_bf16_f32 v123, v62, v63\n\t"
    "v_cvt_pk_bf16_f32 v32, 0, v32\n\t"
    "v_cvt_pk_bf16_f32 v33, 0, v33\n\t"
    "v_cvt_pk_bf16_f32 v34, 0, v34\n\t"
    "v_cvt_pk_bf16_f32 v35, 0, v35\n\t"
    "v_mul_f32 v32, s56, v32\n\t"
    "v_mul_f32 v33, s56, v33\n\t"
    "v_mul_f32 v34, s56, v34\n\t"
    "v_mul_f32 v35, s56, v35\n\t"
    "v_cvt_pk_bf16_f32 v32, 0, v32\n\t"
    "v_cvt_pk_bf16_f32 v33, 0, v33\n\t"
    "v_cvt_pk_bf16_f32 v34, 0, v34\n\t"
    "v_cvt_pk_bf16_f32 v35, 0, v35\n\t"
    "v_fma_f32 v32, v32, s58, v192\n\t"
    "v_fma_f32 v33, v33, s58, v192\n\t"
    "v_fma_f32 v34, v34, s58, v192\n\t"
    "v_fma_f32 v35, v35, s58, v192\n\t"
    "v_exp_f32 v32, v32\n\t"
    "v_exp_f32 v33, v33\n\t"
    "v_exp_f32 v34, v34\n\t"
    "v_exp_f32 v35, v35\n\t"
    "v_exp_f32 v40, v40\n\t"
    "v_exp_f32 v41, v41\n\t"
    "v_exp_f32 v42, v42\n\t"
    "v_exp_f32 v43, v43\n\t"
    "v_cvt_pk_bf16_f32 v76, v32, v33\n\t"
    "v_cvt_pk_bf16_f32 v77, v34, v35\n\t"
    "v_cvt_pk_bf16_f32 v108, v40, v41\n\t"
    "v_cvt_pk_bf16_f32 v109, v42, v43\n\t"
    "s_waitcnt vmcnt(0) lgkmcnt(0)\n\t"
    "s_barrier\n\t"
    "v_mfma_f32_16x16x32_bf16 v[48:51], a[208:211], a[128:131], 0\n\t"
    "v_cvt_pk_bf16_f32 v36, 0, v36\n\t"
    "v_cvt_pk_bf16_f32 v37, 0, v37\n\t"
    "v_cvt_pk_bf16_f32 v38, 0, v38\n\t"
    "v_mfma_f32_16x16x32_bf16 v[48:51], a[212:215], a[132:135], v[48:51]\n\t"
    "ds_read_b128 a[192:195], v228 offset:32768\n\t"
    "v_cvt_pk_bf16_f32 v39, 0, v39\n\t"
    "v_mul_f32 v36, s56, v36\n\t"
    "v_mfma_f32_16x16x32_bf16 v[48:51], a[216:219], a[136:139], v[48:51]\n\t"
    "v_mul_f32 v37, s56, v37\n\t"
    "v_mul_f32 v38, s56, v38\n\t"
    "v_mul_f32 v39, s56, v39\n\t"
    "v_mfma_f32_16x16x32_bf16 v[48:51], a[220:223], a[140:143], v[48:51]\n\t"
    "s_xor_b32 s42, s42, 0x8000\n\t"
    "s_add_u32 m0, s42, 0\n\t"
    "s_add_u32 s45, s40, 0\n\t"
    "buffer_load_dwordx4 %[ksrc0], %[srdK], s45 offen offset:0 lds\n\t"
    "v_mfma_f32_16x16x32_bf16 v[52:55], a[208:211], a[144:147], 0\n\t"
    "v_cvt_pk_bf16_f32 v36, 0, v36\n\t"
    "v_mfma_f32_16x16x32_bf16 v[52:55], a[212:215], a[148:151], v[52:55]\n\t"
    "ds_read_b128 a[196:199], v229 offset:32768\n\t"
    "v_cvt_pk_bf16_f32 v37, 0, v37\n\t"
    "v_cvt_pk_bf16_f32 v38, 0, v38\n\t"
    "v_mfma_f32_16x16x32_bf16 v[52:55], a[216:219], a[152:155], v[52:55]\n\t"
    "v_cvt_pk_bf16_f32 v39, 0, v39\n\t"
    "v_fma_f32 v36, v36, s58, v193\n\t"
    "v_fma_f32 v37, v37, s58, v193\n\t"
    "v_mfma_f32_16x16x32_bf16 v[52:55], a[220:223], a[156:159], v[52:55]\n\t"
    "v_fma_f32 v38, v38, s58, v193\n\t"
    "v_fma_f32 v39, v39, s58, v193\n\t"
    "v_mfma_f32_16x16x32_bf16 v[56:59], a[208:211], a[160:163], v[196:199]\n\t"
    "v_exp_f32 v36, v36\n\t"
    "v_exp_f32 v37, v37\n\t"
    "v_mfma_f32_16x16x32_bf16 v[56:59], a[212:215], a[164:167], v[56:59]\n\t"
    "ds_read_b128 a[200:203], v230 offset:32768\n\t"
    "v_exp_f32 v38, v38\n\t"
    "v_mfma_f32_16x16x32_bf16 v[56:59], a[216:219], a[168:171], v[56:59]\n\t"
    "v_exp_f32 v39, v39\n\t"
    "v_mfma_f32_16x16x32_bf16 v[56:59], a[220:223], a[172:175], v[56:59]\n\t"
    "buffer_load_dwordx4 v242, %[srdK], s45 offen offset:1024 lds\n\t"
    "v_exp_f32 v44, v44\n\t"
    "v_mfma_f32_16x16x32_bf16 v[60:63], a[208:211], a[176:179], v[200:203]\n\t"
    "v_exp_f32 v45, v45\n\t"
    "v_mfma_f32_16x16x32_bf16 v[60:63], a[212:215], a[180:183], v[60:63]\n\t"
    "ds_read_b128 a[204:207], v231 offset:32768\n\t"
    "v_exp_f32 v46, v46\n\t"
    "v_mfma_f32_16x16x32_bf16 v[60:63], a[216:219], a[184:187], v[60:63]\n\t"
    "v_exp_f32 v47, v47\n\t"
    "v_cvt_pk_bf16_f32 v92, v36, v37\n\t"
    "v_mfma_f32_16x16x32_bf16 v[60:63], a[220:223], a[188:191], v[60:63]\n\t"
    "v_cvt_pk_bf16_f32 v93, v38, v39\n\t"
    "v_cvt_pk_bf16_f32 v124, v44, v45\n\t"
    "v_cvt_pk_bf16_f32 v125, v46, v47\n\t"
    "ds_read_b64_tr_b16 a[224:225], %[vb] offset:0\n\t"
    "ds_read_b64_tr_b16 a[226:227], %[vb] offset:4096\n\t"
    "buffer_load_dwordx4 v243, %[srdK], s45 offen offset:2048 lds\n\t"
    "ds_read_b64_tr_b16 a[228:229], %[vb] offset:8192\n\t"
    "ds_read_b64_tr_b16 a[230:231], %[vb] offset:12288\n\t"
    "ds_read_b64_tr_b16 a[232:233], %[vb] offset:16384\n\t"
    "ds_read_b64_tr_b16 a[234:235], %[vb] offset:20480\n\t"
    "buffer_load_dwordx4 v244, %[srdK], s45 offen offset:3072 lds\n\t"
    "ds_read_b64_tr_b16 a[236:237], %[vb] offset:24576\n\t"
    "ds_read_b64_tr_b16 a[238:239], %[vb] offset:28672\n\t"
    "v_cvt_pk_bf16_f32 v48, 0, v48\n\t"
    "v_cvt_pk_bf16_f32 v49, 0, v49\n\t"
    "v_cvt_pk_bf16_f32 v50, 0, v50\n\t"
    "v_cvt_pk_bf16_f32 v51, 0, v51\n\t"
    "v_mul_f32 v48, s56, v48\n\t"
    "v_mul_f32 v49, s56, v49\n\t"
    "v_mul_f32 v50, s56, v50\n\t"
    "v_mul_f32 v51, s56, v51\n\t"
    "v_cvt_pk_bf16_f32 v48, 0, v48\n\t"
    "v_cvt_pk_bf16_f32 v49, 0, v49\n\t"
    "v_cvt_pk_bf16_f32 v50, 0, v50\n\t"
    "v_cvt_pk_bf16_f32 v51, 0, v51\n\t"
    "v_fma_f32 v48, v48, s58, v192\n\t"
    "v_fma_f32 v49, v49, s58, v192\n\t"
    "v_fma_f32 v50, v50, s58, v192\n\t"
    "v_fma_f32 v51, v51, s58, v192\n\t"
    "v_exp_f32 v48, v48\n\t"
    "v_exp_f32 v49, v49\n\t"
    "v_exp_f32 v50, v50\n\t"
    "v_exp_f32 v51, v51\n\t"
    "v_exp_f32 v56, v56\n\t"
    "v_exp_f32 v57, v57\n\t"
    "v_exp_f32 v58, v58\n\t"
    "v_exp_f32 v59, v59\n\t"
    "v_cvt_pk_bf16_f32 v78, v48, v49\n\t"
    "v_cvt_pk_bf16_f32 v79, v50, v51\n\t"
    "v_cvt_pk_bf16_f32 v110, v56, v57\n\t"
    "v_cvt_pk_bf16_f32 v111, v58, v59\n\t"
    "s_mov_b32 s44, 7\n\t"
    "LOOP%=:\n\t"
    "s_waitcnt lgkmcnt(8)\n\t"
    "v_mfma_f32_16x16x32_bf16 v[32:35], a[192:195], a[128:131], 0\n\t"
    "v_cvt_pk_bf16_f32 v52, 0, v52\n\t"
    "v_cvt_pk_bf16_f32 v53, 0, v53\n\t"
    "v_cvt_pk_bf16_f32 v54, 0, v54\n\t"
    "v_mfma_f32_16x16x32_bf16 v[32:35], a[196:199], a[132:135], v[32:35]\n\t"
    "ds_read_b128 a[208:211], v228 offset:36864\n\t"
    "v_cvt_pk_bf16_f32 v55, 0, v55\n\t"
    "v_mul_f32 v52, s56, v52\n\t"
    "v_mfma_f32_16x16x32_bf16 v[32:35], a[200:203], a[136:139], v[32:35]\n\t"
    "v_mul_f32 v53, s56, v53\n\t"
    "v_mul_f32 v54, s56, v54\n\t"
    "v_mul_f32 v55, s56, v55\n\t"
    "v_mfma_f32_16x16x32_bf16 v[32:35], a[204:207], a[140:143], v[32:35]\n\t"
    "s_add_u32 m0, s42, 4096\n\t"
    "s_add_u32 s45, s40, 4096\n\t"
    "buffer_load_dwordx4 %[ksrc0], %[srdK], s45 offen offset:0 lds\n\t"
    "v_mfma_f32_16x16x32_bf16 v[36:39], a[192:195], a[144:147], 0\n\t"
    "v_cvt_pk_bf16_f32 v52, 0, v52\n\t"
    "v_mfma_f32_16x16x32_bf16 v[36:39], a[196:199], a[148:151], v[36:39]\n\t"
    "ds_read_b128 a[212:215], v229 offset:36864\n\t"
    "v_cvt_pk_bf16_f32 v53, 0, v53\n\t"
    "v_cvt_pk_bf16_f32 v54, 0, v54\n\t"
    "v_mfma_f32_16x16x32_bf16 v[36:39], a[200:203], a[152:155], v[36:39]\n\t"
    "v_cvt_pk_bf16_f32 v55, 0, v55\n\t"
    "v_fma_f32 v52, v52, s58, v193\n\t"
    "v_fma_f32 v53, v53, s58, v193\n\t"
    "v_mfma_f32_16x16x32_bf16 v[36:39], a[204:207], a[156:159], v[36:39]\n\t"
    "v_fma_f32 v54, v54, s58, v193\n\t"
    "v_fma_f32 v55, v55, s58, v193\n\t"
    "v_exp_f32 v52, v52\n\t"
    "v_mfma_f32_16x16x32_bf16 v[40:43], a[192:195], a[160:163], v[196:199]\n\t"
    "v_exp_f32 v53, v53\n\t"
    "v_mfma_f32_16x16x32_bf16 v[40:43], a[196:199], a[164:167], v[40:43]\n\t"
    "ds_read_b128 a[216:219], v230 offset:36864\n\t"
    "v_exp_f32 v54, v54\n\t"
    "v_mfma_f32_16x16x32_bf16 v[40:43], a[200:203], a[168:171], v[40:43]\n\t"
    "v_exp_f32 v55, v55\n\t"
    "v_mfma_f32_16x16x32_bf16 v[40:43], a[204:207], a[172:175], v[40:43]\n\t"
    "buffer_load_dwordx4 v242, %[srdK], s45 offen offset:1024 lds\n\t"
    "v_exp_f32 v60, v60\n\t"
    "v_mfma_f32_16x16x32_bf16 v[44:47], a[192:195], a[176:179], v[200:203]\n\t"
    "v_exp_f32 v61, v61\n\t"
    "v_mfma_f32_16x16x32_bf16 v[44:47], a[196:199], a[180:183], v[44:47]\n\t"
    "ds_read_b128 a[220:223], v231 offset:36864\n\t"
    "v_exp_f32 v62, v62\n\t"
    "v_mfma_f32_16x16x32_bf16 v[44:47], a[200:203], a[184:187], v[44:47]\n\t"
    "v_exp_f32 v63, v63\n\t"
    "v_cvt_pk_bf16_f32 v94, v52, v53\n\t"
    "v_mfma_f32_16x16x32_bf16 v[44:47], a[204:207], a[188:191], v[44:47]\n\t"
    "v_cvt_pk_bf16_f32 v95, v54, v55\n\t"
    "v_cvt_pk_bf16_f32 v126, v60, v61\n\t"
    "v_cvt_pk_bf16_f32 v127, v62, v63\n\t"
    "s_waitcnt lgkmcnt(4)\n\t"
    "v_mfma_f32_16x16x32_bf16 a[0:3], a[224:227], v[64:67], a[0:3]\n\t"
    "v_cvt_pk_bf16_f32 v32, 0, v32\n\t"
    "v_cvt_pk_bf16_f32 v33, 0, v33\n\t"
    "v_cvt_pk_bf16_f32 v34, 0, v34\n\t"
    "v_mfma_f32_16x16x32_bf16 a[0:3], a[228:231], v[68:71], a[0:3]\n\t"
    "ds_read_b64_tr_b16 a[240:241], %[vb] offset:256\n\t"
    "ds_read_b64_tr_b16 a[242:243], %[vb] offset:4352\n\t"
    "v_mfma_f32_16x16x32_bf16 a[0:3], a[232:235], v[72:75], a[0:3]\n\t"
    "v_cvt_pk_bf16_f32 v35, 0, v35\n\t"
    "v_mul_f32 v32, s56, v32\n\t"
    "v_mul_f32 v33, s56, v33\n\t"
    "v_mfma_f32_16x16x32_bf16 a[0:3], a[236:239], v[76:79], a[0:3]\n\t"
    "buffer_load_dwordx4 v243, %[srdK], s45 offen offset:2048 lds\n\t"
    "v_mul_f32 v34, s56, v34\n\t"
    "v_mfma_f32_16x16x32_bf16 a[4:7], a[224:227], v[80:83], a[4:7]\n\t"
    "v_mul_f32 v35, s56, v35\n\t"
    "v_cvt_pk_bf16_f32 v32, 0, v32\n\t"
    "v_mfma_f32_16x16x32_bf16 a[4:7], a[228:231], v[84:87], a[4:7]\n\t"
    "ds_read_b64_tr_b16 a[244:245], %[vb] offset:8448\n\t"
    "ds_read_b64_tr_b16 a[246:247], %[vb] offset:12544\n\t"
    "v_cvt_pk_bf16_f32 v33, 0, v33\n\t"
    "v_mfma_f32_16x16x32_bf16 a[4:7], a[232:235], v[88:91], a[4:7]\n\t"
    "v_cvt_pk_bf16_f32 v34, 0, v34\n\t"
    "v_cvt_pk_bf16_f32 v35, 0, v35\n\t"
    "v_fma_f32 v32, v32, s58, v192\n\t"
    "v_mfma_f32_16x16x32_bf16 a[4:7], a[236:239], v[92:95], a[4:7]\n\t"
    "v_fma_f32 v33, v33, s58, v192\n\t"
    "v_fma_f32 v34, v34, s58, v192\n\t"
    "v_mfma_f32_16x16x32_bf16 a[8:11], a[224:227], v[96:99], a[8:11]\n\t"
    "v_fma_f32 v35, v35, s58, v192\n\t"
    "v_exp_f32 v32, v32\n\t"
    "v_mfma_f32_16x16x32_bf16 a[8:11], a[228:231], v[100:103], a[8:11]\n\t"
    "ds_read_b64_tr_b16 a[248:249], %[vb] offset:16640\n\t"
    "ds_read_b64_tr_b16 a[250:251], %[vb] offset:20736\n\t"
    "v_mfma_f32_16x16x32_bf16 a[8:11], a[232:235], v[104:107], a[8:11]\n\t"
    "v_exp_f32 v33, v33\n\t"
    "v_exp_f32 v34, v34\n\t"
    "v_mfma_f32_16x16x32_bf16 a[8:11], a[236:239], v[108:111], a[8:11]\n\t"
    "buffer_load_dwordx4 v244, %[srdK], s45 offen offset:3072 lds\n\t"
    "v_mfma_f32_16x16x32_bf16 a[12:15], a[224:227], v[112:115], a[12:15]\n\t"
    "v_exp_f32 v35, v35\n\t"
    "v_mfma_f32_16x16x32_bf16 a[12:15], a[228:231], v[116:119], a[12:15]\n\t"
    "ds_read_b64_tr_b16 a[252:253], %[vb] offset:24832\n\t"
    "ds_read_b64_tr_b16 a[254:255], %[vb] offset:28928\n\t"
    "v_exp_f32 v40, v40\n\t"
    "v_mfma_f32_16x16x32_bf16 a[12:15], a[232:235], v[120:123], a[12:15]\n\t"
    "v_exp_f32 v41, v41\n\t"
    "v_mfma_f32_16x16x32_bf16 a[12:15], a[236:239], v[124:127], a[12:15]\n\t"
    "v_exp_f32 v42, v42\n\t"
    "v_mfma_f32_16x16x32_bf16 v[208:211], v[224:227], v[64:67], v[208:211]\n\t"
    "v_exp_f32 v43, v43\n\t"
    "v_cvt_pk_bf16_f32 v128, v32, v33\n\t"
    "v_mfma_f32_16x16x32_bf16 v[208:211], v[224:227], v[68:71], v[208:211]\n\t"
    "v_cvt_pk_bf16_f32 v129, v34, v35\n\t"
    "v_cvt_pk_bf16_f32 v160, v40, v41\n\t"
    "v_cvt_pk_bf16_f32 v161, v42, v43\n\t"
    "s_waitcnt lgkmcnt(8)\n\t"
    "v_mfma_f32_16x16x32_bf16 v[48:51], a[208:211], a[128:131], 0\n\t"
    "v_cvt_pk_bf16_f32 v36, 0, v36\n\t"
    "v_cvt_pk_bf16_f32 v37, 0, v37\n\t"
    "v_cvt_pk_bf16_f32 v38, 0, v38\n\t"
    "v_mfma_f32_16x16x32_bf16 v[48:51], a[212:215], a[132:135], v[48:51]\n\t"
    "ds_read_b128 a[192:195], v228 offset:40960\n\t"
    "v_cvt_pk_bf16_f32 v39, 0, v39\n\t"
    "v_mul_f32 v36, s56, v36\n\t"
    "v_mfma_f32_16x16x32_bf16 v[48:51], a[216:219], a[136:139], v[48:51]\n\t"
    "v_mul_f32 v37, s56, v37\n\t"
    "v_mul_f32 v38, s56, v38\n\t"
    "v_mul_f32 v39, s56, v39\n\t"
    "v_mfma_f32_16x16x32_bf16 v[48:51], a[220:223], a[140:143], v[48:51]\n\t"
    "s_add_u32 s40, s40, 0x8000\n\t"
    "s_xor_b32 s43, s43, 0x8000\n\t"
    "s_add_u32 m0, s43, 0\n\t"
    "s_add_u32 s46, s41, 0\n\t"
    "buffer_load_dwordx4 %[vsrc0], %[srdV], s46 offen offset:0 lds\n\t"
    "v_mfma_f32_16x16x32_bf16 v[52:55], a[208:211], a[144:147], 0\n\t"
    "v_mfma_f32_16x16x32_bf16 v[52:55], a[212:215], a[148:151], v[52:55]\n\t"
    "ds_read_b128 a[196:199], v229 offset:40960\n\t"
    "v_cvt_pk_bf16_f32 v36, 0, v36\n\t"
    "v_cvt_pk_bf16_f32 v37, 0, v37\n\t"
    "v_mfma_f32_16x16x32_bf16 v[52:55], a[216:219], a[152:155], v[52:55]\n\t"
    "v_cvt_pk_bf16_f32 v38, 0, v38\n\t"
    "v_cvt_pk_bf16_f32 v39, 0, v39\n\t"
    "v_fma_f32 v36, v36, s58, v193\n\t"
    "v_mfma_f32_16x16x32_bf16 v[52:55], a[220:223], a[156:159], v[52:55]\n\t"
    "v_fma_f32 v37, v37, s58, v193\n\t"
    "v_fma_f32 v38, v38, s58, v193\n\t"
    "v_fma_f32 v39, v39, s58, v193\n\t"
    "v_mfma_f32_16x16x32_bf16 v[56:59], a[208:211], a[160:163], v[196:199]\n\t"
    "v_exp_f32 v36, v36\n\t"
    "v_exp_f32 v37, v37\n\t"
    "v_mfma_f32_16x16x32_bf16 v[56:59], a[212:215], a[164:167], v[56:59]\n\t"
    "ds_read_b128 a[200:203], v230 offset:40960\n\t"
    "v_exp_f32 v38, v38\n\t"
    "v_mfma_f32_16x16x32_bf16 v[56:59], a[216:219], a[168:171], v[56:59]\n\t"
    "v_exp_f32 v39, v39\n\t"
    "v_mfma_f32_16x16x32_bf16 v[56:59], a[220:223], a[172:175], v[56:59]\n\t"
    "buffer_load_dwordx4 v245, %[srdV], s46 offen offset:1024 lds\n\t"
    "v_exp_f32 v44, v44\n\t"
    "v_mfma_f32_16x16x32_bf16 v[60:63], a[208:211], a[176:179], v[200:203]\n\t"
    "v_exp_f32 v45, v45\n\t"
    "v_mfma_f32_16x16x32_bf16 v[60:63], a[212:215], a[180:183], v[60:63]\n\t"
    "ds_read_b128 a[204:207], v231 offset:40960\n\t"
    "v_exp_f32 v46, v46\n\t"
    "v_mfma_f32_16x16x32_bf16 v[60:63], a[216:219], a[184:187], v[60:63]\n\t"
    "v_exp_f32 v47, v47\n\t"
    "v_cvt_pk_bf16_f32 v144, v36, v37\n\t"
    "v_mfma_f32_16x16x32_bf16 v[60:63], a[220:223], a[188:191], v[60:63]\n\t"
    "v_cvt_pk_bf16_f32 v145, v38, v39\n\t"
    "v_cvt_pk_bf16_f32 v176, v44, v45\n\t"
    "v_cvt_pk_bf16_f32 v177, v46, v47\n\t"
    "s_waitcnt lgkmcnt(4)\n\t"
    "v_mfma_f32_16x16x32_bf16 a[16:19], a[240:243], v[64:67], a[16:19]\n\t"
    "v_cvt_pk_bf16_f32 v48, 0, v48\n\t"
    "v_cvt_pk_bf16_f32 v49, 0, v49\n\t"
    "v_cvt_pk_bf16_f32 v50, 0, v50\n\t"
    "v_mfma_f32_16x16x32_bf16 a[16:19], a[244:247], v[68:71], a[16:19]\n\t"
    "ds_read_b64_tr_b16 a[224:225], %[vb] offset:512\n\t"
    "ds_read_b64_tr_b16 a[226:227], %[vb] offset:4608\n\t"
    "v_mfma_f32_16x16x32_bf16 a[16:19], a[248:251], v[72:75], a[16:19]\n\t"
    "v_cvt_pk_bf16_f32 v51, 0, v51\n\t"
    "v_mul_f32 v48, s56, v48\n\t"
    "v_mul_f32 v49, s56, v49\n\t"
    "v_mfma_f32_16x16x32_bf16 a[16:19], a[252:255], v[76:79], a[16:19]\n\t"
    "buffer_load_dwordx4 %[vsrc0], %[srdV], s46 offen offset:2048 lds\n\t"
    "v_mul_f32 v50, s56, v50\n\t"
    "v_mfma_f32_16x16x32_bf16 a[20:23], a[240:243], v[80:83], a[20:23]\n\t"
    "v_mul_f32 v51, s56, v51\n\t"
    "v_cvt_pk_bf16_f32 v48, 0, v48\n\t"
    "v_mfma_f32_16x16x32_bf16 a[20:23], a[244:247], v[84:87], a[20:23]\n\t"
    "ds_read_b64_tr_b16 a[228:229], %[vb] offset:8704\n\t"
    "ds_read_b64_tr_b16 a[230:231], %[vb] offset:12800\n\t"
    "v_cvt_pk_bf16_f32 v49, 0, v49\n\t"
    "v_mfma_f32_16x16x32_bf16 a[20:23], a[248:251], v[88:91], a[20:23]\n\t"
    "v_cvt_pk_bf16_f32 v50, 0, v50\n\t"
    "v_cvt_pk_bf16_f32 v51, 0, v51\n\t"
    "v_fma_f32 v48, v48, s58, v192\n\t"
    "v_mfma_f32_16x16x32_bf16 a[20:23], a[252:255], v[92:95], a[20:23]\n\t"
    "v_fma_f32 v49, v49, s58, v192\n\t"
    "v_fma_f32 v50, v50, s58, v192\n\t"
    "v_mfma_f32_16x16x32_bf16 a[24:27], a[240:243], v[96:99], a[24:27]\n\t"
    "v_fma_f32 v51, v51, s58, v192\n\t"
    "v_exp_f32 v48, v48\n\t"
    "v_mfma_f32_16x16x32_bf16 a[24:27], a[244:247], v[100:103], a[24:27]\n\t"
    "ds_read_b64_tr_b16 a[232:233], %[vb] offset:16896\n\t"
    "ds_read_b64_tr_b16 a[234:235], %[vb] offset:20992\n\t"
    "v_mfma_f32_16x16x32_bf16 a[24:27], a[248:251], v[104:107], a[24:27]\n\t"
    "v_exp_f32 v49, v49\n\t"
    "v_exp_f32 v50, v50\n\t"
    "v_mfma_f32_16x16x32_bf16 a[24:27], a[252:255], v[108:111], a[24:27]\n\t"
    "buffer_load_dwordx4 v245, %[srdV], s46 offen offset:3072 lds\n\t"
    "v_mfma_f32_16x16x32_bf16 a[28:31], a[240:243], v[112:115], a[28:31]\n\t"
    "v_exp_f32 v51, v51\n\t"
    "v_mfma_f32_16x16x32_bf16 a[28:31], a[244:247], v[116:119], a[28:31]\n\t"
    "ds_read_b64_tr_b16 a[236:237], %[vb] offset:25088\n\t"
    "ds_read_b64_tr_b16 a[238:239], %[vb] offset:29184\n\t"
    "v_exp_f32 v56, v56\n\t"
    "v_mfma_f32_16x16x32_bf16 a[28:31], a[248:251], v[120:123], a[28:31]\n\t"
    "v_exp_f32 v57, v57\n\t"
    "v_mfma_f32_16x16x32_bf16 a[28:31], a[252:255], v[124:127], a[28:31]\n\t"
    "v_exp_f32 v58, v58\n\t"
    "v_mfma_f32_16x16x32_bf16 v[208:211], v[224:227], v[72:75], v[208:211]\n\t"
    "v_exp_f32 v59, v59\n\t"
    "v_cvt_pk_bf16_f32 v130, v48, v49\n\t"
    "v_mfma_f32_16x16x32_bf16 v[208:211], v[224:227], v[76:79], v[208:211]\n\t"
    "v_cvt_pk_bf16_f32 v131, v50, v51\n\t"
    "v_cvt_pk_bf16_f32 v162, v56, v57\n\t"
    "v_cvt_pk_bf16_f32 v163, v58, v59\n\t"
    "s_waitcnt lgkmcnt(8)\n\t"
    "v_mfma_f32_16x16x32_bf16 v[32:35], a[192:195], a[128:131], 0\n\t"
    "v_cvt_pk_bf16_f32 v52, 0, v52\n\t"
    "v_cvt_pk_bf16_f32 v53, 0, v53\n\t"
    "v_cvt_pk_bf16_f32 v54, 0, v54\n\t"
    "v_mfma_f32_16x16x32_bf16 v[32:35], a[196:199], a[132:135], v[32:35]\n\t"
    "ds_read_b128 a[208:211], v228 offset:45056\n\t"
    "v_cvt_pk_bf16_f32 v55, 0, v55\n\t"
    "v_mul_f32 v52, s56, v52\n\t"
    "v_mfma_f32_16x16x32_bf16 v[32:35], a[200:203], a[136:139], v[32:35]\n\t"
    "v_mul_f32 v53, s56, v53\n\t"
    "v_mul_f32 v54, s56, v54\n\t"
    "v_mul_f32 v55, s56, v55\n\t"
    "v_mfma_f32_16x16x32_bf16 v[32:35], a[204:207], a[140:143], v[32:35]\n\t"
    "s_add_u32 m0, s43, 4096\n\t"
    "s_add_u32 s46, s41, 4096\n\t"
    "buffer_load_dwordx4 %[vsrc0], %[srdV], s46 offen offset:0 lds\n\t"
    "v_mfma_f32_16x16x32_bf16 v[36:39], a[192:195], a[144:147], 0\n\t"
    "v_cvt_pk_bf16_f32 v52, 0, v52\n\t"
    "v_mfma_f32_16x16x32_bf16 v[36:39], a[196:199], a[148:151], v[36:39]\n\t"
    "ds_read_b128 a[212:215], v229 offset:45056\n\t"
    "v_cvt_pk_bf16_f32 v53, 0, v53\n\t"
    "v_cvt_pk_bf16_f32 v54, 0, v54\n\t"
    "v_mfma_f32_16x16x32_bf16 v[36:39], a[200:203], a[152:155], v[36:39]\n\t"
    "v_cvt_pk_bf16_f32 v55, 0, v55\n\t"
    "v_fma_f32 v52, v52, s58, v193\n\t"
    "v_fma_f32 v53, v53, s58, v193\n\t"
    "v_mfma_f32_16x16x32_bf16 v[36:39], a[204:207], a[156:159], v[36:39]\n\t"
    "v_fma_f32 v54, v54, s58, v193\n\t"
    "v_fma_f32 v55, v55, s58, v193\n\t"
    "v_exp_f32 v52, v52\n\t"
    "v_mfma_f32_16x16x32_bf16 v[40:43], a[192:195], a[160:163], v[196:199]\n\t"
    "v_exp_f32 v53, v53\n\t"
    "v_mfma_f32_16x16x32_bf16 v[40:43], a[196:199], a[164:167], v[40:43]\n\t"
    "ds_read_b128 a[216:219], v230 offset:45056\n\t"
    "v_exp_f32 v54, v54\n\t"
    "v_mfma_f32_16x16x32_bf16 v[40:43], a[200:203], a[168:171], v[40:43]\n\t"
    "v_exp_f32 v55, v55\n\t"
    "v_mfma_f32_16x16x32_bf16 v[40:43], a[204:207], a[172:175], v[40:43]\n\t"
    "buffer_load_dwordx4 v245, %[srdV], s46 offen offset:1024 lds\n\t"
    "v_exp_f32 v60, v60\n\t"
    "v_mfma_f32_16x16x32_bf16 v[44:47], a[192:195], a[176:179], v[200:203]\n\t"
    "v_exp_f32 v61, v61\n\t"
    "v_mfma_f32_16x16x32_bf16 v[44:47], a[196:199], a[180:183], v[44:47]\n\t"
    "ds_read_b128 a[220:223], v231 offset:45056\n\t"
    "v_exp_f32 v62, v62\n\t"
    "v_mfma_f32_16x16x32_bf16 v[44:47], a[200:203], a[184:187], v[44:47]\n\t"
    "v_exp_f32 v63, v63\n\t"
    "v_cvt_pk_bf16_f32 v146, v52, v53\n\t"
    "v_mfma_f32_16x16x32_bf16 v[44:47], a[204:207], a[188:191], v[44:47]\n\t"
    "v_cvt_pk_bf16_f32 v147, v54, v55\n\t"
    "v_cvt_pk_bf16_f32 v178, v60, v61\n\t"
    "v_cvt_pk_bf16_f32 v179, v62, v63\n\t"
    "s_waitcnt lgkmcnt(4)\n\t"
    "v_mfma_f32_16x16x32_bf16 a[32:35], a[224:227], v[64:67], a[32:35]\n\t"
    "v_cvt_pk_bf16_f32 v32, 0, v32\n\t"
    "v_cvt_pk_bf16_f32 v33, 0, v33\n\t"
    "v_cvt_pk_bf16_f32 v34, 0, v34\n\t"
    "v_mfma_f32_16x16x32_bf16 a[32:35], a[228:231], v[68:71], a[32:35]\n\t"
    "ds_read_b64_tr_b16 a[240:241], %[vb] offset:768\n\t"
    "ds_read_b64_tr_b16 a[242:243], %[vb] offset:4864\n\t"
    "v_mfma_f32_16x16x32_bf16 a[32:35], a[232:235], v[72:75], a[32:35]\n\t"
    "v_cvt_pk_bf16_f32 v35, 0, v35\n\t"
    "v_mul_f32 v32, s56, v32\n\t"
    "v_mul_f32 v33, s56, v33\n\t"
    "v_mfma_f32_16x16x32_bf16 a[32:35], a[236:239], v[76:79], a[32:35]\n\t"
    "buffer_load_dwordx4 %[vsrc0], %[srdV], s46 offen offset:2048 lds\n\t"
    "v_mul_f32 v34, s56, v34\n\t"
    "v_mfma_f32_16x16x32_bf16 a[36:39], a[224:227], v[80:83], a[36:39]\n\t"
    "v_mul_f32 v35, s56, v35\n\t"
    "v_cvt_pk_bf16_f32 v32, 0, v32\n\t"
    "v_mfma_f32_16x16x32_bf16 a[36:39], a[228:231], v[84:87], a[36:39]\n\t"
    "ds_read_b64_tr_b16 a[244:245], %[vb] offset:8960\n\t"
    "ds_read_b64_tr_b16 a[246:247], %[vb] offset:13056\n\t"
    "v_cvt_pk_bf16_f32 v33, 0, v33\n\t"
    "v_mfma_f32_16x16x32_bf16 a[36:39], a[232:235], v[88:91], a[36:39]\n\t"
    "v_cvt_pk_bf16_f32 v34, 0, v34\n\t"
    "v_cvt_pk_bf16_f32 v35, 0, v35\n\t"
    "v_fma_f32 v32, v32, s58, v192\n\t"
    "v_mfma_f32_16x16x32_bf16 a[36:39], a[236:239], v[92:95], a[36:39]\n\t"
    "v_fma_f32 v33, v33, s58, v192\n\t"
    "v_fma_f32 v34, v34, s58, v192\n\t"
    "v_mfma_f32_16x16x32_bf16 a[40:43], a[224:227], v[96:99], a[40:43]\n\t"
    "v_fma_f32 v35, v35, s58, v192\n\t"
    "v_exp_f32 v32, v32\n\t"
    "v_mfma_f32_16x16x32_bf16 a[40:43], a[228:231], v[100:103], a[40:43]\n\t"
    "ds_read_b64_tr_b16 a[248:249], %[vb] offset:17152\n\t"
    "ds_read_b64_tr_b16 a[250:251], %[vb] offset:21248\n\t"
    "v_mfma_f32_16x16x32_bf16 a[40:43], a[232:235], v[104:107], a[40:43]\n\t"
    "v_exp_f32 v33, v33\n\t"
    "v_exp_f32 v34, v34\n\t"
    "v_mfma_f32_16x16x32_bf16 a[40:43], a[236:239], v[108:111], a[40:43]\n\t"
    "buffer_load_dwordx4 v245, %[srdV], s46 offen offset:3072 lds\n\t"
    "v_mfma_f32_16x16x32_bf16 a[44:47], a[224:227], v[112:115], a[44:47]\n\t"
    "v_exp_f32 v35, v35\n\t"
    "v_mfma_f32_16x16x32_bf16 a[44:47], a[228:231], v[116:119], a[44:47]\n\t"
    "ds_read_b64_tr_b16 a[252:253], %[vb] offset:25344\n\t"
    "ds_read_b64_tr_b16 a[254:255], %[vb] offset:29440\n\t"
    "v_exp_f32 v40, v40\n\t"
    "v_mfma_f32_16x16x32_bf16 a[44:47], a[232:235], v[120:123], a[44:47]\n\t"
    "v_exp_f32 v41, v41\n\t"
    "v_mfma_f32_16x16x32_bf16 a[44:47], a[236:239], v[124:127], a[44:47]\n\t"
    "v_exp_f32 v42, v42\n\t"
    "v_mfma_f32_16x16x32_bf16 v[212:215], v[224:227], v[80:83], v[212:215]\n\t"
    "v_exp_f32 v43, v43\n\t"
    "v_cvt_pk_bf16_f32 v132, v32, v33\n\t"
    "v_mfma_f32_16x16x32_bf16 v[212:215], v[224:227], v[84:87], v[212:215]\n\t"
    "v_cvt_pk_bf16_f32 v133, v34, v35\n\t"
    "v_cvt_pk_bf16_f32 v164, v40, v41\n\t"
    "v_cvt_pk_bf16_f32 v165, v42, v43\n\t"
    "s_waitcnt lgkmcnt(8)\n\t"
    "v_mfma_f32_16x16x32_bf16 v[48:51], a[208:211], a[128:131], 0\n\t"
    "v_cvt_pk_bf16_f32 v36, 0, v36\n\t"
    "v_cvt_pk_bf16_f32 v37, 0, v37\n\t"
    "v_cvt_pk_bf16_f32 v38, 0, v38\n\t"
    "v_mfma_f32_16x16x32_bf16 v[48:51], a[212:215], a[132:135], v[48:51]\n\t"
    "ds_read_b128 a[192:195], v228 offset:49152\n\t"
    "v_cvt_pk_bf16_f32 v39, 0, v39\n\t"
    "v_mul_f32 v36, s56, v36\n\t"
    "v_mfma_f32_16x16x32_bf16 v[48:51], a[216:219], a[136:139], v[48:51]\n\t"
    "v_mul_f32 v37, s56, v37\n\t"
    "v_mul_f32 v38, s56, v38\n\t"
    "v_mul_f32 v39, s56, v39\n\t"
    "v_mfma_f32_16x16x32_bf16 v[48:51], a[220:223], a[140:143], v[48:51]\n\t"
    "s_add_u32 s41, s41, 0x8000\n\t"
    "s_cmp_gt_u32 s44, 5\n\t"
    "s_cselect_b32 s47, 0x7fffff00, s51\n\t"
    "s_cselect_b32 s48, 0, %[pfstep]\n\t"
    "buffer_load_dword v246, %[pfv], %[srdPF], s47 offen\n\t"
    "v_mfma_f32_16x16x32_bf16 v[52:55], a[208:211], a[144:147], 0\n\t"
    "v_mfma_f32_16x16x32_bf16 v[52:55], a[212:215], a[148:151], v[52:55]\n\t"
    "ds_read_b128 a[196:199], v229 offset:49152\n\t"
    "v_cvt_pk_bf16_f32 v36, 0, v36\n\t"
    "v_cvt_pk_bf16_f32 v37, 0, v37\n\t"
    "v_mfma_f32_16x16x32_bf16 v[52:55], a[216:219], a[152:155], v[52:55]\n\t"
    "v_cvt_pk_bf16_f32 v38, 0, v38\n\t"
    "v_cvt_pk_bf16_f32 v39, 0, v39\n\t"
    "v_fma_f32 v36, v36, s58, v193\n\t"
    "v_mfma_f32_16x16x32_bf16 v[52:55], a[220:223], a[156:159], v[52:55]\n\t"
    "v_fma_f32 v37, v37, s58, v193\n\t"
    "v_fma_f32 v38, v38, s58, v193\n\t"
    "v_fma_f32 v39, v39, s58, v193\n\t"
    "v_mfma_f32_16x16x32_bf16 v[56:59], a[208:211], a[160:163], v[196:199]\n\t"
    "v_exp_f32 v36, v36\n\t"
    "v_mfma_f32_16x16x32_bf16 v[56:59], a[212:215], a[164:167], v[56:59]\n\t"
    "ds_read_b128 a[200:203], v230 offset:49152\n\t"
    "v_exp_f32 v37, v37\n\t"
    "v_mfma_f32_16x16x32_bf16 v[56:59], a[216:219], a[168:171], v[56:59]\n\t"
    "v_exp_f32 v38, v38\n\t"
    "v_exp_f32 v39, v39\n\t"
    "v_mfma_f32_16x16x32_bf16 v[56:59], a[220:223], a[172:175], v[56:59]\n\t"
    "s_add_u32 s51, s51, s48\n\t"
    "v_exp_f32 v44, v44\n\t"
    "v_mfma_f32_16x16x32_bf16 v[60:63], a[208:211], a[176:179], v[200:203]\n\t"
    "v_exp_f32 v45, v45\n\t"
    "v_mfma_f32_16x16x32_bf16 v[60:63], a[212:215], a[180:183], v[60:63]\n\t"
    "ds_read_b128 a[204:207], v231 offset:49152\n\t"
    "v_exp_f32 v46, v46\n\t"
    "v_mfma_f32_16x16x32_bf16 v[60:63], a[216:219], a[184:187], v[60:63]\n\t"
    "v_exp_f32 v47, v47\n\t"
    "v_cvt_pk_bf16_f32 v148, v36, v37\n\t"
    "v_mfma_f32_16x16x32_bf16 v[60:63], a[220:223], a[188:191], v[60:63]\n\t"
    "v_cvt_pk_bf16_f32 v149, v38, v39\n\t"
    "v_cvt_pk_bf16_f32 v180, v44, v45\n\t"
    "v_cvt_pk_bf16_f32 v181, v46, v47\n\t"
    "s_waitcnt lgkmcnt(4)\n\t"
    "v_mfma_f32_16x16x32_bf16 a[48:51], a[240:243], v[64:67], a[48:51]\n\t"
    "v_cvt_pk_bf16_f32 v48, 0, v48\n\t"
    "v_cvt_pk_bf16_f32 v49, 0, v49\n\t"
    "v_mfma_f32_16x16x32_bf16 a[48:51], a[244:247], v[68:71], a[48:51]\n\t"
    "ds_read_b64_tr_b16 a[224:225], %[vb] offset:1024\n\t"
    "ds_read_b64_tr_b16 a[226:227], %[vb] offset:5120\n\t"
    "v_cvt_pk_bf16_f32 v50, 0, v50\n\t"
    "v_mfma_f32_16x16x32_bf16 a[48:51], a[248:251], v[72:75], a[48:51]\n\t"
    "v_cvt_pk_bf16_f32 v51, 0, v51\n\t"
    "v_mul_f32 v48, s56, v48\n\t"
    "v_mfma_f32_16x16x32_bf16 a[48:51], a[252:255], v[76:79], a[48:51]\n\t"
    "v_mul_f32 v49, s56, v49\n\t"
    "v_mul_f32 v50, s56, v50\n\t"
    "v_mul_f32 v51, s56, v51\n\t"
    "v_mfma_f32_16x16x32_bf16 a[52:55], a[240:243], v[80:83], a[52:55]\n\t"
    "v_cvt_pk_bf16_f32 v48, 0, v48\n\t"
    "v_cvt_pk_bf16_f32 v49, 0, v49\n\t"
    "v_mfma_f32_16x16x32_bf16 a[52:55], a[244:247], v[84:87], a[52:55]\n\t"
    "ds_read_b64_tr_b16 a[228:229], %[vb] offset:9216\n\t"
    "ds_read_b64_tr_b16 a[230:231], %[vb] offset:13312\n\t"
    "v_cvt_pk_bf16_f32 v50, 0, v50\n\t"
    "v_mfma_f32_16x16x32_bf16 a[52:55], a[248:251], v[88:91], a[52:55]\n\t"
    "v_cvt_pk_bf16_f32 v51, 0, v51\n\t"
    "v_fma_f32 v48, v48, s58, v192\n\t"
    "v_mfma_f32_16x16x32_bf16 a[52:55], a[252:255], v[92:95], a[52:55]\n\t"
    "v_fma_f32 v49, v49, s58, v192\n\t"
    "v_fma_f32 v50, v50, s58, v192\n\t"
    "v_fma_f32 v51, v51, s58, v192\n\t"
    "v_mfma_f32_16x16x32_bf16 a[56:59], a[240:243], v[96:99], a[56:59]\n\t"
    "v_exp_f32 v48, v48\n\t"
    "v_mfma_f32_16x16x32_bf16 a[56:59], a[244:247], v[100:103], a[56:59]\n\t"
    "ds_read_b64_tr_b16 a[232:233], %[vb] offset:17408\n\t"
    "ds_read_b64_tr_b16 a[234:235], %[vb] offset:21504\n\t"
    "v_mfma_f32_16x16x32_bf16 a[56:59], a[248:251], v[104:107], a[56:59]\n\t"
    "v_exp_f32 v49, v49\n\t"
    "v_mfma_f32_16x16x32_bf16 a[56:59], a[252:255], v[108:111], a[56:59]\n\t"
    "v_exp_f32 v50, v50\n\t"
    "v_exp_f32 v51, v51\n\t"
    "v_mfma_f32_16x16x32_bf16 a[60:63], a[240:243], v[112:115], a[60:63]\n\t"
    "v_exp_f32 v56, v56\n\t"
    "v_mfma_f32_16x16x32_bf16 a[60:63], a[244:247], v[116:119], a[60:63]\n\t"
    "ds_read_b64_tr_b16 a[236:237], %[vb] offset:25600\n\t"
    "ds_read_b64_tr_b16 a[238:239], %[vb] offset:29696\n\t"
    "v_mfma_f32_16x16x32_bf16 a[60:63], a[248:251], v[120:123], a[60:63]\n\t"
    "v_exp_f32 v57, v57\n\t"
    "v_mfma_f32_16x16x32_bf16 a[60:63], a[252:255], v[124:127], a[60:63]\n\t"
    "v_exp_f32 v58, v58\n\t"
    "v_exp_f32 v59, v59\n\t"
    "v_mfma_f32_16x16x32_bf16 v[212:215], v[224:227], v[88:91], v[212:215]\n\t"
    "v_cvt_pk_bf16_f32 v134, v48, v49\n\t"
    "v_cvt_pk_bf16_f32 v135, v50, v51\n\t"
    "v_mfma_f32_16x16x32_bf16 v[212:215], v[224:227], v[92:95], v[212:215]\n\t"
    "v_cvt_pk_bf16_f32 v166, v56, v57\n\t"
    "v_cvt_pk_bf16_f32 v167, v58, v59\n\t"
    "s_waitcnt lgkmcnt(8)\n\t"
    "v_mfma_f32_16x16x32_bf16 v[32:35], a[192:195], a[128:131], 0\n\t"
    "v_cvt_pk_bf16_f32 v52, 0, v52\n\t"
    "v_cvt_pk_bf16_f32 v53, 0, v53\n\t"
    "v_cvt_pk_bf16_f32 v54, 0, v54\n\t"
    "v_mfma_f32_16x16x32_bf16 v[32:35], a[196:199], a[132:135], v[32:35]\n\t"
    "ds_read_b128 a[208:211], v228 offset:53248\n\t"
    "v_cvt_pk_bf16_f32 v55, 0, v55\n\t"
    "v_mfma_f32_16x16x32_bf16 v[32:35], a[200:203], a[136:139], v[32:35]\n\t"
    "v_mul_f32 v52, s56, v52\n\t"
    "v_mul_f32 v53, s56, v53\n\t"
    "v_mul_f32 v54, s56, v54\n\t"
    "v_mfma_f32_16x16x32_bf16 v[32:35], a[204:207], a[140:143], v[32:35]\n\t"
    "v_mul_f32 v55, s56, v55\n\t"
    "v_cvt_pk_bf16_f32 v52, 0, v52\n\t"
    "v_mfma_f32_16x16x32_bf16 v[36:39], a[192:195], a[144:147], 0\n\t"
    "v_cvt_pk_bf16_f32 v53, 0, v53\n\t"
    "v_cvt_pk_bf16_f32 v54, 0, v54\n\t"
    "v_cvt_pk_bf16_f32 v55, 0, v55\n\t"
    "v_mfma_f32_16x16x32_bf16 v[36:39], a[196:199], a[148:151], v[36:39]\n\t"
    "ds_read_b128 a[212:215], v229 offset:53248\n\t"
    "v_fma_f32 v52, v52, s58, v193\n\t"
    "v_mfma_f32_16x16x32_bf16 v[36:39], a[200:203], a[152:155], v[36:39]\n\t"
    "v_fma_f32 v53, v53, s58, v193\n\t"
    "v_fma_f32 v54, v54, s58, v193\n\t"
    "v_fma_f32 v55, v55, s58, v193\n\t"
    "v_mfma_f32_16x16x32_bf16 v[36:39], a[204:207], a[156:159], v[36:39]\n\t"
    "v_exp_f32 v52, v52\n\t"
    "v_mfma_f32_16x16x32_bf16 v[40:43], a[192:195], a[160:163], v[196:199]\n\t"
    "v_exp_f32 v53, v53\n\t"
    "v_mfma_f32_16x16x32_bf16 v[40:43], a[196:199], a[164:167], v[40:43]\n\t"
    "ds_read_b128 a[216:219], v230 offset:53248\n\t"
    "v_exp_f32 v54, v54\n\t"
    "v_mfma_f32_16x16x32_bf16 v[40:43], a[200:203], a[168:171], v[40:43]\n\t"
    "v_exp_f32 v55, v55\n\t"
    "v_mfma_f32_16x16x32_bf16 v[40:43], a[204:207], a[172:175], v[40:43]\n\t"
    "v_exp_f32 v60, v60\n\t"
    "v_exp_f32 v61, v61\n\t"
    "v_mfma_f32_16x16x32_bf16 v[44:47], a[192:195], a[176:179], v[200:203]\n\t"
    "v_exp_f32 v62, v62\n\t"
    "v_mfma_f32_16x16x32_bf16 v[44:47], a[196:199], a[180:183], v[44:47]\n\t"
    "ds_read_b128 a[220:223], v231 offset:53248\n\t"
    "v_exp_f32 v63, v63\n\t"
    "v_mfma_f32_16x16x32_bf16 v[44:47], a[200:203], a[184:187], v[44:47]\n\t"
    "v_cvt_pk_bf16_f32 v150, v52, v53\n\t"
    "v_cvt_pk_bf16_f32 v151, v54, v55\n\t"
    "v_mfma_f32_16x16x32_bf16 v[44:47], a[204:207], a[188:191], v[44:47]\n\t"
    "v_cvt_pk_bf16_f32 v182, v60, v61\n\t"
    "v_cvt_pk_bf16_f32 v183, v62, v63\n\t"
    "s_waitcnt lgkmcnt(4)\n\t"
    "v_mfma_f32_16x16x32_bf16 a[64:67], a[224:227], v[64:67], a[64:67]\n\t"
    "v_cvt_pk_bf16_f32 v32, 0, v32\n\t"
    "v_cvt_pk_bf16_f32 v33, 0, v33\n\t"
    "v_mfma_f32_16x16x32_bf16 a[64:67], a[228:231], v[68:71], a[64:67]\n\t"
    "ds_read_b64_tr_b16 a[240:241], %[vb] offset:1280\n\t"
    "ds_read_b64_tr_b16 a[242:243], %[vb] offset:5376\n\t"
    "v_cvt_pk_bf16_f32 v34, 0, v34\n\t"
    "v_mfma_f32_16x16x32_bf16 a[64:67], a[232:235], v[72:75], a[64:67]\n\t"
    "v_cvt_pk_bf16_f32 v35, 0, v35\n\t"
    "v_mul_f32 v32, s56, v32\n\t"
    "v_mfma_f32_16x16x32_bf16 a[64:67], a[236:239], v[76:79], a[64:67]\n\t"
    "v_mul_f32 v33, s56, v33\n\t"
    "v_mul_f32 v34, s56, v34\n\t"
    "v_mul_f32 v35, s56, v35\n\t"
    "v_mfma_f32_16x16x32_bf16 a[68:71], a[224:227], v[80:83], a[68:71]\n\t"
    "v_cvt_pk_bf16_f32 v32, 0, v32\n\t"
    "v_cvt_pk_bf16_f32 v33, 0, v33\n\t"
    "v_mfma_f32_16x16x32_bf16 a[68:71], a[228:231], v[84:87], a[68:71]\n\t"
    "ds_read_b64_tr_b16 a[244:245], %[vb] offset:9472\n\t"
    "ds_read_b64_tr_b16 a[246:247], %[vb] offset:13568\n\t"
    "v_cvt_pk_bf16_f32 v34, 0, v34\n\t"
    "v_mfma_f32_16x16x32_bf16 a[68:71], a[232:235], v[88:91], a[68:71]\n\t"
    "v_cvt_pk_bf16_f32 v35, 0, v35\n\t"
    "v_fma_f32 v32, v32, s58, v192\n\t"
    "v_mfma_f32_16x16x32_bf16 a[68:71], a[236:239], v[92:95], a[68:71]\n\t"
    "v_fma_f32 v33, v33, s58, v192\n\t"
    "v_fma_f32 v34, v34, s58, v192\n\t"
    "v_fma_f32 v35, v35, s58, v192\n\t"
    "v_mfma_f32_16x16x32_bf16 a[72:75], a[224:227], v[96:99], a[72:75]\n\t"
    "v_exp_f32 v32, v32\n\t"
    "v_mfma_f32_16x16x32_bf16 a[72:75], a[228:231], v[100:103], a[72:75]\n\t"
    "ds_read_b64_tr_b16 a[248:249], %[vb] offset:17664\n\t"
    "ds_read_b64_tr_b16 a[250:251], %[vb] offset:21760\n\t"
    "v_mfma_f32_16x16x32_bf16 a[72:75], a[232:235], v[104:107], a[72:75]\n\t"
    "v_exp_f32 v33, v33\n\t"
    "v_mfma_f32_16x16x32_bf16 a[72:75], a[236:239], v[108:111], a[72:75]\n\t"
    "v_exp_f32 v34, v34\n\t"
    "v_exp_f32 v35, v35\n\t"
    "v_mfma_f32_16x16x32_bf16 a[76:79], a[224:227], v[112:115], a[76:79]\n\t"
    "v_exp_f32 v40, v40\n\t"
    "v_mfma_f32_16x16x32_bf16 a[76:79], a[228:231], v[116:119], a[76:79]\n\t"
    "ds_read_b64_tr_b16 a[252:253], %[vb] offset:25856\n\t"
    "ds_read_b64_tr_b16 a[254:255], %[vb] offset:29952\n\t"
    "v_mfma_f32_16x16x32_bf16 a[76:79], a[232:235], v[120:123], a[76:79]\n\t"
    "v_exp_f32 v41, v41\n\t"
    "v_mfma_f32_16x16x32_bf16 a[76:79], a[236:239], v[124:127], a[76:79]\n\t"
    "v_exp_f32 v42, v42\n\t"
    "v_exp_f32 v43, v43\n\t"
    "v_mfma_f32_16x16x32_bf16 v[216:219], v[224:227], v[96:99], v[216:219]\n\t"
    "v_cvt_pk_bf16_f32 v136, v32, v33\n\t"
    "v_cvt_pk_bf16_f32 v137, v34, v35\n\t"
    "v_mfma_f32_16x16x32_bf16 v[216:219], v[224:227], v[100:103], v[216:219]\n\t"
    "v_cvt_pk_bf16_f32 v168, v40, v41\n\t"
    "v_cvt_pk_bf16_f32 v169, v42, v43\n\t"
    "s_waitcnt lgkmcnt(8)\n\t"
    "v_mfma_f32_16x16x32_bf16 v[48:51], a[208:211], a[128:131], 0\n\t"
    "v_cvt_pk_bf16_f32 v36, 0, v36\n\t"
    "v_cvt_pk_bf16_f32 v37, 0, v37\n\t"
    "v_cvt_pk_bf16_f32 v38, 0, v38\n\t"
    "v_mfma_f32_16x16x32_bf16 v[48:51], a[212:215], a[132:135], v[48:51]\n\t"
    "ds_read_b128 a[192:195], v228 offset:57344\n\t"
    "v_cvt_pk_bf16_f32 v39, 0, v39\n\t"
    "v_mfma_f32_16x16x32_bf16 v[48:51], a[216:219], a[136:139], v[48:51]\n\t"
    "v_mul_f32 v36, s56, v36\n\t"
    "v_mul_f32 v37, s56, v37\n\t"
    "v_mul_f32 v38, s56, v38\n\t"
    "v_mfma_f32_16x16x32_bf16 v[48:51], a[220:223], a[140:143], v[48:51]\n\t"
    "v_mul_f32 v39, s56, v39\n\t"
    "v_cvt_pk_bf16_f32 v36, 0, v36\n\t"
    "v_mfma_f32_16x16x32_bf16 v[52:55], a[208:211], a[144:147], 0\n\t"
    "v_cvt_pk_bf16_f32 v37, 0, v37\n\t"
    "v_cvt_pk_bf16_f32 v38, 0, v38\n\t"
    "v_cvt_pk_bf16_f32 v39, 0, v39\n\t"
    "v_mfma_f32_16x16x32_bf16 v[52:55], a[212:215], a[148:151], v[52:55]\n\t"
    "ds_read_b128 a[196:199], v229 offset:57344\n\t"
    "v_fma_f32 v36, v36, s58, v193\n\t"
    "v_mfma_f32_16x16x32_bf16 v[52:55], a[216:219], a[152:155], v[52:55]\n\t"
    "v_fma_f32 v37, v37, s58, v193\n\t"
    "v_fma_f32 v38, v38, s58, v193\n\t"
    "v_fma_f32 v39, v39, s58, v193\n\t"
    "v_mfma_f32_16x16x32_bf16 v[52:55], a[220:223], a[156:159], v[52:55]\n\t"
    "v_exp_f32 v36, v36\n\t"
    "v_mfma_f32_16x16x32_bf16 v[56:59], a[208:211], a[160:163], v[196:199]\n\t"
    "v_exp_f32 v37, v37\n\t"
    "v_mfma_f32_16x16x32_bf16 v[56:59], a[212:215], a[164:167], v[56:59]\n\t"
    "ds_read_b128 a[200:203], v230 offset:57344\n\t"
    "v_exp_f32 v38, v38\n\t"
    "v_mfma_f32_16x16x32_bf16 v[56:59], a[216:219], a[168:171], v[56:59]\n\t"
    "v_exp_f32 v39, v39\n\t"
    "v_mfma_f32_16x16x32_bf16 v[56:59], a[220:223], a[172:175], v[56:59]\n\t"
    "v_exp_f32 v44, v44\n\t"
    "v_exp_f32 v45, v45\n\t"
    "v_mfma_f32_16x16x32_bf16 v[60:63], a[208:211], a[176:179], v[200:203]\n\t"
    "v_exp_f32 v46, v46\n\t"
    "v_mfma_f32_16x16x32_bf16 v[60:63], a[212:215], a[180:183], v[60:63]\n\t"
    "ds_read_b128 a[204:207], v231 offset:57344\n\t"
    "v_exp_f32 v47, v47\n\t"
    "v_mfma_f32_16x16x32_bf16 v[60:63], a[216:219], a[184:187], v[60:63]\n\t"
    "v_cvt_pk_bf16_f32 v152, v36, v37\n\t"
    "v_cvt_pk_bf16_f32 v153, v38, v39\n\t"
    "v_mfma_f32_16x16x32_bf16 v[60:63], a[220:223], a[188:191], v[60:63]\n\t"
    "v_cvt_pk_bf16_f32 v184, v44, v45\n\t"
    "v_cvt_pk_bf16_f32 v185, v46, v47\n\t"
    "s_waitcnt lgkmcnt(4)\n\t"
    "v_mfma_f32_16x16x32_bf16 a[80:83], a[240:243], v[64:67], a[80:83]\n\t"
    "v_cvt_pk_bf16_f32 v48, 0, v48\n\t"
    "v_cvt_pk_bf16_f32 v49, 0, v49\n\t"
    "v_mfma_f32_16x16x32_bf16 a[80:83], a[244:247], v[68:71], a[80:83]\n\t"
    "ds_read_b64_tr_b16 a[224:225], %[vb] offset:1536\n\t"
    "ds_read_b64_tr_b16 a[226:227], %[vb] offset:5632\n\t"
    "v_cvt_pk_bf16_f32 v50, 0, v50\n\t"
    "v_mfma_f32_16x16x32_bf16 a[80:83], a[248:251], v[72:75], a[80:83]\n\t"
    "v_cvt_pk_bf16_f32 v51, 0, v51\n\t"
    "v_mul_f32 v48, s56, v48\n\t"
    "v_mfma_f32_16x16x32_bf16 a[80:83], a[252:255], v[76:79], a[80:83]\n\t"
    "v_mul_f32 v49, s56, v49\n\t"
    "v_mul_f32 v50, s56, v50\n\t"
    "v_mul_f32 v51, s56, v51\n\t"
    "v_mfma_f32_16x16x32_bf16 a[84:87], a[240:243], v[80:83], a[84:87]\n\t"
    "v_cvt_pk_bf16_f32 v48, 0, v48\n\t"
    "v_cvt_pk_bf16_f32 v49, 0, v49\n\t"
    "v_mfma_f32_16x16x32_bf16 a[84:87], a[244:247], v[84:87], a[84:87]\n\t"
    "ds_read_b64_tr_b16 a[228:229], %[vb] offset:9728\n\t"
    "ds_read_b64_tr_b16 a[230:231], %[vb] offset:13824\n\t"
    "v_cvt_pk_bf16_f32 v50, 0, v50\n\t"
    "v_mfma_f32_16x16x32_bf16 a[84:87], a[248:251], v[88:91], a[84:87]\n\t"
    "v_cvt_pk_bf16_f32 v51, 0, v51\n\t"
    "v_fma_f32 v48, v48, s58, v192\n\t"
    "v_mfma_f32_16x16x32_bf16 a[84:87], a[252:255], v[92:95], a[84:87]\n\t"
    "v_fma_f32 v49, v49, s58, v192\n\t"
    "v_fma_f32 v50, v50, s58, v192\n\t"
    "v_fma_f32 v51, v51, s58, v192\n\t"
    "v_mfma_f32_16x16x32_bf16 a[88:91], a[240:243], v[96:99], a[88:91]\n\t"
    "v_exp_f32 v48, v48\n\t"
    "v_mfma_f32_16x16x32_bf16 a[88:91], a[244:247], v[100:103], a[88:91]\n\t"
    "ds_read_b64_tr_b16 a[232:233], %[vb] offset:17920\n\t"
    "ds_read_b64_tr_b16 a[234:235], %[vb] offset:22016\n\t"
    "v_mfma_f32_16x16x32_bf16 a[88:91], a[248:251], v[104:107], a[88:91]\n\t"
    "v_exp_f32 v49, v49\n\t"
    "v_mfma_f32_16x16x32_bf16 a[88:91], a[252:255], v[108:111], a[88:91]\n\t"
    "v_exp_f32 v50, v50\n\t"
    "v_exp_f32 v51, v51\n\t"
    "v_mfma_f32_16x16x32_bf16 a[92:95], a[240:243], v[112:115], a[92:95]\n\t"
    "v_exp_f32 v56, v56\n\t"
    "v_mfma_f32_16x16x32_bf16 a[92:95], a[244:247], v[116:119], a[92:95]\n\t"
    "ds_read_b64_tr_b16 a[236:237], %[vb] offset:26112\n\t"
    "ds_read_b64_tr_b16 a[238:239], %[vb] offset:30208\n\t"
    "v_mfma_f32_16x16x32_bf16 a[92:95], a[248:251], v[120:123], a[92:95]\n\t"
    "v_exp_f32 v57, v57\n\t"
    "v_mfma_f32_16x16x32_bf16 a[92:95], a[252:255], v[124:127], a[92:95]\n\t"
    "v_exp_f32 v58, v58\n\t"
    "v_exp_f32 v59, v59\n\t"
    "v_mfma_f32_16x16x32_bf16 v[216:219], v[224:227], v[104:107], v[216:219]\n\t"
    "v_cvt_pk_bf16_f32 v138, v48, v49\n\t"
    "v_cvt_pk_bf16_f32 v139, v50, v51\n\t"
    "v_mfma_f32_16x16x32_bf16 v[216:219], v[224:227], v[108:111], v[216:219]\n\t"
    "v_cvt_pk_bf16_f32 v170, v56, v57\n\t"
    "v_cvt_pk_bf16_f32 v171, v58, v59\n\t"
    "s_waitcnt lgkmcnt(8)\n\t"
    "v_mfma_f32_16x16x32_bf16 v[32:35], a[192:195], a[128:131], 0\n\t"
    "v_cvt_pk_bf16_f32 v52, 0, v52\n\t"
    "v_cvt_pk_bf16_f32 v53, 0, v53\n\t"
    "v_cvt_pk_bf16_f32 v54, 0, v54\n\t"
    "v_mfma_f32_16x16x32_bf16 v[32:35], a[196:199], a[132:135], v[32:35]\n\t"
    "ds_read_b128 a[208:211], v228 offset:61440\n\t"
    "v_cvt_pk_bf16_f32 v55, 0, v55\n\t"
    "v_mfma_f32_16x16x32_bf16 v[32:35], a[200:203], a[136:139], v[32:35]\n\t"
    "v_mul_f32 v52, s56, v52\n\t"
    "v_mul_f32 v53, s56, v53\n\t"
    "v_mul_f32 v54, s56, v54\n\t"
    "v_mfma_f32_16x16x32_bf16 v[32:35], a[204:207], a[140:143], v[32:35]\n\t"
    "v_mul_f32 v55, s56, v55\n\t"
    "v_cvt_pk_bf16_f32 v52, 0, v52\n\t"
    "v_mfma_f32_16x16x32_bf16 v[36:39], a[192:195], a[144:147], 0\n\t"
    "v_cvt_pk_bf16_f32 v53, 0, v53\n\t"
    "v_cvt_pk_bf16_f32 v54, 0, v54\n\t"
    "v_cvt_pk_bf16_f32 v55, 0, v55\n\t"
    "v_mfma_f32_16x16x32_bf16 v[36:39], a[196:199], a[148:151], v[36:39]\n\t"
    "ds_read_b128 a[212:215], v229 offset:61440\n\t"
    "v_fma_f32 v52, v52, s58, v193\n\t"
    "v_mfma_f32_16x16x32_bf16 v[36:39], a[200:203], a[152:155], v[36:39]\n\t"
    "v_fma_f32 v53, v53, s58, v193\n\t"
    "v_fma_f32 v54, v54, s58, v193\n\t"
    "v_fma_f32 v55, v55, s58, v193\n\t"
    "v_mfma_f32_16x16x32_bf16 v[36:39], a[204:207], a[156:159], v[36:39]\n\t"
    "v_exp_f32 v52, v52\n\t"
    "v_mfma_f32_16x16x32_bf16 v[40:43], a[192:195], a[160:163], v[196:199]\n\t"
    "v_exp_f32 v53, v53\n\t"
    "v_mfma_f32_16x16x32_bf16 v[40:43], a[196:199], a[164:167], v[40:43]\n\t"
    "ds_read_b128 a[216:219], v230 offset:61440\n\t"
    "v_exp_f32 v54, v54\n\t"
    "v_mfma_f32_16x16x32_bf16 v[40:43], a[200:203], a[168:171], v[40:43]\n\t"
    "v_exp_f32 v55, v55\n\t"
    "v_mfma_f32_16x16x32_bf16 v[40:43], a[204:207], a[172:175], v[40:43]\n\t"
    "v_exp_f32 v60, v60\n\t"
    "v_exp_f32 v61, v61\n\t"
    "v_mfma_f32_16x16x32_bf16 v[44:47], a[192:195], a[176:179], v[200:203]\n\t"
    "v_exp_f32 v62, v62\n\t"
    "v_mfma_f32_16x16x32_bf16 v[44:47], a[196:199], a[180:183], v[44:47]\n\t"
    "ds_read_b128 a[220:223], v231 offset:61440\n\t"
    "v_exp_f32 v63, v63\n\t"
    "v_mfma_f32_16x16x32_bf16 v[44:47], a[200:203], a[184:187], v[44:47]\n\t"
    "v_cvt_pk_bf16_f32 v154, v52, v53\n\t"
    "v_cvt_pk_bf16_f32 v155, v54, v55\n\t"
    "v_mfma_f32_16x16x32_bf16 v[44:47], a[204:207], a[188:191], v[44:47]\n\t"
    "v_cvt_pk_bf16_f32 v186, v60, v61\n\t"
    "v_cvt_pk_bf16_f32 v187, v62, v63\n\t"
    "s_waitcnt lgkmcnt(4)\n\t"
    "v_mfma_f32_16x16x32_bf16 a[96:99], a[224:227], v[64:67], a[96:99]\n\t"
    "v_cvt_pk_bf16_f32 v32, 0, v32\n\t"
    "v_cvt_pk_bf16_f32 v33, 0, v33\n\t"
    "v_mfma_f32_16x16x32_bf16 a[96:99], a[228:231], v[68:71], a[96:99]\n\t"
    "ds_read_b64_tr_b16 a[240:241], %[vb] offset:1792\n\t"
    "ds_read_b64_tr_b16 a[242:243], %[vb] offset:5888\n\t"
    "v_cvt_pk_bf16_f32 v34, 0, v34\n\t"
    "v_mfma_f32_16x16x32_bf16 a[96:99], a[232:235], v[72:75], a[96:99]\n\t"
    "v_cvt_pk_bf16_f32 v35, 0, v35\n\t"
    "v_mul_f32 v32, s56, v32\n\t"
    "v_mfma_f32_16x16x32_bf16 a[96:99], a[236:239], v[76:79], a[96:99]\n\t"
    "v_mul_f32 v33, s56, v33\n\t"
    "v_mul_f32 v34, s56, v34\n\t"
    "v_mul_f32 v35, s56, v35\n\t"
    "v_mfma_f32_16x16x32_bf16 a[100:103], a[224:227], v[80:83], a[100:103]\n\t"
    "v_cvt_pk_bf16_f32 v32, 0, v32\n\t"
    "v_cvt_pk_bf16_f32 v33, 0, v33\n\t"
    "v_mfma_f32_16x16x32_bf16 a[100:103], a[228:231], v[84:87], a[100:103]\n\t"
    "ds_read_b64_tr_b16 a[244:245], %[vb] offset:9984\n\t"
    "ds_read_b64_tr_b16 a[246:247], %[vb] offset:14080\n\t"
    "v_cvt_pk_bf16_f32 v34, 0, v34\n\t"
    "v_mfma_f32_16x16x32_bf16 a[100:103], a[232:235], v[88:91], a[100:103]\n\t"
    "v_cvt_pk_bf16_f32 v35, 0, v35\n\t"
    "v_fma_f32 v32, v32, s58, v192\n\t"
    "v_mfma_f32_16x16x32_bf16 a[100:103], a[236:239], v[92:95], a[100:103]\n\t"
    "v_fma_f32 v33, v33, s58, v192\n\t"
    "v_fma_f32 v34, v34, s58, v192\n\t"
    "v_fma_f32 v35, v35, s58, v192\n\t"
    "v_mfma_f32_16x16x32_bf16 a[104:107], a[224:227], v[96:99], a[104:107]\n\t"
    "v_exp_f32 v32, v32\n\t"
    "v_mfma_f32_16x16x32_bf16 a[104:107], a[228:231], v[100:103], a[104:107]\n\t"
    "ds_read_b64_tr_b16 a[248:249], %[vb] offset:18176\n\t"
    "ds_read_b64_tr_b16 a[250:251], %[vb] offset:22272\n\t"
    "v_mfma_f32_16x16x32_bf16 a[104:107], a[232:235], v[104:107], a[104:107]\n\t"
    "v_exp_f32 v33, v33\n\t"
    "v_mfma_f32_16x16x32_bf16 a[104:107], a[236:239], v[108:111], a[104:107]\n\t"
    "v_exp_f32 v34, v34\n\t"
    "v_exp_f32 v35, v35\n\t"
    "v_mfma_f32_16x16x32_bf16 a[108:111], a[224:227], v[112:115], a[108:111]\n\t"
    "v_exp_f32 v40, v40\n\t"
    "v_mfma_f32_16x16x32_bf16 a[108:111], a[228:231], v[116:119], a[108:111]\n\t"
    "ds_read_b64_tr_b16 a[252:253], %[vb] offset:26368\n\t"
    "ds_read_b64_tr_b16 a[254:255], %[vb] offset:30464\n\t"
    "v_mfma_f32_16x16x32_bf16 a[108:111], a[232:235], v[120:123], a[108:111]\n\t"
    "v_exp_f32 v41, v41\n\t"
    "v_mfma_f32_16x16x32_bf16 a[108:111], a[236:239], v[124:127], a[108:111]\n\t"
    "v_exp_f32 v42, v42\n\t"
    "v_exp_f32 v43, v43\n\t"
    "v_mfma_f32_16x16x32_bf16 v[220:223], v[224:227], v[112:115], v[220:223]\n\t"
    "v_cvt_pk_bf16_f32 v140, v32, v33\n\t"
    "v_cvt_pk_bf16_f32 v141, v34, v35\n\t"
    "v_mfma_f32_16x16x32_bf16 v[220:223], v[224:227], v[116:119], v[220:223]\n\t"
    "v_cvt_pk_bf16_f32 v172, v40, v41\n\t"
    "v_cvt_pk_bf16_f32 v173, v42, v43\n\t"
    "s_waitcnt vmcnt(0) lgkmcnt(0)\n\t"
    "s_barrier\n\t"
    "v_mfma_f32_16x16x32_bf16 v[48:51], a[208:211], a[128:131], 0\n\t"
    "v_cvt_pk_bf16_f32 v36, 0, v36\n\t"
    "v_cvt_pk_bf16_f32 v37, 0, v37\n\t"
    "v_cvt_pk_bf16_f32 v38, 0, v38\n\t"
    "v_mfma_f32_16x16x32_bf16 v[48:51], a[212:215], a[132:135], v[48:51]\n\t"
    "ds_read_b128 a[192:195], v228 offset:0\n\t"
    "v_cvt_pk_bf16_f32 v39, 0, v39\n\t"
    "v_mul_f32 v36, s56, v36\n\t"
    "v_mfma_f32_16x16x32_bf16 v[48:51], a[216:219], a[136:139], v[48:51]\n\t"
    "v_mul_f32 v37, s56, v37\n\t"
    "v_mul_f32 v38, s56, v38\n\t"
    "v_mul_f32 v39, s56, v39\n\t"
    "v_mfma_f32_16x16x32_bf16 v[48:51], a[220:223], a[140:143], v[48:51]\n\t"
    "s_xor_b32 s42, s42, 0x8000\n\t"
    "s_add_u32 m0, s42, 0\n\t"
    "s_add_u32 s45, s40, 0\n\t"
    "buffer_load_dwordx4 %[ksrc0], %[srdK], s45 offen offset:0 lds\n\t"
    "v_mfma_f32_16x16x32_bf16 v[52:55], a[208:211], a[144:147], 0\n\t"
    "v_cvt_pk_bf16_f32 v36, 0, v36\n\t"
    "v_mfma_f32_16x16x32_bf16 v[52:55], a[212:215], a[148:151], v[52:55]\n\t"
    "ds_read_b128 a[196:199], v229 offset:0\n\t"
    "v_cvt_pk_bf16_f32 v37, 0, v37\n\t"
    "v_cvt_pk_bf16_f32 v38, 0, v38\n\t"
    "v_mfma_f32_16x16x32_bf16 v[52:55], a[216:219], a[152:155], v[52:55]\n\t"
    "v_cvt_pk_bf16_f32 v39, 0, v39\n\t"
    "v_fma_f32 v36, v36, s58, v193\n\t"
    "v_fma_f32 v37, v37, s58, v193\n\t"
    "v_mfma_f32_16x16x32_bf16 v[52:55], a[220:223], a[156:159], v[52:55]\n\t"
    "v_fma_f32 v38, v38, s58, v193\n\t"
    "v_fma_f32 v39, v39, s58, v193\n\t"
    "v_mfma_f32_16x16x32_bf16 v[56:59], a[208:211], a[160:163], v[196:199]\n\t"
    "v_exp_f32 v36, v36\n\t"
    "v_exp_f32 v37, v37\n\t"
    "v_mfma_f32_16x16x32_bf16 v[56:59], a[212:215], a[164:167], v[56:59]\n\t"
    "ds_read_b128 a[200:203], v230 offset:0\n\t"
    "v_exp_f32 v38, v38\n\t"
    "v_mfma_f32_16x16x32_bf16 v[56:59], a[216:219], a[168:171], v[56:59]\n\t"
    "v_exp_f32 v39, v39\n\t"
    "v_mfma_f32_16x16x32_bf16 v[56:59], a[220:223], a[172:175], v[56:59]\n\t"
    "buffer_load_dwordx4 v242, %[srdK], s45 offen offset:1024 lds\n\t"
    "v_exp_f32 v44, v44\n\t"
    "v_mfma_f32_16x16x32_bf16 v[60:63], a[208:211], a[176:179], v[200:203]\n\t"
    "v_exp_f32 v45, v45\n\t"
    "v_mfma_f32_16x16x32_bf16 v[60:63], a[212:215], a[180:183], v[60:63]\n\t"
    "ds_read_b128 a[204:207], v231 offset:0\n\t"
    "v_exp_f32 v46, v46\n\t"
    "v_mfma_f32_16x16x32_bf16 v[60:63], a[216:219], a[184:187], v[60:63]\n\t"
    "v_exp_f32 v47, v47\n\t"
    "v_cvt_pk_bf16_f32 v156, v36, v37\n\t"
    "v_mfma_f32_16x16x32_bf16 v[60:63], a[220:223], a[188:191], v[60:63]\n\t"
    "v_cvt_pk_bf16_f32 v157, v38, v39\n\t"
    "v_cvt_pk_bf16_f32 v188, v44, v45\n\t"
    "v_cvt_pk_bf16_f32 v189, v46, v47\n\t"
    "s_waitcnt lgkmcnt(4)\n\t"
    "v_mfma_f32_16x16x32_bf16 a[112:115], a[240:243], v[64:67], a[112:115]\n\t"
    "v_cvt_pk_bf16_f32 v48, 0, v48\n\t"
    "v_cvt_pk_bf16_f32 v49, 0, v49\n\t"
    "v_cvt_pk_bf16_f32 v50, 0, v50\n\t"
    "v_mfma_f32_16x16x32_bf16 a[112:115], a[244:247], v[68:71], a[112:115]\n\t"
    "ds_read_b64_tr_b16 a[224:225], %[vb] offset:32768\n\t"
    "ds_read_b64_tr_b16 a[226:227], %[vb] offset:36864\n\t"
    "v_mfma_f32_16x16x32_bf16 a[112:115], a[248:251], v[72:75], a[112:115]\n\t"
    "v_cvt_pk_bf16_f32 v51, 0, v51\n\t"
    "v_mul_f32 v48, s56, v48\n\t"
    "v_mul_f32 v49, s56, v49\n\t"
    "v_mfma_f32_16x16x32_bf16 a[112:115], a[252:255], v[76:79], a[112:115]\n\t"
    "buffer_load_dwordx4 v243, %[srdK], s45 offen offset:2048 lds\n\t"
    "v_mul_f32 v50, s56, v50\n\t"
    "v_mfma_f32_16x16x32_bf16 a[116:119], a[240:243], v[80:83], a[116:119]\n\t"
    "v_mul_f32 v51, s56, v51\n\t"
    "v_cvt_pk_bf16_f32 v48, 0, v48\n\t"
    "v_mfma_f32_16x16x32_bf16 a[116:119], a[244:247], v[84:87], a[116:119]\n\t"
    "ds_read_b64_tr_b16 a[228:229], %[vb] offset:40960\n\t"
    "ds_read_b64_tr_b16 a[230:231], %[vb] offset:45056\n\t"
    "v_cvt_pk_bf16_f32 v49, 0, v49\n\t"
    "v_mfma_f32_16x16x32_bf16 a[116:119], a[248:251], v[88:91], a[116:119]\n\t"
    "v_cvt_pk_bf16_f32 v50, 0, v50\n\t"
    "v_cvt_pk_bf16_f32 v51, 0, v51\n\t"
    "v_fma_f32 v48, v48, s58, v192\n\t"
    "v_mfma_f32_16x16x32_bf16 a[116:119], a[252:255], v[92:95], a[116:119]\n\t"
    "v_fma_f32 v49, v49, s58, v192\n\t"
    "v_fma_f32 v50, v50, s58, v192\n\t"
    "v_mfma_f32_16x16x32_bf16 a[120:123], a[240:243], v[96:99], a[120:123]\n\t"
    "v_fma_f32 v51, v51, s58, v192\n\t"
    "v_exp_f32 v48, v48\n\t"
    "v_mfma_f32_16x16x32_bf16 a[120:123], a[244:247], v[100:103], a[120:123]\n\t"
    "ds_read_b64_tr_b16 a[232:233], %[vb] offset:49152\n\t"
    "ds_read_b64_tr_b16 a[234:235], %[vb] offset:53248\n\t"
    "v_mfma_f32_16x16x32_bf16 a[120:123], a[248:251], v[104:107], a[120:123]\n\t"
    "v_exp_f32 v49, v49\n\t"
    "v_exp_f32 v50, v50\n\t"
    "v_mfma_f32_16x16x32_bf16 a[120:123], a[252:255], v[108:111], a[120:123]\n\t"
    "buffer_load_dwordx4 v244, %[srdK], s45 offen offset:3072 lds\n\t"
    "v_mfma_f32_16x16x32_bf16 a[124:127], a[240:243], v[112:115], a[124:127]\n\t"
    "v_exp_f32 v51, v51\n\t"
    "v_mfma_f32_16x16x32_bf16 a[124:127], a[244:247], v[116:119], a[124:127]\n\t"
    "ds_read_b64_tr_b16 a[236:237], %[vb] offset:57344\n\t"
    "ds_read_b64_tr_b16 a[238:239], %[vb] offset:61440\n\t"
    "v_exp_f32 v56, v56\n\t"
    "v_mfma_f32_16x16x32_bf16 a[124:127], a[248:251], v[120:123], a[124:127]\n\t"
    "v_exp_f32 v57, v57\n\t"
    "v_mfma_f32_16x16x32_bf16 a[124:127], a[252:255], v[124:127], a[124:127]\n\t"
    "v_exp_f32 v58, v58\n\t"
    "v_mfma_f32_16x16x32_bf16 v[220:223], v[224:227], v[120:123], v[220:223]\n\t"
    "v_exp_f32 v59, v59\n\t"
    "v_cvt_pk_bf16_f32 v142, v48, v49\n\t"
    "v_mfma_f32_16x16x32_bf16 v[220:223], v[224:227], v[124:127], v[220:223]\n\t"
    "v_cvt_pk_bf16_f32 v143, v50, v51\n\t"
    "v_cvt_pk_bf16_f32 v174, v56, v57\n\t"
    "v_cvt_pk_bf16_f32 v175, v58, v59\n\t"
    "s_waitcnt lgkmcnt(8)\n\t"
    "v_mfma_f32_16x16x32_bf16 v[32:35], a[192:195], a[128:131], 0\n\t"
    "v_cvt_pk_bf16_f32 v52, 0, v52\n\t"
    "v_cvt_pk_bf16_f32 v53, 0, v53\n\t"
    "v_cvt_pk_bf16_f32 v54, 0, v54\n\t"
    "v_mfma_f32_16x16x32_bf16 v[32:35], a[196:199], a[132:135], v[32:35]\n\t"
    "ds_read_b128 a[208:211], v228 offset:4096\n\t"
    "v_cvt_pk_bf16_f32 v55, 0, v55\n\t"
    "v_mul_f32 v52, s56, v52\n\t"
    "v_mfma_f32_16x16x32_bf16 v[32:35], a[200:203], a[136:139], v[32:35]\n\t"
    "v_mul_f32 v53, s56, v53\n\t"
    "v_mul_f32 v54, s56, v54\n\t"
    "v_mul_f32 v55, s56, v55\n\t"
    "v_mfma_f32_16x16x32_bf16 v[32:35], a[204:207], a[140:143], v[32:35]\n\t"
    "s_add_u32 m0, s42, 4096\n\t"
    "s_add_u32 s45, s40, 4096\n\t"
    "buffer_load_dwordx4 %[ksrc0], %[srdK], s45 offen offset:0 lds\n\t"
    "v_mfma_f32_16x16x32_bf16 v[36:39], a[192:195], a[144:147], 0\n\t"
    "v_cvt_pk_bf16_f32 v52, 0, v52\n\t"
    "v_mfma_f32_16x16x32_bf16 v[36:39], a[196:199], a[148:151], v[36:39]\n\t"
    "ds_read_b128 a[212:215], v229 offset:4096\n\t"
    "v_cvt_pk_bf16_f32 v53, 0, v53\n\t"
    "v_cvt_pk_bf16_f32 v54, 0, v54\n\t"
    "v_mfma_f32_16x16x32_bf16 v[36:39], a[200:203], a[152:155], v[36:39]\n\t"
    "v_cvt_pk_bf16_f32 v55, 0, v55\n\t"
    "v_fma_f32 v52, v52, s58, v193\n\t"
    "v_fma_f32 v53, v53, s58, v193\n\t"
    "v_mfma_f32_16x16x32_bf16 v[36:39], a[204:207], a[156:159], v[36:39]\n\t"
    "v_fma_f32 v54, v54, s58, v193\n\t"
    "v_fma_f32 v55, v55, s58, v193\n\t"
    "v_exp_f32 v52, v52\n\t"
    "v_mfma_f32_16x16x32_bf16 v[40:43], a[192:195], a[160:163], v[196:199]\n\t"
    "v_exp_f32 v53, v53\n\t"
    "v_mfma_f32_16x16x32_bf16 v[40:43], a[196:199], a[164:167], v[40:43]\n\t"
    "ds_read_b128 a[216:219], v230 offset:4096\n\t"
    "v_exp_f32 v54, v54\n\t"
    "v_mfma_f32_16x16x32_bf16 v[40:43], a[200:203], a[168:171], v[40:43]\n\t"
    "v_exp_f32 v55, v55\n\t"
    "v_mfma_f32_16x16x32_bf16 v[40:43], a[204:207], a[172:175], v[40:43]\n\t"
    "buffer_load_dwordx4 v242, %[srdK], s45 offen offset:1024 lds\n\t"
    "v_exp_f32 v60, v60\n\t"
    "v_mfma_f32_16x16x32_bf16 v[44:47], a[192:195], a[176:179], v[200:203]\n\t"
    "v_exp_f32 v61, v61\n\t"
    "v_mfma_f32_16x16x32_bf16 v[44:47], a[196:199], a[180:183], v[44:47]\n\t"
    "ds_read_b128 a[220:223], v231 offset:4096\n\t"
    "v_exp_f32 v62, v62\n\t"
    "v_mfma_f32_16x16x32_bf16 v[44:47], a[200:203], a[184:187], v[44:47]\n\t"
    "v_exp_f32 v63, v63\n\t"
    "v_cvt_pk_bf16_f32 v158, v52, v53\n\t"
    "v_mfma_f32_16x16x32_bf16 v[44:47], a[204:207], a[188:191], v[44:47]\n\t"
    "v_cvt_pk_bf16_f32 v159, v54, v55\n\t"
    "v_cvt_pk_bf16_f32 v190, v60, v61\n\t"
    "v_cvt_pk_bf16_f32 v191, v62, v63\n\t"
    "s_waitcnt lgkmcnt(4)\n\t"
    "v_mfma_f32_16x16x32_bf16 a[0:3], a[224:227], v[128:131], a[0:3]\n\t"
    "v_cvt_pk_bf16_f32 v32, 0, v32\n\t"
    "v_cvt_pk_bf16_f32 v33, 0, v33\n\t"
    "v_cvt_pk_bf16_f32 v34, 0, v34\n\t"
    "v_mfma_f32_16x16x32_bf16 a[0:3], a[228:231], v[132:135], a[0:3]\n\t"
    "ds_read_b64_tr_b16 a[240:241], %[vb] offset:33024\n\t"
    "ds_read_b64_tr_b16 a[242:243], %[vb] offset:37120\n\t"
    "v_mfma_f32_16x16x32_bf16 a[0:3], a[232:235], v[136:139], a[0:3]\n\t"
    "v_cvt_pk_bf16_f32 v35, 0, v35\n\t"
    "v_mul_f32 v32, s56, v32\n\t"
    "v_mul_f32 v33, s56, v33\n\t"
    "v_mfma_f32_16x16x32_bf16 a[0:3], a[236:239], v[140:143], a[0:3]\n\t"
    "buffer_load_dwordx4 v243, %[srdK], s45 offen offset:2048 lds\n\t"
    "v_mul_f32 v34, s56, v34\n\t"
    "v_mfma_f32_16x16x32_bf16 a[4:7], a[224:227], v[144:147], a[4:7]\n\t"
    "v_mul_f32 v35, s56, v35\n\t"
    "v_cvt_pk_bf16_f32 v32, 0, v32\n\t"
    "v_mfma_f32_16x16x32_bf16 a[4:7], a[228:231], v[148:151], a[4:7]\n\t"
    "ds_read_b64_tr_b16 a[244:245], %[vb] offset:41216\n\t"
    "ds_read_b64_tr_b16 a[246:247], %[vb] offset:45312\n\t"
    "v_cvt_pk_bf16_f32 v33, 0, v33\n\t"
    "v_mfma_f32_16x16x32_bf16 a[4:7], a[232:235], v[152:155], a[4:7]\n\t"
    "v_cvt_pk_bf16_f32 v34, 0, v34\n\t"
    "v_cvt_pk_bf16_f32 v35, 0, v35\n\t"
    "v_fma_f32 v32, v32, s58, v192\n\t"
    "v_mfma_f32_16x16x32_bf16 a[4:7], a[236:239], v[156:159], a[4:7]\n\t"
    "v_fma_f32 v33, v33, s58, v192\n\t"
    "v_fma_f32 v34, v34, s58, v192\n\t"
    "v_mfma_f32_16x16x32_bf16 a[8:11], a[224:227], v[160:163], a[8:11]\n\t"
    "v_fma_f32 v35, v35, s58, v192\n\t"
    "v_exp_f32 v32, v32\n\t"
    "v_mfma_f32_16x16x32_bf16 a[8:11], a[228:231], v[164:167], a[8:11]\n\t"
    "ds_read_b64_tr_b16 a[248:249], %[vb] offset:49408\n\t"
    "ds_read_b64_tr_b16 a[250:251], %[vb] offset:53504\n\t"
    "v_mfma_f32_16x16x32_bf16 a[8:11], a[232:235], v[168:171], a[8:11]\n\t"
    "v_exp_f32 v33, v33\n\t"
    "v_exp_f32 v34, v34\n\t"
    "v_mfma_f32_16x16x32_bf16 a[8:11], a[236:239], v[172:175], a[8:11]\n\t"
    "buffer_load_dwordx4 v244, %[srdK], s45 offen offset:3072 lds\n\t"
    "v_mfma_f32_16x16x32_bf16 a[12:15], a[224:227], v[176:179], a[12:15]\n\t"
    "v_exp_f32 v35, v35\n\t"
    "v_mfma_f32_16x16x32_bf16 a[12:15], a[228:231], v[180:183], a[12:15]\n\t"
    "ds_read_b64_tr_b16 a[252:253], %[vb] offset:57600\n\t"
    "ds_read_b64_tr_b16 a[254:255], %[vb] offset:61696\n\t"
    "v_exp_f32 v40, v40\n\t"
    "v_mfma_f32_16x16x32_bf16 a[12:15], a[232:235], v[184:187], a[12:15]\n\t"
    "v_exp_f32 v41, v41\n\t"
    "v_mfma_f32_16x16x32_bf16 a[12:15], a[236:239], v[188:191], a[12:15]\n\t"
    "v_exp_f32 v42, v42\n\t"
    "v_mfma_f32_16x16x32_bf16 v[208:211], v[224:227], v[128:131], v[208:211]\n\t"
    "v_exp_f32 v43, v43\n\t"
    "v_cvt_pk_bf16_f32 v64, v32, v33\n\t"
    "v_mfma_f32_16x16x32_bf16 v[208:211], v[224:227], v[132:135], v[208:211]\n\t"
    "v_cvt_pk_bf16_f32 v65, v34, v35\n\t"
    "v_cvt_pk_bf16_f32 v96, v40, v41\n\t"
    "v_cvt_pk_bf16_f32 v97, v42, v43\n\t"
    "s_waitcnt lgkmcnt(8)\n\t"
    "v_mfma_f32_16x16x32_bf16 v[48:51], a[208:211], a[128:131], 0\n\t"
    "v_cvt_pk_bf16_f32 v36, 0, v36\n\t"
    "v_cvt_pk_bf16_f32 v37, 0, v37\n\t"
    "v_cvt_pk_bf16_f32 v38, 0, v38\n\t"
    "v_mfma_f32_16x16x32_bf16 v[48:51], a[212:215], a[132:135], v[48:51]\n\t"
    "ds_read_b128 a[192:195], v228 offset:8192\n\t"
    "v_cvt_pk_bf16_f32 v39, 0, v39\n\t"
    "v_mul_f32 v36, s56, v36\n\t"
    "v_mfma_f32_16x16x32_bf16 v[48:51], a[216:219], a[136:139], v[48:51]\n\t"
    "v_mul_f32 v37, s56, v37\n\t"
    "v_mul_f32 v38, s56, v38\n\t"
    "v_mul_f32 v39, s56, v39\n\t"
    "v_mfma_f32_16x16x32_bf16 v[48:51], a[220:223], a[140:143], v[48:51]\n\t"
    "s_add_u32 s40, s40, 0x8000\n\t"
    "s_xor_b32 s43, s43, 0x8000\n\t"
    "s_add_u32 m0, s43, 0\n\t"
    "s_add_u32 s46, s41, 0\n\t"
    "buffer_load_dwordx4 %[vsrc0], %[srdV], s46 offen offset:0 lds\n\t"
    "v_mfma_f32_16x16x32_bf16 v[52:55], a[208:211], a[144:147], 0\n\t"
    "v_mfma_f32_16x16x32_bf16 v[52:55], a[212:215], a[148:151], v[52:55]\n\t"
    "ds_read_b128 a[196:199], v229 offset:8192\n\t"
    "v_cvt_pk_bf16_f32 v36, 0, v36\n\t"
    "v_cvt_pk_bf16_f32 v37, 0, v37\n\t"
    "v_mfma_f32_16x16x32_bf16 v[52:55], a[216:219], a[152:155], v[52:55]\n\t"
    "v_cvt_pk_bf16_f32 v38, 0, v38\n\t"
    "v_cvt_pk_bf16_f32 v39, 0, v39\n\t"
    "v_fma_f32 v36, v36, s58, v193\n\t"
    "v_mfma_f32_16x16x32_bf16 v[52:55], a[220:223], a[156:159], v[52:55]\n\t"
    "v_fma_f32 v37, v37, s58, v193\n\t"
    "v_fma_f32 v38, v38, s58, v193\n\t"
    "v_fma_f32 v39, v39, s58, v193\n\t"
    "v_mfma_f32_16x16x32_bf16 v[56:59], a[208:211], a[160:163], v[196:199]\n\t"
    "v_exp_f32 v36, v36\n\t"
    "v_exp_f32 v37, v37\n\t"
    "v_mfma_f32_16x16x32_bf16 v[56:59], a[212:215], a[164:167], v[56:59]\n\t"
    "ds_read_b128 a[200:203], v230 offset:8192\n\t"
    "v_exp_f32 v38, v38\n\t"
    "v_mfma_f32_16x16x32_bf16 v[56:59], a[216:219], a[168:171], v[56:59]\n\t"
    "v_exp_f32 v39, v39\n\t"
    "v_mfma_f32_16x16x32_bf16 v[56:59], a[220:223], a[172:175], v[56:59]\n\t"
    "buffer_load_dwordx4 v245, %[srdV], s46 offen offset:1024 lds\n\t"
    "v_exp_f32 v44, v44\n\t"
    "v_mfma_f32_16x16x32_bf16 v[60:63], a[208:211], a[176:179], v[200:203]\n\t"
    "v_exp_f32 v45, v45\n\t"
    "v_mfma_f32_16x16x32_bf16 v[60:63], a[212:215], a[180:183], v[60:63]\n\t"
    "ds_read_b128 a[204:207], v231 offset:8192\n\t"
    "v_exp_f32 v46, v46\n\t"
    "v_mfma_f32_16x16x32_bf16 v[60:63], a[216:219], a[184:187], v[60:63]\n\t"
    "v_exp_f32 v47, v47\n\t"
    "v_cvt_pk_bf16_f32 v80, v36, v37\n\t"
    "v_mfma_f32_16x16x32_bf16 v[60:63], a[220:223], a[188:191], v[60:63]\n\t"
    "v_cvt_pk_bf16_f32 v81, v38, v39\n\t"
    "v_cvt_pk_bf16_f32 v112, v44, v45\n\t"
    "v_cvt_pk_bf16_f32 v113, v46, v47\n\t"
    "s_waitcnt lgkmcnt(4)\n\t"
    "v_mfma_f32_16x16x32_bf16 a[16:19], a[240:243], v[128:131], a[16:19]\n\t"
    "v_cvt_pk_bf16_f32 v48, 0, v48\n\t"
    "v_cvt_pk_bf16_f32 v49, 0, v49\n\t"
    "v_cvt_pk_bf16_f32 v50, 0, v50\n\t"
    "v_mfma_f32_16x16x32_bf16 a[16:19], a[244:247], v[132:135], a[16:19]\n\t"
    "ds_read_b64_tr_b16 a[224:225], %[vb] offset:33280\n\t"
    "ds_read_b64_tr_b16 a[226:227], %[vb] offset:37376\n\t"
    "v_mfma_f32_16x16x32_bf16 a[16:19], a[248:251], v[136:139], a[16:19]\n\t"
    "v_cvt_pk_bf16_f32 v51, 0, v51\n\t"
    "v_mul_f32 v48, s56, v48\n\t"
    "v_mul_f32 v49, s56, v49\n\t"
    "v_mfma_f32_16x16x32_bf16 a[16:19], a[252:255], v[140:143], a[16:19]\n\t"
    "buffer_load_dwordx4 %[vsrc0], %[srdV], s46 offen offset:2048 lds\n\t"
    "v_mul_f32 v50, s56, v50\n\t"
    "v_mfma_f32_16x16x32_bf16 a[20:23], a[240:243], v[144:147], a[20:23]\n\t"
    "v_mul_f32 v51, s56, v51\n\t"
    "v_cvt_pk_bf16_f32 v48, 0, v48\n\t"
    "v_mfma_f32_16x16x32_bf16 a[20:23], a[244:247], v[148:151], a[20:23]\n\t"
    "ds_read_b64_tr_b16 a[228:229], %[vb] offset:41472\n\t"
    "ds_read_b64_tr_b16 a[230:231], %[vb] offset:45568\n\t"
    "v_cvt_pk_bf16_f32 v49, 0, v49\n\t"
    "v_mfma_f32_16x16x32_bf16 a[20:23], a[248:251], v[152:155], a[20:23]\n\t"
    "v_cvt_pk_bf16_f32 v50, 0, v50\n\t"
    "v_cvt_pk_bf16_f32 v51, 0, v51\n\t"
    "v_fma_f32 v48, v48, s58, v192\n\t"
    "v_mfma_f32_16x16x32_bf16 a[20:23], a[252:255], v[156:159], a[20:23]\n\t"
    "v_fma_f32 v49, v49, s58, v192\n\t"
    "v_fma_f32 v50, v50, s58, v192\n\t"
    "v_mfma_f32_16x16x32_bf16 a[24:27], a[240:243], v[160:163], a[24:27]\n\t"
    "v_fma_f32 v51, v51, s58, v192\n\t"
    "v_exp_f32 v48, v48\n\t"
    "v_mfma_f32_16x16x32_bf16 a[24:27], a[244:247], v[164:167], a[24:27]\n\t"
    "ds_read_b64_tr_b16 a[232:233], %[vb] offset:49664\n\t"
    "ds_read_b64_tr_b16 a[234:235], %[vb] offset:53760\n\t"
    "v_mfma_f32_16x16x32_bf16 a[24:27], a[248:251], v[168:171], a[24:27]\n\t"
    "v_exp_f32 v49, v49\n\t"
    "v_exp_f32 v50, v50\n\t"
    "v_mfma_f32_16x16x32_bf16 a[24:27], a[252:255], v[172:175], a[24:27]\n\t"
    "buffer_load_dwordx4 v245, %[srdV], s46 offen offset:3072 lds\n\t"
    "v_mfma_f32_16x16x32_bf16 a[28:31], a[240:243], v[176:179], a[28:31]\n\t"
    "v_exp_f32 v51, v51\n\t"
    "v_mfma_f32_16x16x32_bf16 a[28:31], a[244:247], v[180:183], a[28:31]\n\t"
    "ds_read_b64_tr_b16 a[236:237], %[vb] offset:57856\n\t"
    "ds_read_b64_tr_b16 a[238:239], %[vb] offset:61952\n\t"
    "v_exp_f32 v56, v56\n\t"
    "v_mfma_f32_16x16x32_bf16 a[28:31], a[248:251], v[184:187], a[28:31]\n\t"
    "v_exp_f32 v57, v57\n\t"
    "v_mfma_f32_16x16x32_bf16 a[28:31], a[252:255], v[188:191], a[28:31]\n\t"
    "v_exp_f32 v58, v58\n\t"
    "v_mfma_f32_16x16x32_bf16 v[208:211], v[224:227], v[136:139], v[208:211]\n\t"
    "v_exp_f32 v59, v59\n\t"
    "v_cvt_pk_bf16_f32 v66, v48, v49\n\t"
    "v_mfma_f32_16x16x32_bf16 v[208:211], v[224:227], v[140:143], v[208:211]\n\t"
    "v_cvt_pk_bf16_f32 v67, v50, v51\n\t"
    "v_cvt_pk_bf16_f32 v98, v56, v57\n\t"
    "v_cvt_pk_bf16_f32 v99, v58, v59\n\t"
    "s_waitcnt lgkmcnt(8)\n\t"
    "v_mfma_f32_16x16x32_bf16 v[32:35], a[192:195], a[128:131], 0\n\t"
    "v_cvt_pk_bf16_f32 v52, 0, v52\n\t"
    "v_cvt_pk_bf16_f32 v53, 0, v53\n\t"
    "v_cvt_pk_bf16_f32 v54, 0, v54\n\t"
    "v_mfma_f32_16x16x32_bf16 v[32:35], a[196:199], a[132:135], v[32:35]\n\t"
    "ds_read_b128 a[208:211], v228 offset:12288\n\t"
    "v_cvt_pk_bf16_f32 v55, 0, v55\n\t"
    "v_mul_f32 v52, s56, v52\n\t"
    "v_mfma_f32_16x16x32_bf16 v[32:35], a[200:203], a[136:139], v[32:35]\n\t"
    "v_mul_f32 v53, s56, v53\n\t"
    "v_mul_f32 v54, s56, v54\n\t"
    "v_mul_f32 v55, s56, v55\n\t"
    "v_mfma_f32_16x16x32_bf16 v[32:35], a[204:207], a[140:143], v[32:35]\n\t"
    "s_add_u32 m0, s43, 4096\n\t"
    "s_add_u32 s46, s41, 4096\n\t"
    "buffer_load_dwordx4 %[vsrc0], %[srdV], s46 offen offset:0 lds\n\t"
    "v_mfma_f32_16x16x32_bf16 v[36:39], a[192:195], a[144:147], 0\n\t"
    "v_cvt_pk_bf16_f32 v52, 0, v52\n\t"
    "v_mfma_f32_16x16x32_bf16 v[36:39], a[196:199], a[148:151], v[36:39]\n\t"
    "ds_read_b128 a[212:215], v229 offset:12288\n\t"
    "v_cvt_pk_bf16_f32 v53, 0, v53\n\t"
    "v_cvt_pk_bf16_f32 v54, 0, v54\n\t"
    "v_mfma_f32_16x16x32_bf16 v[36:39], a[200:203], a[152:155], v[36:39]\n\t"
    "v_cvt_pk_bf16_f32 v55, 0, v55\n\t"
    "v_fma_f32 v52, v52, s58, v193\n\t"
    "v_fma_f32 v53, v53, s58, v193\n\t"
    "v_mfma_f32_16x16x32_bf16 v[36:39], a[204:207], a[156:159], v[36:39]\n\t"
    "v_fma_f32 v54, v54, s58, v193\n\t"
    "v_fma_f32 v55, v55, s58, v193\n\t"
    "v_exp_f32 v52, v52\n\t"
    "v_mfma_f32_16x16x32_bf16 v[40:43], a[192:195], a[160:163], v[196:199]\n\t"
    "v_exp_f32 v53, v53\n\t"
    "v_mfma_f32_16x16x32_bf16 v[40:43], a[196:199], a[164:167], v[40:43]\n\t"
    "ds_read_b128 a[216:219], v230 offset:12288\n\t"
    "v_exp_f32 v54, v54\n\t"
    "v_mfma_f32_16x16x32_bf16 v[40:43], a[200:203], a[168:171], v[40:43]\n\t"
    "v_exp_f32 v55, v55\n\t"
    "v_mfma_f32_16x16x32_bf16 v[40:43], a[204:207], a[172:175], v[40:43]\n\t"
    "buffer_load_dwordx4 v245, %[srdV], s46 offen offset:1024 lds\n\t"
    "v_exp_f32 v60, v60\n\t"
    "v_mfma_f32_16x16x32_bf16 v[44:47], a[192:195], a[176:179], v[200:203]\n\t"
    "v_exp_f32 v61, v61\n\t"
    "v_mfma_f32_16x16x32_bf16 v[44:47], a[196:199], a[180:183], v[44:47]\n\t"
    "ds_read_b128 a[220:223], v231 offset:12288\n\t"
    "v_exp_f32 v62, v62\n\t"
    "v_mfma_f32_16x16x32_bf16 v[44:47], a[200:203], a[184:187], v[44:47]\n\t"
    "v_exp_f32 v63, v63\n\t"
    "v_cvt_pk_bf16_f32 v82, v52, v53\n\t"
    "v_mfma_f32_16x16x32_bf16 v[44:47], a[204:207], a[188:191], v[44:47]\n\t"
    "v_cvt_pk_bf16_f32 v83, v54, v55\n\t"
    "v_cvt_pk_bf16_f32 v114, v60, v61\n\t"
    "v_cvt_pk_bf16_f32 v115, v62, v63\n\t"
    "s_waitcnt lgkmcnt(4)\n\t"
    "v_mfma_f32_16x16x32_bf16 a[32:35], a[224:227], v[128:131], a[32:35]\n\t"
    "v_cvt_pk_bf16_f32 v32, 0, v32\n\t"
    "v_cvt_pk_bf16_f32 v33, 0, v33\n\t"
    "v_cvt_pk_bf16_f32 v34, 0, v34\n\t"
    "v_mfma_f32_16x16x32_bf16 a[32:35], a[228:231], v[132:135], a[32:35]\n\t"
    "ds_read_b64_tr_b16 a[240:241], %[vb] offset:33536\n\t"
    "ds_read_b64_tr_b16 a[242:243], %[vb] offset:37632\n\t"
    "v_mfma_f32_16x16x32_bf16 a[32:35], a[232:235], v[136:139], a[32:35]\n\t"
    "v_cvt_pk_bf16_f32 v35, 0, v35\n\t"
    "v_mul_f32 v32, s56, v32\n\t"
    "v_mul_f32 v33, s56, v33\n\t"
    "v_mfma_f32_16x16x32_bf16 a[32:35], a[236:239], v[140:143], a[32:35]\n\t"
    "buffer_load_dwordx4 %[vsrc0], %[srdV], s46 offen offset:2048 lds\n\t"
    "v_mul_f32 v34, s56, v34\n\t"
    "v_mfma_f32_16x16x32_bf16 a[36:39], a[224:227], v[144:147], a[36:39]\n\t"
    "v_mul_f32 v35, s56, v35\n\t"
    "v_cvt_pk_bf16_f32 v32, 0, v32\n\t"
    "v_mfma_f32_16x16x32_bf16 a[36:39], a[228:231], v[148:151], a[36:39]\n\t"
    "ds_read_b64_tr_b16 a[244:245], %[vb] offset:41728\n\t"
    "ds_read_b64_tr_b16 a[246:247], %[vb] offset:45824\n\t"
    "v_cvt_pk_bf16_f32 v33, 0, v33\n\t"
    "v_mfma_f32_16x16x32_bf16 a[36:39], a[232:235], v[152:155], a[36:39]\n\t"
    "v_cvt_pk_bf16_f32 v34, 0, v34\n\t"
    "v_cvt_pk_bf16_f32 v35, 0, v35\n\t"
    "v_fma_f32 v32, v32, s58, v192\n\t"
    "v_mfma_f32_16x16x32_bf16 a[36:39], a[236:239], v[156:159], a[36:39]\n\t"
    "v_fma_f32 v33, v33, s58, v192\n\t"
    "v_fma_f32 v34, v34, s58, v192\n\t"
    "v_mfma_f32_16x16x32_bf16 a[40:43], a[224:227], v[160:163], a[40:43]\n\t"
    "v_fma_f32 v35, v35, s58, v192\n\t"
    "v_exp_f32 v32, v32\n\t"
    "v_mfma_f32_16x16x32_bf16 a[40:43], a[228:231], v[164:167], a[40:43]\n\t"
    "ds_read_b64_tr_b16 a[248:249], %[vb] offset:49920\n\t"
    "ds_read_b64_tr_b16 a[250:251], %[vb] offset:54016\n\t"
    "v_mfma_f32_16x16x32_bf16 a[40:43], a[232:235], v[168:171], a[40:43]\n\t"
    "v_exp_f32 v33, v33\n\t"
    "v_exp_f32 v34, v34\n\t"
    "v_mfma_f32_16x16x32_bf16 a[40:43], a[236:239], v[172:175], a[40:43]\n\t"
    "buffer_load_dwordx4 v245, %[srdV], s46 offen offset:3072 lds\n\t"
    "v_mfma_f32_16x16x32_bf16 a[44:47], a[224:227], v[176:179], a[44:47]\n\t"
    "v_exp_f32 v35, v35\n\t"
    "v_mfma_f32_16x16x32_bf16 a[44:47], a[228:231], v[180:183], a[44:47]\n\t"
    "ds_read_b64_tr_b16 a[252:253], %[vb] offset:58112\n\t"
    "ds_read_b64_tr_b16 a[254:255], %[vb] offset:62208\n\t"
    "v_exp_f32 v40, v40\n\t"
    "v_mfma_f32_16x16x32_bf16 a[44:47], a[232:235], v[184:187], a[44:47]\n\t"
    "v_exp_f32 v41, v41\n\t"
    "v_mfma_f32_16x16x32_bf16 a[44:47], a[236:239], v[188:191], a[44:47]\n\t"
    "v_exp_f32 v42, v42\n\t"
    "v_mfma_f32_16x16x32_bf16 v[212:215], v[224:227], v[144:147], v[212:215]\n\t"
    "v_exp_f32 v43, v43\n\t"
    "v_cvt_pk_bf16_f32 v68, v32, v33\n\t"
    "v_mfma_f32_16x16x32_bf16 v[212:215], v[224:227], v[148:151], v[212:215]\n\t"
    "v_cvt_pk_bf16_f32 v69, v34, v35\n\t"
    "v_cvt_pk_bf16_f32 v100, v40, v41\n\t"
    "v_cvt_pk_bf16_f32 v101, v42, v43\n\t"
    "s_waitcnt lgkmcnt(8)\n\t"
    "v_mfma_f32_16x16x32_bf16 v[48:51], a[208:211], a[128:131], 0\n\t"
    "v_cvt_pk_bf16_f32 v36, 0, v36\n\t"
    "v_cvt_pk_bf16_f32 v37, 0, v37\n\t"
    "v_cvt_pk_bf16_f32 v38, 0, v38\n\t"
    "v_mfma_f32_16x16x32_bf16 v[48:51], a[212:215], a[132:135], v[48:51]\n\t"
    "ds_read_b128 a[192:195], v228 offset:16384\n\t"
    "v_cvt_pk_bf16_f32 v39, 0, v39\n\t"
    "v_mul_f32 v36, s56, v36\n\t"
    "v_mfma_f32_16x16x32_bf16 v[48:51], a[216:219], a[136:139], v[48:51]\n\t"
    "v_mul_f32 v37, s56, v37\n\t"
    "v_mul_f32 v38, s56, v38\n\t"
    "v_mul_f32 v39, s56, v39\n\t"
    "v_mfma_f32_16x16x32_bf16 v[48:51], a[220:223], a[140:143], v[48:51]\n\t"
    "s_add_u32 s41, s41, 0x8000\n\t"
    "s_cmp_gt_u32 s44, 5\n\t"
    "s_cselect_b32 s47, 0x7fffff00, s51\n\t"
    "s_cselect_b32 s48, 0, %[pfstep]\n\t"
    "buffer_load_dword v246, %[pfv], %[srdPF], s47 offen\n\t"
    "v_mfma_f32_16x16x32_bf16 v[52:55], a[208:211], a[144:147], 0\n\t"
    "v_mfma_f32_16x16x32_bf16 v[52:55], a[212:215], a[148:151], v[52:55]\n\t"
    "ds_read_b128 a[196:199], v229 offset:16384\n\t"
    "v_cvt_pk_bf16_f32 v36, 0, v36\n\t"
    "v_cvt_pk_bf16_f32 v37, 0, v37\n\t"
    "v_mfma_f32_16x16x32_bf16 v[52:55], a[216:219], a[152:155], v[52:55]\n\t"
    "v_cvt_pk_bf16_f32 v38, 0, v38\n\t"
    "v_cvt_pk_bf16_f32 v39, 0, v39\n\t"
    "v_fma_f32 v36, v36, s58, v193\n\t"
    "v_mfma_f32_16x16x32_bf16 v[52:55], a[220:223], a[156:159], v[52:55]\n\t"
    "v_fma_f32 v37, v37, s58, v193\n\t"
    "v_fma_f32 v38, v38, s58, v193\n\t"
    "v_fma_f32 v39, v39, s58, v193\n\t"
    "v_mfma_f32_16x16x32_bf16 v[56:59], a[208:211], a[160:163], v[196:199]\n\t"
    "v_exp_f32 v36, v36\n\t"
    "v_mfma_f32_16x16x32_bf16 v[56:59], a[212:215], a[164:167], v[56:59]\n\t"
    "ds_read_b128 a[200:203], v230 offset:16384\n\t"
    "v_exp_f32 v37, v37\n\t"
    "v_mfma_f32_16x16x32_bf16 v[56:59], a[216:219], a[168:171], v[56:59]\n\t"
    "v_exp_f32 v38, v38\n\t"
    "v_exp_f32 v39, v39\n\t"
    "v_mfma_f32_16x16x32_bf16 v[56:59], a[220:223], a[172:175], v[56:59]\n\t"
    "s_add_u32 s51, s51, s48\n\t"
    "v_exp_f32 v44, v44\n\t"
    "v_mfma_f32_16x16x32_bf16 v[60:63], a[208:211], a[176:179], v[200:203]\n\t"
    "v_exp_f32 v45, v45\n\t"
    "v_mfma_f32_16x16x32_bf16 v[60:63], a[212:215], a[180:183], v[60:63]\n\t"
    "ds_read_b128 a[204:207], v231 offset:16384\n\t"
    "v_exp_f32 v46, v46\n\t"
    "v_mfma_f32_16x16x32_bf16 v[60:63], a[216:219], a[184:187], v[60:63]\n\t"
    "v_exp_f32 v47, v47\n\t"
    "v_cvt_pk_bf16_f32 v84, v36, v37\n\t"
    "v_mfma_f32_16x16x32_bf16 v[60:63], a[220:223], a[188:191], v[60:63]\n\t"
    "v_cvt_pk_bf16_f32 v85, v38, v39\n\t"
    "v_cvt_pk_bf16_f32 v116, v44, v45\n\t"
    "v_cvt_pk_bf16_f32 v117, v46, v47\n\t"
    "s_waitcnt lgkmcnt(4)\n\t"
    "v_mfma_f32_16x16x32_bf16 a[48:51], a[240:243], v[128:131], a[48:51]\n\t"
    "v_cvt_pk_bf16_f32 v48, 0, v48\n\t"
    "v_cvt_pk_bf16_f32 v49, 0, v49\n\t"
    "v_mfma_f32_16x16x32_bf16 a[48:51], a[244:247], v[132:135], a[48:51]\n\t"
    "ds_read_b64_tr_b16 a[224:225], %[vb] offset:33792\n\t"
    "ds_read_b64_tr_b16 a[226:227], %[vb] offset:37888\n\t"
    "v_cvt_pk_bf16_f32 v50, 0, v50\n\t"
    "v_mfma_f32_16x16x32_bf16 a[48:51], a[248:251], v[136:139], a[48:51]\n\t"
    "v_cvt_pk_bf16_f32 v51, 0, v51\n\t"
    "v_mul_f32 v48, s56, v48\n\t"
    "v_mfma_f32_16x16x32_bf16 a[48:51], a[252:255], v[140:143], a[48:51]\n\t"
    "v_mul_f32 v49, s56, v49\n\t"
    "v_mul_f32 v50, s56, v50\n\t"
    "v_mul_f32 v51, s56, v51\n\t"
    "v_mfma_f32_16x16x32_bf16 a[52:55], a[240:243], v[144:147], a[52:55]\n\t"
    "v_cvt_pk_bf16_f32 v48, 0, v48\n\t"
    "v_cvt_pk_bf16_f32 v49, 0, v49\n\t"
    "v_mfma_f32_16x16x32_bf16 a[52:55], a[244:247], v[148:151], a[52:55]\n\t"
    "ds_read_b64_tr_b16 a[228:229], %[vb] offset:41984\n\t"
    "ds_read_b64_tr_b16 a[230:231], %[vb] offset:46080\n\t"
    "v_cvt_pk_bf16_f32 v50, 0, v50\n\t"
    "v_mfma_f32_16x16x32_bf16 a[52:55], a[248:251], v[152:155], a[52:55]\n\t"
    "v_cvt_pk_bf16_f32 v51, 0, v51\n\t"
    "v_fma_f32 v48, v48, s58, v192\n\t"
    "v_mfma_f32_16x16x32_bf16 a[52:55], a[252:255], v[156:159], a[52:55]\n\t"
    "v_fma_f32 v49, v49, s58, v192\n\t"
    "v_fma_f32 v50, v50, s58, v192\n\t"
    "v_fma_f32 v51, v51, s58, v192\n\t"
    "v_mfma_f32_16x16x32_bf16 a[56:59], a[240:243], v[160:163], a[56:59]\n\t"
    "v_exp_f32 v48, v48\n\t"
    "v_mfma_f32_16x16x32_bf16 a[56:59], a[244:247], v[164:167], a[56:59]\n\t"
    "ds_read_b64_tr_b16 a[232:233], %[vb] offset:50176\n\t"
    "ds_read_b64_tr_b16 a[234:235], %[vb] offset:54272\n\t"
    "v_mfma_f32_16x16x32_bf16 a[56:59], a[248:251], v[168:171], a[56:59]\n\t"
    "v_exp_f32 v49, v49\n\t"
    "v_mfma_f32_16x16x32_bf16 a[56:59], a[252:255], v[172:175], a[56:59]\n\t"
    "v_exp_f32 v50, v50\n\t"
    "v_exp_f32 v51, v51\n\t"
    "v_mfma_f32_16x16x32_bf16 a[60:63], a[240:243], v[176:179], a[60:63]\n\t"
    "v_exp_f32 v56, v56\n\t"
    "v_mfma_f32_16x16x32_bf16 a[60:63], a[244:247], v[180:183], a[60:63]\n\t"
    "ds_read_b64_tr_b16 a[236:237], %[vb] offset:58368\n\t"
    "ds_read_b64_tr_b16 a[238:239], %[vb] offset:62464\n\t"
    "v_mfma_f32_16x16x32_bf16 a[60:63], a[248:251], v[184:187], a[60:63]\n\t"
    "v_exp_f32 v57, v57\n\t"
    "v_mfma_f32_16x16x32_bf16 a[60:63], a[252:255], v[188:191], a[60:63]\n\t"
    "v_exp_f32 v58, v58\n\t"
    "v_exp_f32 v59, v59\n\t"
    "v_mfma_f32_16x16x32_bf16 v[212:215], v[224:227], v[152:155], v[212:215]\n\t"
    "v_cvt_pk_bf16_f32 v70, v48, v49\n\t"
    "v_cvt_pk_bf16_f32 v71, v50, v51\n\t"
    "v_mfma_f32_16x16x32_bf16 v[212:215], v[224:227], v[156:159], v[212:215]\n\t"
    "v_cvt_pk_bf16_f32 v102, v56, v57\n\t"
    "v_cvt_pk_bf16_f32 v103, v58, v59\n\t"
    "s_waitcnt lgkmcnt(8)\n\t"
    "v_mfma_f32_16x16x32_bf16 v[32:35], a[192:195], a[128:131], 0\n\t"
    "v_cvt_pk_bf16_f32 v52, 0, v52\n\t"
    "v_cvt_pk_bf16_f32 v53, 0, v53\n\t"
    "v_cvt_pk_bf16_f32 v54, 0, v54\n\t"
    "v_mfma_f32_16x16x32_bf16 v[32:35], a[196:199], a[132:135], v[32:35]\n\t"
    "ds_read_b128 a[208:211], v228 offset:20480\n\t"
    "v_cvt_pk_bf16_f32 v55, 0, v55\n\t"
    "v_mfma_f32_16x16x32_bf16 v[32:35], a[200:203], a[136:139], v[32:35]\n\t"
    "v_mul_f32 v52, s56, v52\n\t"
    "v_mul_f32 v53, s56, v53\n\t"
    "v_mul_f32 v54, s56, v54\n\t"
    "v_mfma_f32_16x16x32_bf16 v[32:35], a[204:207], a[140:143], v[32:35]\n\t"
    "v_mul_f32 v55, s56, v55\n\t"
    "v_cvt_pk_bf16_f32 v52, 0, v52\n\t"
    "v_mfma_f32_16x16x32_bf16 v[36:39], a[192:195], a[144:147], 0\n\t"
    "v_cvt_pk_bf16_f32 v53, 0, v53\n\t"
    "v_cvt_pk_bf16_f32 v54, 0, v54\n\t"
    "v_cvt_pk_bf16_f32 v55, 0, v55\n\t"
    "v_mfma_f32_16x16x32_bf16 v[36:39], a[196:199], a[148:151], v[36:39]\n\t"
    "ds_read_b128 a[212:215], v229 offset:20480\n\t"
    "v_fma_f32 v52, v52, s58, v193\n\t"
    "v_mfma_f32_16x16x32_bf16 v[36:39], a[200:203], a[152:155], v[36:39]\n\t"
    "v_fma_f32 v53, v53, s58, v193\n\t"
    "v_fma_f32 v54, v54, s58, v193\n\t"
    "v_fma_f32 v55, v55, s58, v193\n\t"
    "v_mfma_f32_16x16x32_bf16 v[36:39], a[204:207], a[156:159], v[36:39]\n\t"
    "v_exp_f32 v52, v52\n\t"
    "v_mfma_f32_16x16x32_bf16 v[40:43], a[192:195], a[160:163], v[196:199]\n\t"
    "v_exp_f32 v53, v53\n\t"
    "v_mfma_f32_16x16x32_bf16 v[40:43], a[196:199], a[164:167], v[40:43]\n\t"
    "ds_read_b128 a[216:219], v230 offset:20480\n\t"
    "v_exp_f32 v54, v54\n\t"
    "v_mfma_f32_16x16x32_bf16 v[40:43], a[200:203], a[168:171], v[40:43]\n\t"
    "v_exp_f32 v55, v55\n\t"
    "v_mfma_f32_16x16x32_bf16 v[40:43], a[204:207], a[172:175], v[40:43]\n\t"
    "v_exp_f32 v60, v60\n\t"
    "v_exp_f32 v61, v61\n\t"
    "v_mfma_f32_16x16x32_bf16 v[44:47], a[192:195], a[176:179], v[200:203]\n\t"
    "v_exp_f32 v62, v62\n\t"
    "v_mfma_f32_16x16x32_bf16 v[44:47], a[196:199], a[180:183], v[44:47]\n\t"
    "ds_read_b128 a[220:223], v231 offset:20480\n\t"
    "v_exp_f32 v63, v63\n\t"
    "v_mfma_f32_16x16x32_bf16 v[44:47], a[200:203], a[184:187], v[44:47]\n\t"
    "v_cvt_pk_bf16_f32 v86, v52, v53\n\t"
    "v_cvt_pk_bf16_f32 v87, v54, v55\n\t"
    "v_mfma_f32_16x16x32_bf16 v[44:47], a[204:207], a[188:191], v[44:47]\n\t"
    "v_cvt_pk_bf16_f32 v118, v60, v61\n\t"
    "v_cvt_pk_bf16_f32 v119, v62, v63\n\t"
    "s_waitcnt lgkmcnt(4)\n\t"
    "v_mfma_f32_16x16x32_bf16 a[64:67], a[224:227], v[128:131], a[64:67]\n\t"
    "v_cvt_pk_bf16_f32 v32, 0, v32\n\t"
    "v_cvt_pk_bf16_f32 v33, 0, v33\n\t"
    "v_mfma_f32_16x16x32_bf16 a[64:67], a[228:231], v[132:135], a[64:67]\n\t"
    "ds_read_b64_tr_b16 a[240:241], %[vb] offset:34048\n\t"
    "ds_read_b64_tr_b16 a[242:243], %[vb] offset:38144\n\t"
    "v_cvt_pk_bf16_f32 v34, 0, v34\n\t"
    "v_mfma_f32_16x16x32_bf16 a[64:67], a[232:235], v[136:139], a[64:67]\n\t"
    "v_cvt_pk_bf16_f32 v35, 0, v35\n\t"
    "v_mul_f32 v32, s56, v32\n\t"
    "v_mfma_f32_16x16x32_bf16 a[64:67], a[236:239], v[140:143], a[64:67]\n\t"
    "v_mul_f32 v33, s56, v33\n\t"
    "v_mul_f32 v34, s56, v34\n\t"
    "v_mul_f32 v35, s56, v35\n\t"
    "v_mfma_f32_16x16x32_bf16 a[68:71], a[224:227], v[144:147], a[68:71]\n\t"
    "v_cvt_pk_bf16_f32 v32, 0, v32\n\t"
    "v_cvt_pk_bf16_f32 v33, 0, v33\n\t"
    "v_mfma_f32_16x16x32_bf16 a[68:71], a[228:231], v[148:151], a[68:71]\n\t"
    "ds_read_b64_tr_b16 a[244:245], %[vb] offset:42240\n\t"
    "ds_read_b64_tr_b16 a[246:247], %[vb] offset:46336\n\t"
    "v_cvt_pk_bf16_f32 v34, 0, v34\n\t"
    "v_mfma_f32_16x16x32_bf16 a[68:71], a[232:235], v[152:155], a[68:71]\n\t"
    "v_cvt_pk_bf16_f32 v35, 0, v35\n\t"
    "v_fma_f32 v32, v32, s58, v192\n\t"
    "v_mfma_f32_16x16x32_bf16 a[68:71], a[236:239], v[156:159], a[68:71]\n\t"
    "v_fma_f32 v33, v33, s58, v192\n\t"
    "v_fma_f32 v34, v34, s58, v192\n\t"
    "v_fma_f32 v35, v35, s58, v192\n\t"
    "v_mfma_f32_16x16x32_bf16 a[72:75], a[224:227], v[160:163], a[72:75]\n\t"
    "v_exp_f32 v32, v32\n\t"
    "v_mfma_f32_16x16x32_bf16 a[72:75], a[228:231], v[164:167], a[72:75]\n\t"
    "ds_read_b64_tr_b16 a[248:249], %[vb] offset:50432\n\t"
    "ds_read_b64_tr_b16 a[250:251], %[vb] offset:54528\n\t"
    "v_mfma_f32_16x16x32_bf16 a[72:75], a[232:235], v[168:171], a[72:75]\n\t"
    "v_exp_f32 v33, v33\n\t"
    "v_mfma_f32_16x16x32_bf16 a[72:75], a[236:239], v[172:175], a[72:75]\n\t"
    "v_exp_f32 v34, v34\n\t"
    "v_exp_f32 v35, v35\n\t"
    "v_mfma_f32_16x16x32_bf16 a[76:79], a[224:227], v[176:179], a[76:79]\n\t"
    "v_exp_f32 v40, v40\n\t"
    "v_mfma_f32_16x16x32_bf16 a[76:79], a[228:231], v[180:183], a[76:79]\n\t"
    "ds_read_b64_tr_b16 a[252:253], %[vb] offset:58624\n\t"
    "ds_read_b64_tr_b16 a[254:255], %[vb] offset:62720\n\t"
    "v_mfma_f32_16x16x32_bf16 a[76:79], a[232:235], v[184:187], a[76:79]\n\t"
    "v_exp_f32 v41, v41\n\t"
    "v_mfma_f32_16x16x32_bf16 a[76:79], a[236:239], v[188:191], a[76:79]\n\t"
    "v_exp_f32 v42, v42\n\t"
    "v_exp_f32 v43, v43\n\t"
    "v_mfma_f32_16x16x32_bf16 v[216:219], v[224:227], v[160:163], v[216:219]\n\t"
    "v_cvt_pk_bf16_f32 v72, v32, v33\n\t"
    "v_cvt_pk_bf16_f32 v73, v34, v35\n\t"
    "v_mfma_f32_16x16x32_bf16 v[216:219], v[224:227], v[164:167], v[216:219]\n\t"
    "v_cvt_pk_bf16_f32 v104, v40, v41\n\t"
    "v_cvt_pk_bf16_f32 v105, v42, v43\n\t"
    "s_waitcnt lgkmcnt(8)\n\t"
    "v_mfma_f32_16x16x32_bf16 v[48:51], a[208:211], a[128:131], 0\n\t"
    "v_cvt_pk_bf16_f32 v36, 0, v36\n\t"
    "v_cvt_pk_bf16_f32 v37, 0, v37\n\t"
    "v_cvt_pk_bf16_f32 v38, 0, v38\n\t"
    "v_mfma_f32_16x16x32_bf16 v[48:51], a[212:215], a[132:135], v[48:51]\n\t"
    "ds_read_b128 a[192:195], v228 offset:24576\n\t"
    "v_cvt_pk_bf16_f32 v39, 0, v39\n\t"
    "v_mfma_f32_16x16x32_bf16 v[48:51], a[216:219], a[136:139], v[48:51]\n\t"
    "v_mul_f32 v36, s56, v36\n\t"
    "v_mul_f32 v37, s56, v37\n\t"
    "v_mul_f32 v38, s56, v38\n\t"
    "v_mfma_f32_16x16x32_bf16 v[48:51], a[220:223], a[140:143], v[48:51]\n\t"
    "v_mul_f32 v39, s56, v39\n\t"
    "v_cvt_pk_bf16_f32 v36, 0, v36\n\t"
    "v_mfma_f32_16x16x32_bf16 v[52:55], a[208:211], a[144:147], 0\n\t"
    "v_cvt_pk_bf16_f32 v37, 0, v37\n\t"
    "v_cvt_pk_bf16_f32 v38, 0, v38\n\t"
    "v_cvt_pk_bf16_f32 v39, 0, v39\n\t"
    "v_mfma_f32_16x16x32_bf16 v[52:55], a[212:215], a[148:151], v[52:55]\n\t"
    "ds_read_b128 a[196:199], v229 offset:24576\n\t"
    "v_fma_f32 v36, v36, s58, v193\n\t"
    "v_mfma_f32_16x16x32_bf16 v[52:55], a[216:219], a[152:155], v[52:55]\n\t"
    "v_fma_f32 v37, v37, s58, v193\n\t"
    "v_fma_f32 v38, v38, s58, v193\n\t"
    "v_fma_f32 v39, v39, s58, v193\n\t"
    "v_mfma_f32_16x16x32_bf16 v[52:55], a[220:223], a[156:159], v[52:55]\n\t"
    "v_exp_f32 v36, v36\n\t"
    "v_mfma_f32_16x16x32_bf16 v[56:59], a[208:211], a[160:163], v[196:199]\n\t"
    "v_exp_f32 v37, v37\n\t"
    "v_mfma_f32_16x16x32_bf16 v[56:59], a[212:215], a[164:167], v[56:59]\n\t"
    "ds_read_b128 a[200:203], v230 offset:24576\n\t"
    "v_exp_f32 v38, v38\n\t"
    "v_mfma_f32_16x16x32_bf16 v[56:59], a[216:219], a[168:171], v[56:59]\n\t"
    "v_exp_f32 v39, v39\n\t"
    "v_mfma_f32_16x16x32_bf16 v[56:59], a[220:223], a[172:175], v[56:59]\n\t"
    "v_exp_f32 v44, v44\n\t"
    "v_exp_f32 v45, v45\n\t"
    "v_mfma_f32_16x16x32_bf16 v[60:63], a[208:211], a[176:179], v[200:203]\n\t"
    "v_exp_f32 v46, v46\n\t"
    "v_mfma_f32_16x16x32_bf16 v[60:63], a[212:215], a[180:183], v[60:63]\n\t"
    "ds_read_b128 a[204:207], v231 offset:24576\n\t"
    "v_exp_f32 v47, v47\n\t"
    "v_mfma_f32_16x16x32_bf16 v[60:63], a[216:219], a[184:187], v[60:63]\n\t"
    "v_cvt_pk_bf16_f32 v88, v36, v37\n\t"
    "v_cvt_pk_bf16_f32 v89, v38, v39\n\t"
    "v_mfma_f32_16x16x32_bf16 v[60:63], a[220:223], a[188:191], v[60:63]\n\t"
    "v_cvt_pk_bf16_f32 v120, v44, v45\n\t"
    "v_cvt_pk_bf16_f32 v121, v46, v47\n\t"
    "s_waitcnt lgkmcnt(4)\n\t"
    "v_mfma_f32_16x16x32_bf16 a[80:83], a[240:243], v[128:131], a[80:83]\n\t"
    "v_cvt_pk_bf16_f32 v48, 0, v48\n\t"
    "v_cvt_pk_bf16_f32 v49, 0, v49\n\t"
    "v_mfma_f32_16x16x32_bf16 a[80:83], a[244:247], v[132:135], a[80:83]\n\t"
    "ds_read_b64_tr_b16 a[224:225], %[vb] offset:34304\n\t"
    "ds_read_b64_tr_b16 a[226:227], %[vb] offset:38400\n\t"
    "v_cvt_pk_bf16_f32 v50, 0, v50\n\t"
    "v_mfma_f32_16x16x32_bf16 a[80:83], a[248:251], v[136:139], a[80:83]\n\t"
    "v_cvt_pk_bf16_f32 v51, 0, v51\n\t"
    "v_mul_f32 v48, s56, v48\n\t"
    "v_mfma_f32_16x16x32_bf16 a[80:83], a[252:255], v[140:143], a[80:83]\n\t"
    "v_mul_f32 v49, s56, v49\n\t"
    "v_mul_f32 v50, s56, v50\n\t"
    "v_mul_f32 v51, s56, v51\n\t"
    "v_mfma_f32_16x16x32_bf16 a[84:87], a[240:243], v[144:147], a[84:87]\n\t"
    "v_cvt_pk_bf16_f32 v48, 0, v48\n\t"
    "v_cvt_pk_bf16_f32 v49, 0, v49\n\t"
    "v_mfma_f32_16x16x32_bf16 a[84:87], a[244:247], v[148:151], a[84:87]\n\t"
    "ds_read_b64_tr_b16 a[228:229], %[vb] offset:42496\n\t"
    "ds_read_b64_tr_b16 a[230:231], %[vb] offset:46592\n\t"
    "v_cvt_pk_bf16_f32 v50, 0, v50\n\t"
    "v_mfma_f32_16x16x32_bf16 a[84:87], a[248:251], v[152:155], a[84:87]\n\t"
    "v_cvt_pk_bf16_f32 v51, 0, v51\n\t"
    "v_fma_f32 v48, v48, s58, v192\n\t"
    "v_mfma_f32_16x16x32_bf16 a[84:87], a[252:255], v[156:159], a[84:87]\n\t"
    "v_fma_f32 v49, v49, s58, v192\n\t"
    "v_fma_f32 v50, v50, s58, v192\n\t"
    "v_fma_f32 v51, v51, s58, v192\n\t"
    "v_mfma_f32_16x16x32_bf16 a[88:91], a[240:243], v[160:163], a[88:91]\n\t"
    "v_exp_f32 v48, v48\n\t"
    "v_mfma_f32_16x16x32_bf16 a[88:91], a[244:247], v[164:167], a[88:91]\n\t"
    "ds_read_b64_tr_b16 a[232:233], %[vb] offset:50688\n\t"
    "ds_read_b64_tr_b16 a[234:235], %[vb] offset:54784\n\t"
    "v_mfma_f32_16x16x32_bf16 a[88:91], a[248:251], v[168:171], a[88:91]\n\t"
    "v_exp_f32 v49, v49\n\t"
    "v_mfma_f32_16x16x32_bf16 a[88:91], a[252:255], v[172:175], a[88:91]\n\t"
    "v_exp_f32 v50, v50\n\t"
    "v_exp_f32 v51, v51\n\t"
    "v_mfma_f32_16x16x32_bf16 a[92:95], a[240:243], v[176:179], a[92:95]\n\t"
    "v_exp_f32 v56, v56\n\t"
    "v_mfma_f32_16x16x32_bf16 a[92:95], a[244:247], v[180:183], a[92:95]\n\t"
    "ds_read_b64_tr_b16 a[236:237], %[vb] offset:58880\n\t"
    "ds_read_b64_tr_b16 a[238:239], %[vb] offset:62976\n\t"
    "v_mfma_f32_16x16x32_bf16 a[92:95], a[248:251], v[184:187], a[92:95]\n\t"
    "v_exp_f32 v57, v57\n\t"
    "v_mfma_f32_16x16x32_bf16 a[92:95], a[252:255], v[188:191], a[92:95]\n\t"
    "v_exp_f32 v58, v58\n\t"
    "v_exp_f32 v59, v59\n\t"
    "v_mfma_f32_16x16x32_bf16 v[216:219], v[224:227], v[168:171], v[216:219]\n\t"
    "v_cvt_pk_bf16_f32 v74, v48, v49\n\t"
    "v_cvt_pk_bf16_f32 v75, v50, v51\n\t"
    "v_mfma_f32_16x16x32_bf16 v[216:219], v[224:227], v[172:175], v[216:219]\n\t"
    "v_cvt_pk_bf16_f32 v106, v56, v57\n\t"
    "v_cvt_pk_bf16_f32 v107, v58, v59\n\t"
    "s_waitcnt lgkmcnt(8)\n\t"
    "v_mfma_f32_16x16x32_bf16 v[32:35], a[192:195], a[128:131], 0\n\t"
    "v_cvt_pk_bf16_f32 v52, 0, v52\n\t"
    "v_cvt_pk_bf16_f32 v53, 0, v53\n\t"
    "v_cvt_pk_bf16_f32 v54, 0, v54\n\t"
    "v_mfma_f32_16x16x32_bf16 v[32:35], a[196:199], a[132:135], v[32:35]\n\t"
    "ds_read_b128 a[208:211], v228 offset:28672\n\t"
    "v_cvt_pk_bf16_f32 v55, 0, v55\n\t"
    "v_mfma_f32_16x16x32_bf16 v[32:35], a[200:203], a[136:139], v[32:35]\n\t"
    "v_mul_f32 v52, s56, v52\n\t"
    "v_mul_f32 v53, s56, v53\n\t"
    "v_mul_f32 v54, s56, v54\n\t"
    "v_mfma_f32_16x16x32_bf16 v[32:35], a[204:207], a[140:143], v[32:35]\n\t"
    "v_mul_f32 v55, s56, v55\n\t"
    "v_cvt_pk_bf16_f32 v52, 0, v52\n\t"
    "v_mfma_f32_16x16x32_bf16 v[36:39], a[192:195], a[144:147], 0\n\t"
    "v_cvt_pk_bf16_f32 v53, 0, v53\n\t"
    "v_cvt_pk_bf16_f32 v54, 0, v54\n\t"
    "v_cvt_pk_bf16_f32 v55, 0, v55\n\t"
    "v_mfma_f32_16x16x32_bf16 v[36:39], a[196:199], a[148:151], v[36:39]\n\t"
    "ds_read_b128 a[212:215], v229 offset:28672\n\t"
    "v_fma_f32 v52, v52, s58, v193\n\t"
    "v_mfma_f32_16x16x32_bf16 v[36:39], a[200:203], a[152:155], v[36:39]\n\t"
    "v_fma_f32 v53, v53, s58, v193\n\t"
    "v_fma_f32 v54, v54, s58, v193\n\t"
    "v_fma_f32 v55, v55, s58, v193\n\t"
    "v_mfma_f32_16x16x32_bf16 v[36:39], a[204:207], a[156:159], v[36:39]\n\t"
    "v_exp_f32 v52, v52\n\t"
    "v_mfma_f32_16x16x32_bf16 v[40:43], a[192:195], a[160:163], v[196:199]\n\t"
    "v_exp_f32 v53, v53\n\t"
    "v_mfma_f32_16x16x32_bf16 v[40:43], a[196:199], a[164:167], v[40:43]\n\t"
    "ds_read_b128 a[216:219], v230 offset:28672\n\t"
    "v_exp_f32 v54, v54\n\t"
    "v_mfma_f32_16x16x32_bf16 v[40:43], a[200:203], a[168:171], v[40:43]\n\t"
    "v_exp_f32 v55, v55\n\t"
    "v_mfma_f32_16x16x32_bf16 v[40:43], a[204:207], a[172:175], v[40:43]\n\t"
    "v_exp_f32 v60, v60\n\t"
    "v_exp_f32 v61, v61\n\t"
    "v_mfma_f32_16x16x32_bf16 v[44:47], a[192:195], a[176:179], v[200:203]\n\t"
    "v_exp_f32 v62, v62\n\t"
    "v_mfma_f32_16x16x32_bf16 v[44:47], a[196:199], a[180:183], v[44:47]\n\t"
    "ds_read_b128 a[220:223], v231 offset:28672\n\t"
    "v_exp_f32 v63, v63\n\t"
    "v_mfma_f32_16x16x32_bf16 v[44:47], a[200:203], a[184:187], v[44:47]\n\t"
    "v_cvt_pk_bf16_f32 v90, v52, v53\n\t"
    "v_cvt_pk_bf16_f32 v91, v54, v55\n\t"
    "v_mfma_f32_16x16x32_bf16 v[44:47], a[204:207], a[188:191], v[44:47]\n\t"
    "v_cvt_pk_bf16_f32 v122, v60, v61\n\t"
    "v_cvt_pk_bf16_f32 v123, v62, v63\n\t"
    "s_waitcnt lgkmcnt(4)\n\t"
    "v_mfma_f32_16x16x32_bf16 a[96:99], a[224:227], v[128:131], a[96:99]\n\t"
    "v_cvt_pk_bf16_f32 v32, 0, v32\n\t"
    "v_cvt_pk_bf16_f32 v33, 0, v33\n\t"
    "v_mfma_f32_16x16x32_bf16 a[96:99], a[228:231], v[132:135], a[96:99]\n\t"
    "ds_read_b64_tr_b16 a[240:241], %[vb] offset:34560\n\t"
    "ds_read_b64_tr_b16 a[242:243], %[vb] offset:38656\n\t"
    "v_cvt_pk_bf16_f32 v34, 0, v34\n\t"
    "v_mfma_f32_16x16x32_bf16 a[96:99], a[232:235], v[136:139], a[96:99]\n\t"
    "v_cvt_pk_bf16_f32 v35, 0, v35\n\t"
    "v_mul_f32 v32, s56, v32\n\t"
    "v_mfma_f32_16x16x32_bf16 a[96:99], a[236:239], v[140:143], a[96:99]\n\t"
    "v_mul_f32 v33, s56, v33\n\t"
    "v_mul_f32 v34, s56, v34\n\t"
    "v_mul_f32 v35, s56, v35\n\t"
    "v_mfma_f32_16x16x32_bf16 a[100:103], a[224:227], v[144:147], a[100:103]\n\t"
    "v_cvt_pk_bf16_f32 v32, 0, v32\n\t"
    "v_cvt_pk_bf16_f32 v33, 0, v33\n\t"
    "v_mfma_f32_16x16x32_bf16 a[100:103], a[228:231], v[148:151], a[100:103]\n\t"
    "ds_read_b64_tr_b16 a[244:245], %[vb] offset:42752\n\t"
    "ds_read_b64_tr_b16 a[246:247], %[vb] offset:46848\n\t"
    "v_cvt_pk_bf16_f32 v34, 0, v34\n\t"
    "v_mfma_f32_16x16x32_bf16 a[100:103], a[232:235], v[152:155], a[100:103]\n\t"
    "v_cvt_pk_bf16_f32 v35, 0, v35\n\t"
    "v_fma_f32 v32, v32, s58, v192\n\t"
    "v_mfma_f32_16x16x32_bf16 a[100:103], a[236:239], v[156:159], a[100:103]\n\t"
    "v_fma_f32 v33, v33, s58, v192\n\t"
    "v_fma_f32 v34, v34, s58, v192\n\t"
    "v_fma_f32 v35, v35, s58, v192\n\t"
    "v_mfma_f32_16x16x32_bf16 a[104:107], a[224:227], v[160:163], a[104:107]\n\t"
    "v_exp_f32 v32, v32\n\t"
    "v_mfma_f32_16x16x32_bf16 a[104:107], a[228:231], v[164:167], a[104:107]\n\t"
    "ds_read_b64_tr_b16 a[248:249], %[vb] offset:50944\n\t"
    "ds_read_b64_tr_b16 a[250:251], %[vb] offset:55040\n\t"
    "v_mfma_f32_16x16x32_bf16 a[104:107], a[232:235], v[168:171], a[104:107]\n\t"
    "v_exp_f32 v33, v33\n\t"
    "v_mfma_f32_16x16x32_bf16 a[104:107], a[236:239], v[172:175], a[104:107]\n\t"
    "v_exp_f32 v34, v34\n\t"
    "v_exp_f32 v35, v35\n\t"
    "v_mfma_f32_16x16x32_bf16 a[108:111], a[224:227], v[176:179], a[108:111]\n\t"
    "v_exp_f32 v40, v40\n\t"
    "v_mfma_f32_16x16x32_bf16 a[108:111], a[228:231], v[180:183], a[108:111]\n\t"
    "ds_read_b64_tr_b16 a[252:253], %[vb] offset:59136\n\t"
    "ds_read_b64_tr_b16 a[254:255], %[vb] offset:63232\n\t"
    "v_mfma_f32_16x16x32_bf16 a[108:111], a[232:235], v[184:187], a[108:111]\n\t"
    "v_exp_f32 v41, v41\n\t"
    "v_mfma_f32_16x16x32_bf16 a[108:111], a[236:239], v[188:191], a[108:111]\n\t"
    "v_exp_f32 v42, v42\n\t"
    "v_exp_f32 v43, v43\n\t"
    "v_mfma_f32_16x16x32_bf16 v[220:223], v[224:227], v[176:179], v[220:223]\n\t"
    "v_cvt_pk_bf16_f32 v76, v32, v33\n\t"
    "v_cvt_pk_bf16_f32 v77, v34, v35\n\t"
    "v_mfma_f32_16x16x32_bf16 v[220:223], v[224:227], v[180:183], v[220:223]\n\t"
    "v_cvt_pk_bf16_f32 v108, v40, v41\n\t"
    "v_cvt_pk_bf16_f32 v109, v42, v43\n\t"
    "s_waitcnt vmcnt(0) lgkmcnt(0)\n\t"
    "s_barrier\n\t"
    "v_mfma_f32_16x16x32_bf16 v[48:51], a[208:211], a[128:131], 0\n\t"
    "v_cvt_pk_bf16_f32 v36, 0, v36\n\t"
    "v_cvt_pk_bf16_f32 v37, 0, v37\n\t"
    "v_cvt_pk_bf16_f32 v38, 0, v38\n\t"
    "v_mfma_f32_16x16x32_bf16 v[48:51], a[212:215], a[132:135], v[48:51]\n\t"
    "ds_read_b128 a[192:195], v228 offset:32768\n\t"
    "v_cvt_pk_bf16_f32 v39, 0, v39\n\t"
    "v_mul_f32 v36, s56, v36\n\t"
    "v_mfma_f32_16x16x32_bf16 v[48:51], a[216:219], a[136:139], v[48:51]\n\t"
    "v_mul_f32 v37, s56, v37\n\t"
    "v_mul_f32 v38, s56, v38\n\t"
    "v_mul_f32 v39, s56, v39\n\t"
    "v_mfma_f32_16x16x32_bf16 v[48:51], a[220:223], a[140:143], v[48:51]\n\t"
    "s_xor_b32 s42, s42, 0x8000\n\t"
    "s_add_u32 m0, s42, 0\n\t"
    "s_add_u32 s45, s40, 0\n\t"
    "buffer_load_dwordx4 %[ksrc0], %[srdK], s45 offen offset:0 lds\n\t"
    "v_mfma_f32_16x16x32_bf16 v[52:55], a[208:211], a[144:147], 0\n\t"
    "v_cvt_pk_bf16_f32 v36, 0, v36\n\t"
    "v_mfma_f32_16x16x32_bf16 v[52:55], a[212:215], a[148:151], v[52:55]\n\t"
    "ds_read_b128 a[196:199], v229 offset:32768\n\t"
    "v_cvt_pk_bf16_f32 v37, 0, v37\n\t"
    "v_cvt_pk_bf16_f32 v38, 0, v38\n\t"
    "v_mfma_f32_16x16x32_bf16 v[52:55], a[216:219], a[152:155], v[52:55]\n\t"
    "v_cvt_pk_bf16_f32 v39, 0, v39\n\t"
    "v_fma_f32 v36, v36, s58, v193\n\t"
    "v_fma_f32 v37, v37, s58, v193\n\t"
    "v_mfma_f32_16x16x32_bf16 v[52:55], a[220:223], a[156:159], v[52:55]\n\t"
    "v_fma_f32 v38, v38, s58, v193\n\t"
    "v_fma_f32 v39, v39, s58, v193\n\t"
    "v_mfma_f32_16x16x32_bf16 v[56:59], a[208:211], a[160:163], v[196:199]\n\t"
    "v_exp_f32 v36, v36\n\t"
    "v_exp_f32 v37, v37\n\t"
    "v_mfma_f32_16x16x32_bf16 v[56:59], a[212:215], a[164:167], v[56:59]\n\t"
    "ds_read_b128 a[200:203], v230 offset:32768\n\t"
    "v_exp_f32 v38, v38\n\t"
    "v_mfma_f32_16x16x32_bf16 v[56:59], a[216:219], a[168:171], v[56:59]\n\t"
    "v_exp_f32 v39, v39\n\t"
    "v_mfma_f32_16x16x32_bf16 v[56:59], a[220:223], a[172:175], v[56:59]\n\t"
    "buffer_load_dwordx4 v242, %[srdK], s45 offen offset:1024 lds\n\t"
    "v_exp_f32 v44, v44\n\t"
    "v_mfma_f32_16x16x32_bf16 v[60:63], a[208:211], a[176:179], v[200:203]\n\t"
    "v_exp_f32 v45, v45\n\t"
    "v_mfma_f32_16x16x32_bf16 v[60:63], a[212:215], a[180:183], v[60:63]\n\t"
    "ds_read_b128 a[204:207], v231 offset:32768\n\t"
    "v_exp_f32 v46, v46\n\t"
    "v_mfma_f32_16x16x32_bf16 v[60:63], a[216:219], a[184:187], v[60:63]\n\t"
    "v_exp_f32 v47, v47\n\t"
    "v_cvt_pk_bf16_f32 v92, v36, v37\n\t"
    "v_mfma_f32_16x16x32_bf16 v[60:63], a[220:223], a[188:191], v[60:63]\n\t"
    "v_cvt_pk_bf16_f32 v93, v38, v39\n\t"
    "v_cvt_pk_bf16_f32 v124, v44, v45\n\t"
    "v_cvt_pk_bf16_f32 v125, v46, v47\n\t"
    "s_waitcnt lgkmcnt(4)\n\t"
    "v_mfma_f32_16x16x32_bf16 a[112:115], a[240:243], v[128:131], a[112:115]\n\t"
    "v_cvt_pk_bf16_f32 v48, 0, v48\n\t"
    "v_cvt_pk_bf16_f32 v49, 0, v49\n\t"
    "v_cvt_pk_bf16_f32 v50, 0, v50\n\t"
    "v_mfma_f32_16x16x32_bf16 a[112:115], a[244:247], v[132:135], a[112:115]\n\t"
    "ds_read_b64_tr_b16 a[224:225], %[vb] offset:0\n\t"
    "ds_read_b64_tr_b16 a[226:227], %[vb] offset:4096\n\t"
    "v_mfma_f32_16x16x32_bf16 a[112:115], a[248:251], v[136:139], a[112:115]\n\t"
    "v_cvt_pk_bf16_f32 v51, 0, v51\n\t"
    "v_mul_f32 v48, s56, v48\n\t"
    "v_mul_f32 v49, s56, v49\n\t"
    "v_mfma_f32_16x16x32_bf16 a[112:115], a[252:255], v[140:143], a[112:115]\n\t"
    "buffer_load_dwordx4 v243, %[srdK], s45 offen offset:2048 lds\n\t"
    "v_mul_f32 v50, s56, v50\n\t"
    "v_mfma_f32_16x16x32_bf16 a[116:119], a[240:243], v[144:147], a[116:119]\n\t"
    "v_mul_f32 v51, s56, v51\n\t"
    "v_cvt_pk_bf16_f32 v48, 0, v48\n\t"
    "v_mfma_f32_16x16x32_bf16 a[116:119], a[244:247], v[148:151], a[116:119]\n\t"
    "ds_read_b64_tr_b16 a[228:229], %[vb] offset:8192\n\t"
    "ds_read_b64_tr_b16 a[230:231], %[vb] offset:12288\n\t"
    "v_cvt_pk_bf16_f32 v49, 0, v49\n\t"
    "v_mfma_f32_16x16x32_bf16 a[116:119], a[248:251], v[152:155], a[116:119]\n\t"
    "v_cvt_pk_bf16_f32 v50, 0, v50\n\t"
    "v_cvt_pk_bf16_f32 v51, 0, v51\n\t"
    "v_fma_f32 v48, v48, s58, v192\n\t"
    "v_mfma_f32_16x16x32_bf16 a[116:119], a[252:255], v[156:159], a[116:119]\n\t"
    "v_fma_f32 v49, v49, s58, v192\n\t"
    "v_fma_f32 v50, v50, s58, v192\n\t"
    "v_mfma_f32_16x16x32_bf16 a[120:123], a[240:243], v[160:163], a[120:123]\n\t"
    "v_fma_f32 v51, v51, s58, v192\n\t"
    "v_exp_f32 v48, v48\n\t"
    "v_mfma_f32_16x16x32_bf16 a[120:123], a[244:247], v[164:167], a[120:123]\n\t"
    "ds_read_b64_tr_b16 a[232:233], %[vb] offset:16384\n\t"
    "ds_read_b64_tr_b16 a[234:235], %[vb] offset:20480\n\t"
    "v_mfma_f32_16x16x32_bf16 a[120:123], a[248:251], v[168:171], a[120:123]\n\t"
    "v_exp_f32 v49, v49\n\t"
    "v_exp_f32 v50, v50\n\t"
    "v_mfma_f32_16x16x32_bf16 a[120:123], a[252:255], v[172:175], a[120:123]\n\t"
    "buffer_load_dwordx4 v244, %[srdK], s45 offen offset:3072 lds\n\t"
    "v_mfma_f32_16x16x32_bf16 a[124:127], a[240:243], v[176:179], a[124:127]\n\t"
    "v_exp_f32 v51, v51\n\t"
    "v_mfma_f32_16x16x32_bf16 a[124:127], a[244:247], v[180:183], a[124:127]\n\t"
    "ds_read_b64_tr_b16 a[236:237], %[vb] offset:24576\n\t"
    "ds_read_b64_tr_b16 a[238:239], %[vb] offset:28672\n\t"
    "v_exp_f32 v56, v56\n\t"
    "v_mfma_f32_16x16x32_bf16 a[124:127], a[248:251], v[184:187], a[124:127]\n\t"
    "v_exp_f32 v57, v57\n\t"
    "v_mfma_f32_16x16x32_bf16 a[124:127], a[252:255], v[188:191], a[124:127]\n\t"
    "v_exp_f32 v58, v58\n\t"
    "v_mfma_f32_16x16x32_bf16 v[220:223], v[224:227], v[184:187], v[220:223]\n\t"
    "v_exp_f32 v59, v59\n\t"
    "v_cvt_pk_bf16_f32 v78, v48, v49\n\t"
    "v_mfma_f32_16x16x32_bf16 v[220:223], v[224:227], v[188:191], v[220:223]\n\t"
    "v_cvt_pk_bf16_f32 v79, v50, v51\n\t"
    "v_cvt_pk_bf16_f32 v110, v56, v57\n\t"
    "v_cvt_pk_bf16_f32 v111, v58, v59\n\t"
    "s_sub_u32 s44, s44, 1\n\t"
    "s_cmp_lg_u32 s44, 0\n\t"
    "s_cbranch_scc1 LOOP%=\n\t"
    "s_waitcnt lgkmcnt(8)\n\t"
    "v_mfma_f32_16x16x32_bf16 v[32:35], a[192:195], a[128:131], 0\n\t"
    "v_cvt_pk_bf16_f32 v52, 0, v52\n\t"
    "v_cvt_pk_bf16_f32 v53, 0, v53\n\t"
    "v_cvt_pk_bf16_f32 v54, 0, v54\n\t"
    "v_mfma_f32_16x16x32_bf16 v[32:35], a[196:199], a[132:135], v[32:35]\n\t"
    "ds_read_b128 a[208:211], v228 offset:36864\n\t"
    "v_cvt_pk_bf16_f32 v55, 0, v55\n\t"
    "v_mul_f32 v52, s56, v52\n\t"
    "v_mfma_f32_16x16x32_bf16 v[32:35], a[200:203], a[136:139], v[32:35]\n\t"
    "v_mul_f32 v53, s56, v53\n\t"
    "v_mul_f32 v54, s56, v54\n\t"
    "v_mul_f32 v55, s56, v55\n\t"
    "v_mfma_f32_16x16x32_bf16 v[32:35], a[204:207], a[140:143], v[32:35]\n\t"
    "s_add_u32 m0, s42, 4096\n\t"
    "s_add_u32 s45, s40, 4096\n\t"
    "buffer_load_dwordx4 %[ksrc0], %[srdK], s45 offen offset:0 lds\n\t"
    "v_mfma_f32_16x16x32_bf16 v[36:39], a[192:195], a[144:147], 0\n\t"
    "v_cvt_pk_bf16_f32 v52, 0, v52\n\t"
    "v_mfma_f32_16x16x32_bf16 v[36:39], a[196:199], a[148:151], v[36:39]\n\t"
    "ds_read_b128 a[212:215], v229 offset:36864\n\t"
    "v_cvt_pk_bf16_f32 v53, 0, v53\n\t"
    "v_cvt_pk_bf16_f32 v54, 0, v54\n\t"
    "v_mfma_f32_16x16x32_bf16 v[36:39], a[200:203], a[152:155], v[36:39]\n\t"
    "v_cvt_pk_bf16_f32 v55, 0, v55\n\t"
    "v_fma_f32 v52, v52, s58, v193\n\t"
    "v_fma_f32 v53, v53, s58, v193\n\t"
    "v_mfma_f32_16x16x32_bf16 v[36:39], a[204:207], a[156:159], v[36:39]\n\t"
    "v_fma_f32 v54, v54, s58, v193\n\t"
    "v_fma_f32 v55, v55, s58, v193\n\t"
    "v_exp_f32 v52, v52\n\t"
    "v_mfma_f32_16x16x32_bf16 v[40:43], a[192:195], a[160:163], v[196:199]\n\t"
    "v_exp_f32 v53, v53\n\t"
    "v_mfma_f32_16x16x32_bf16 v[40:43], a[196:199], a[164:167], v[40:43]\n\t"
    "ds_read_b128 a[216:219], v230 offset:36864\n\t"
    "v_exp_f32 v54, v54\n\t"
    "v_mfma_f32_16x16x32_bf16 v[40:43], a[200:203], a[168:171], v[40:43]\n\t"
    "v_exp_f32 v55, v55\n\t"
    "v_mfma_f32_16x16x32_bf16 v[40:43], a[204:207], a[172:175], v[40:43]\n\t"
    "buffer_load_dwordx4 v242, %[srdK], s45 offen offset:1024 lds\n\t"
    "v_exp_f32 v60, v60\n\t"
    "v_mfma_f32_16x16x32_bf16 v[44:47], a[192:195], a[176:179], v[200:203]\n\t"
    "v_exp_f32 v61, v61\n\t"
    "v_mfma_f32_16x16x32_bf16 v[44:47], a[196:199], a[180:183], v[44:47]\n\t"
    "ds_read_b128 a[220:223], v231 offset:36864\n\t"
    "v_exp_f32 v62, v62\n\t"
    "v_mfma_f32_16x16x32_bf16 v[44:47], a[200:203], a[184:187], v[44:47]\n\t"
    "v_exp_f32 v63, v63\n\t"
    "v_cvt_pk_bf16_f32 v94, v52, v53\n\t"
    "v_mfma_f32_16x16x32_bf16 v[44:47], a[204:207], a[188:191], v[44:47]\n\t"
    "v_cvt_pk_bf16_f32 v95, v54, v55\n\t"
    "v_cvt_pk_bf16_f32 v126, v60, v61\n\t"
    "v_cvt_pk_bf16_f32 v127, v62, v63\n\t"
    "s_waitcnt lgkmcnt(4)\n\t"
    "v_mfma_f32_16x16x32_bf16 a[0:3], a[224:227], v[64:67], a[0:3]\n\t"
    "v_cvt_pk_bf16_f32 v32, 0, v32\n\t"
    "v_cvt_pk_bf16_f32 v33, 0, v33\n\t"
    "v_cvt_pk_bf16_f32 v34, 0, v34\n\t"
    "v_mfma_f32_16x16x32_bf16 a[0:3], a[228:231], v[68:71], a[0:3]\n\t"
    "ds_read_b64_tr_b16 a[240:241], %[vb] offset:256\n\t"
    "ds_read_b64_tr_b16 a[242:243], %[vb] offset:4352\n\t"
    "v_mfma_f32_16x16x32_bf16 a[0:3], a[232:235], v[72:75], a[0:3]\n\t"
    "v_cvt_pk_bf16_f32 v35, 0, v35\n\t"
    "v_mul_f32 v32, s56, v32\n\t"
    "v_mul_f32 v33, s56, v33\n\t"
    "v_mfma_f32_16x16x32_bf16 a[0:3], a[236:239], v[76:79], a[0:3]\n\t"
    "buffer_load_dwordx4 v243, %[srdK], s45 offen offset:2048 lds\n\t"
    "v_mul_f32 v34, s56, v34\n\t"
    "v_mfma_f32_16x16x32_bf16 a[4:7], a[224:227], v[80:83], a[4:7]\n\t"
    "v_mul_f32 v35, s56, v35\n\t"
    "v_cvt_pk_bf16_f32 v32, 0, v32\n\t"
    "v_mfma_f32_16x16x32_bf16 a[4:7], a[228:231], v[84:87], a[4:7]\n\t"
    "ds_read_b64_tr_b16 a[244:245], %[vb] offset:8448\n\t"
    "ds_read_b64_tr_b16 a[246:247], %[vb] offset:12544\n\t"
    "v_cvt_pk_bf16_f32 v33, 0, v33\n\t"
    "v_mfma_f32_16x16x32_bf16 a[4:7], a[232:235], v[88:91], a[4:7]\n\t"
    "v_cvt_pk_bf16_f32 v34, 0, v34\n\t"
    "v_cvt_pk_bf16_f32 v35, 0, v35\n\t"
    "v_fma_f32 v32, v32, s58, v192\n\t"
    "v_mfma_f32_16x16x32_bf16 a[4:7], a[236:239], v[92:95], a[4:7]\n\t"
    "v_fma_f32 v33, v33, s58, v192\n\t"
    "v_fma_f32 v34, v34, s58, v192\n\t"
    "v_mfma_f32_16x16x32_bf16 a[8:11], a[224:227], v[96:99], a[8:11]\n\t"
    "v_fma_f32 v35, v35, s58, v192\n\t"
    "v_exp_f32 v32, v32\n\t"
    "v_mfma_f32_16x16x32_bf16 a[8:11], a[228:231], v[100:103], a[8:11]\n\t"
    "ds_read_b64_tr_b16 a[248:249], %[vb] offset:16640\n\t"
    "ds_read_b64_tr_b16 a[250:251], %[vb] offset:20736\n\t"
    "v_mfma_f32_16x16x32_bf16 a[8:11], a[232:235], v[104:107], a[8:11]\n\t"
    "v_exp_f32 v33, v33\n\t"
    "v_exp_f32 v34, v34\n\t"
    "v_mfma_f32_16x16x32_bf16 a[8:11], a[236:239], v[108:111], a[8:11]\n\t"
    "buffer_load_dwordx4 v244, %[srdK], s45 offen offset:3072 lds\n\t"
    "v_mfma_f32_16x16x32_bf16 a[12:15], a[224:227], v[112:115], a[12:15]\n\t"
    "v_exp_f32 v35, v35\n\t"
    "v_mfma_f32_16x16x32_bf16 a[12:15], a[228:231], v[116:119], a[12:15]\n\t"
    "ds_read_b64_tr_b16 a[252:253], %[vb] offset:24832\n\t"
    "ds_read_b64_tr_b16 a[254:255], %[vb] offset:28928\n\t"
    "v_exp_f32 v40, v40\n\t"
    "v_mfma_f32_16x16x32_bf16 a[12:15], a[232:235], v[120:123], a[12:15]\n\t"
    "v_exp_f32 v41, v41\n\t"
    "v_mfma_f32_16x16x32_bf16 a[12:15], a[236:239], v[124:127], a[12:15]\n\t"
    "v_exp_f32 v42, v42\n\t"
    "v_mfma_f32_16x16x32_bf16 v[208:211], v[224:227], v[64:67], v[208:211]\n\t"
    "v_exp_f32 v43, v43\n\t"
    "v_cvt_pk_bf16_f32 v128, v32, v33\n\t"
    "v_mfma_f32_16x16x32_bf16 v[208:211], v[224:227], v[68:71], v[208:211]\n\t"
    "v_cvt_pk_bf16_f32 v129, v34, v35\n\t"
    "v_cvt_pk_bf16_f32 v160, v40, v41\n\t"
    "v_cvt_pk_bf16_f32 v161, v42, v43\n\t"
    "s_waitcnt lgkmcnt(8)\n\t"
    "v_mfma_f32_16x16x32_bf16 v[48:51], a[208:211], a[128:131], 0\n\t"
    "v_cvt_pk_bf16_f32 v36, 0, v36\n\t"
    "v_cvt_pk_bf16_f32 v37, 0, v37\n\t"
    "v_cvt_pk_bf16_f32 v38, 0, v38\n\t"
    "v_mfma_f32_16x16x32_bf16 v[48:51], a[212:215], a[132:135], v[48:51]\n\t"
    "ds_read_b128 a[192:195], v228 offset:40960\n\t"
    "v_cvt_pk_bf16_f32 v39, 0, v39\n\t"
    "v_mul_f32 v36, s56, v36\n\t"
    "v_mfma_f32_16x16x32_bf16 v[48:51], a[216:219], a[136:139], v[48:51]\n\t"
    "v_mul_f32 v37, s56, v37\n\t"
    "v_mul_f32 v38, s56, v38\n\t"
    "v_mul_f32 v39, s56, v39\n\t"
    "v_mfma_f32_16x16x32_bf16 v[48:51], a[220:223], a[140:143], v[48:51]\n\t"
    "s_add_u32 s40, s40, 0x8000\n\t"
    "s_xor_b32 s43, s43, 0x8000\n\t"
    "s_add_u32 m0, s43, 0\n\t"
    "s_add_u32 s46, s41, 0\n\t"
    "buffer_load_dwordx4 %[vsrc0], %[srdV], s46 offen offset:0 lds\n\t"
    "v_mfma_f32_16x16x32_bf16 v[52:55], a[208:211], a[144:147], 0\n\t"
    "v_mfma_f32_16x16x32_bf16 v[52:55], a[212:215], a[148:151], v[52:55]\n\t"
    "ds_read_b128 a[196:199], v229 offset:40960\n\t"
    "v_cvt_pk_bf16_f32 v36, 0, v36\n\t"
    "v_cvt_pk_bf16_f32 v37, 0, v37\n\t"
    "v_mfma_f32_16x16x32_bf16 v[52:55], a[216:219], a[152:155], v[52:55]\n\t"
    "v_cvt_pk_bf16_f32 v38, 0, v38\n\t"
    "v_cvt_pk_bf16_f32 v39, 0, v39\n\t"
    "v_fma_f32 v36, v36, s58, v193\n\t"
    "v_mfma_f32_16x16x32_bf16 v[52:55], a[220:223], a[156:159], v[52:55]\n\t"
    "v_fma_f32 v37, v37, s58, v193\n\t"
    "v_fma_f32 v38, v38, s58, v193\n\t"
    "v_fma_f32 v39, v39, s58, v193\n\t"
    "v_mfma_f32_16x16x32_bf16 v[56:59], a[208:211], a[160:163], v[196:199]\n\t"
    "v_exp_f32 v36, v36\n\t"
    "v_exp_f32 v37, v37\n\t"
    "v_mfma_f32_16x16x32_bf16 v[56:59], a[212:215], a[164:167], v[56:59]\n\t"
    "ds_read_b128 a[200:203], v230 offset:40960\n\t"
    "v_exp_f32 v38, v38\n\t"
    "v_mfma_f32_16x16x32_bf16 v[56:59], a[216:219], a[168:171], v[56:59]\n\t"
    "v_exp_f32 v39, v39\n\t"
    "v_mfma_f32_16x16x32_bf16 v[56:59], a[220:223], a[172:175], v[56:59]\n\t"
    "buffer_load_dwordx4 v245, %[srdV], s46 offen offset:1024 lds\n\t"
    "v_exp_f32 v44, v44\n\t"
    "v_mfma_f32_16x16x32_bf16 v[60:63], a[208:211], a[176:179], v[200:203]\n\t"
    "v_exp_f32 v45, v45\n\t"
    "v_mfma_f32_16x16x32_bf16 v[60:63], a[212:215], a[180:183], v[60:63]\n\t"
    "ds_read_b128 a[204:207], v231 offset:40960\n\t"
    "v_exp_f32 v46, v46\n\t"
    "v_mfma_f32_16x16x32_bf16 v[60:63], a[216:219], a[184:187], v[60:63]\n\t"
    "v_exp_f32 v47, v47\n\t"
    "v_cvt_pk_bf16_f32 v144, v36, v37\n\t"
    "v_mfma_f32_16x16x32_bf16 v[60:63], a[220:223], a[188:191], v[60:63]\n\t"
    "v_cvt_pk_bf16_f32 v145, v38, v39\n\t"
    "v_cvt_pk_bf16_f32 v176, v44, v45\n\t"
    "v_cvt_pk_bf16_f32 v177, v46, v47\n\t"
    "s_waitcnt lgkmcnt(4)\n\t"
    "v_mfma_f32_16x16x32_bf16 a[16:19], a[240:243], v[64:67], a[16:19]\n\t"
    "v_cvt_pk_bf16_f32 v48, 0, v48\n\t"
    "v_cvt_pk_bf16_f32 v49, 0, v49\n\t"
    "v_cvt_pk_bf16_f32 v50, 0, v50\n\t"
    "v_mfma_f32_16x16x32_bf16 a[16:19], a[244:247], v[68:71], a[16:19]\n\t"
    "ds_read_b64_tr_b16 a[224:225], %[vb] offset:512\n\t"
    "ds_read_b64_tr_b16 a[226:227], %[vb] offset:4608\n\t"
    "v_mfma_f32_16x16x32_bf16 a[16:19], a[248:251], v[72:75], a[16:19]\n\t"
    "v_cvt_pk_bf16_f32 v51, 0, v51\n\t"
    "v_mul_f32 v48, s56, v48\n\t"
    "v_mul_f32 v49, s56, v49\n\t"
    "v_mfma_f32_16x16x32_bf16 a[16:19], a[252:255], v[76:79], a[16:19]\n\t"
    "buffer_load_dwordx4 %[vsrc0], %[srdV], s46 offen offset:2048 lds\n\t"
    "v_mul_f32 v50, s56, v50\n\t"
    "v_mfma_f32_16x16x32_bf16 a[20:23], a[240:243], v[80:83], a[20:23]\n\t"
    "v_mul_f32 v51, s56, v51\n\t"
    "v_cvt_pk_bf16_f32 v48, 0, v48\n\t"
    "v_mfma_f32_16x16x32_bf16 a[20:23], a[244:247], v[84:87], a[20:23]\n\t"
    "ds_read_b64_tr_b16 a[228:229], %[vb] offset:8704\n\t"
    "ds_read_b64_tr_b16 a[230:231], %[vb] offset:12800\n\t"
    "v_cvt_pk_bf16_f32 v49, 0, v49\n\t"
    "v_mfma_f32_16x16x32_bf16 a[20:23], a[248:251], v[88:91], a[20:23]\n\t"
    "v_cvt_pk_bf16_f32 v50, 0, v50\n\t"
    "v_cvt_pk_bf16_f32 v51, 0, v51\n\t"
    "v_fma_f32 v48, v48, s58, v192\n\t"
    "v_mfma_f32_16x16x32_bf16 a[20:23], a[252:255], v[92:95], a[20:23]\n\t"
    "v_fma_f32 v49, v49, s58, v192\n\t"
    "v_fma_f32 v50, v50, s58, v192\n\t"
    "v_mfma_f32_16x16x32_bf16 a[24:27], a[240:243], v[96:99], a[24:27]\n\t"
    "v_fma_f32 v51, v51, s58, v192\n\t"
    "v_exp_f32 v48, v48\n\t"
    "v_mfma_f32_16x16x32_bf16 a[24:27], a[244:247], v[100:103], a[24:27]\n\t"
    "ds_read_b64_tr_b16 a[232:233], %[vb] offset:16896\n\t"
    "ds_read_b64_tr_b16 a[234:235], %[vb] offset:20992\n\t"
    "v_mfma_f32_16x16x32_bf16 a[24:27], a[248:251], v[104:107], a[24:27]\n\t"
    "v_exp_f32 v49, v49\n\t"
    "v_exp_f32 v50, v50\n\t"
    "v_mfma_f32_16x16x32_bf16 a[24:27], a[252:255], v[108:111], a[24:27]\n\t"
    "buffer_load_dwordx4 v245, %[srdV], s46 offen offset:3072 lds\n\t"
    "v_mfma_f32_16x16x32_bf16 a[28:31], a[240:243], v[112:115], a[28:31]\n\t"
    "v_exp_f32 v51, v51\n\t"
    "v_mfma_f32_16x16x32_bf16 a[28:31], a[244:247], v[116:119], a[28:31]\n\t"
    "ds_read_b64_tr_b16 a[236:237], %[vb] offset:25088\n\t"
    "ds_read_b64_tr_b16 a[238:239], %[vb] offset:29184\n\t"
    "v_exp_f32 v56, v56\n\t"
    "v_mfma_f32_16x16x32_bf16 a[28:31], a[248:251], v[120:123], a[28:31]\n\t"
    "v_exp_f32 v57, v57\n\t"
    "v_mfma_f32_16x16x32_bf16 a[28:31], a[252:255], v[124:127], a[28:31]\n\t"
    "v_exp_f32 v58, v58\n\t"
    "v_mfma_f32_16x16x32_bf16 v[208:211], v[224:227], v[72:75], v[208:211]\n\t"
    "v_exp_f32 v59, v59\n\t"
    "v_cvt_pk_bf16_f32 v130, v48, v49\n\t"
    "v_mfma_f32_16x16x32_bf16 v[208:211], v[224:227], v[76:79], v[208:211]\n\t"
    "v_cvt_pk_bf16_f32 v131, v50, v51\n\t"
    "v_cvt_pk_bf16_f32 v162, v56, v57\n\t"
    "v_cvt_pk_bf16_f32 v163, v58, v59\n\t"
    "s_waitcnt lgkmcnt(8)\n\t"
    "v_mfma_f32_16x16x32_bf16 v[32:35], a[192:195], a[128:131], 0\n\t"
    "v_cvt_pk_bf16_f32 v52, 0, v52\n\t"
    "v_cvt_pk_bf16_f32 v53, 0, v53\n\t"
    "v_cvt_pk_bf16_f32 v54, 0, v54\n\t"
    "v_mfma_f32_16x16x32_bf16 v[32:35], a[196:199], a[132:135], v[32:35]\n\t"
    "ds_read_b128 a[208:211], v228 offset:45056\n\t"
    "v_cvt_pk_bf16_f32 v55, 0, v55\n\t"
    "v_mul_f32 v52, s56, v52\n\t"
    "v_mfma_f32_16x16x32_bf16 v[32:35], a[200:203], a[136:139], v[32:35]\n\t"
    "v_mul_f32 v53, s56, v53\n\t"
    "v_mul_f32 v54, s56, v54\n\t"
    "v_mul_f32 v55, s56, v55\n\t"
    "v_mfma_f32_16x16x32_bf16 v[32:35], a[204:207], a[140:143], v[32:35]\n\t"
    "s_add_u32 m0, s43, 4096\n\t"
    "s_add_u32 s46, s41, 4096\n\t"
    "buffer_load_dwordx4 %[vsrc0], %[srdV], s46 offen offset:0 lds\n\t"
    "v_mfma_f32_16x16x32_bf16 v[36:39], a[192:195], a[144:147], 0\n\t"
    "v_cvt_pk_bf16_f32 v52, 0, v52\n\t"
    "v_mfma_f32_16x16x32_bf16 v[36:39], a[196:199], a[148:151], v[36:39]\n\t"
    "ds_read_b128 a[212:215], v229 offset:45056\n\t"
    "v_cvt_pk_bf16_f32 v53, 0, v53\n\t"
    "v_cvt_pk_bf16_f32 v54, 0, v54\n\t"
    "v_mfma_f32_16x16x32_bf16 v[36:39], a[200:203], a[152:155], v[36:39]\n\t"
    "v_cvt_pk_bf16_f32 v55, 0, v55\n\t"
    "v_fma_f32 v52, v52, s58, v193\n\t"
    "v_fma_f32 v53, v53, s58, v193\n\t"
    "v_mfma_f32_16x16x32_bf16 v[36:39], a[204:207], a[156:159], v[36:39]\n\t"
    "v_fma_f32 v54, v54, s58, v193\n\t"
    "v_fma_f32 v55, v55, s58, v193\n\t"
    "v_exp_f32 v52, v52\n\t"
    "v_mfma_f32_16x16x32_bf16 v[40:43], a[192:195], a[160:163], v[196:199]\n\t"
    "v_exp_f32 v53, v53\n\t"
    "v_mfma_f32_16x16x32_bf16 v[40:43], a[196:199], a[164:167], v[40:43]\n\t"
    "ds_read_b128 a[216:219], v230 offset:45056\n\t"
    "v_exp_f32 v54, v54\n\t"
    "v_mfma_f32_16x16x32_bf16 v[40:43], a[200:203], a[168:171], v[40:43]\n\t"
    "v_exp_f32 v55, v55\n\t"
    "v_mfma_f32_16x16x32_bf16 v[40:43], a[204:207], a[172:175], v[40:43]\n\t"
    "buffer_load_dwordx4 v245, %[srdV], s46 offen offset:1024 lds\n\t"
    "v_exp_f32 v60, v60\n\t"
    "v_mfma_f32_16x16x32_bf16 v[44:47], a[192:195], a[176:179], v[200:203]\n\t"
    "v_exp_f32 v61, v61\n\t"
    "v_mfma_f32_16x16x32_bf16 v[44:47], a[196:199], a[180:183], v[44:47]\n\t"
    "ds_read_b128 a[220:223], v231 offset:45056\n\t"
    "v_exp_f32 v62, v62\n\t"
    "v_mfma_f32_16x16x32_bf16 v[44:47], a[200:203], a[184:187], v[44:47]\n\t"
    "v_exp_f32 v63, v63\n\t"
    "v_cvt_pk_bf16_f32 v146, v52, v53\n\t"
    "v_mfma_f32_16x16x32_bf16 v[44:47], a[204:207], a[188:191], v[44:47]\n\t"
    "v_cvt_pk_bf16_f32 v147, v54, v55\n\t"
    "v_cvt_pk_bf16_f32 v178, v60, v61\n\t"
    "v_cvt_pk_bf16_f32 v179, v62, v63\n\t"
    "s_waitcnt lgkmcnt(4)\n\t"
    "v_mfma_f32_16x16x32_bf16 a[32:35], a[224:227], v[64:67], a[32:35]\n\t"
    "v_cvt_pk_bf16_f32 v32, 0, v32\n\t"
    "v_cvt_pk_bf16_f32 v33, 0, v33\n\t"
    "v_cvt_pk_bf16_f32 v34, 0, v34\n\t"
    "v_mfma_f32_16x16x32_bf16 a[32:35], a[228:231], v[68:71], a[32:35]\n\t"
    "ds_read_b64_tr_b16 a[240:241], %[vb] offset:768\n\t"
    "ds_read_b64_tr_b16 a[242:243], %[vb] offset:4864\n\t"
    "v_mfma_f32_16x16x32_bf16 a[32:35], a[232:235], v[72:75], a[32:35]\n\t"
    "v_cvt_pk_bf16_f32 v35, 0, v35\n\t"
    "v_mul_f32 v32, s56, v32\n\t"
    "v_mul_f32 v33, s56, v33\n\t"
    "v_mfma_f32_16x16x32_bf16 a[32:35], a[236:239], v[76:79], a[32:35]\n\t"
    "buffer_load_dwordx4 %[vsrc0], %[srdV], s46 offen offset:2048 lds\n\t"
    "v_mul_f32 v34, s56, v34\n\t"
    "v_mfma_f32_16x16x32_bf16 a[36:39], a[224:227], v[80:83], a[36:39]\n\t"
    "v_mul_f32 v35, s56, v35\n\t"
    "v_cvt_pk_bf16_f32 v32, 0, v32\n\t"
    "v_mfma_f32_16x16x32_bf16 a[36:39], a[228:231], v[84:87], a[36:39]\n\t"
    "ds_read_b64_tr_b16 a[244:245], %[vb] offset:8960\n\t"
    "ds_read_b64_tr_b16 a[246:247], %[vb] offset:13056\n\t"
    "v_cvt_pk_bf16_f32 v33, 0, v33\n\t"
    "v_mfma_f32_16x16x32_bf16 a[36:39], a[232:235], v[88:91], a[36:39]\n\t"
    "v_cvt_pk_bf16_f32 v34, 0, v34\n\t"
    "v_cvt_pk_bf16_f32 v35, 0, v35\n\t"
    "v_fma_f32 v32, v32, s58, v192\n\t"
    "v_mfma_f32_16x16x32_bf16 a[36:39], a[236:239], v[92:95], a[36:39]\n\t"
    "v_fma_f32 v33, v33, s58, v192\n\t"
    "v_fma_f32 v34, v34, s58, v192\n\t"
    "v_mfma_f32_16x16x32_bf16 a[40:43], a[224:227], v[96:99], a[40:43]\n\t"
    "v_fma_f32 v35, v35, s58, v192\n\t"
    "v_exp_f32 v32, v32\n\t"
    "v_mfma_f32_16x16x32_bf16 a[40:43], a[228:231], v[100:103], a[40:43]\n\t"
    "ds_read_b64_tr_b16 a[248:249], %[vb] offset:17152\n\t"
    "ds_read_b64_tr_b16 a[250:251], %[vb] offset:21248\n\t"
    "v_mfma_f32_16x16x32_bf16 a[40:43], a[232:235], v[104:107], a[40:43]\n\t"
    "v_exp_f32 v33, v33\n\t"
    "v_exp_f32 v34, v34\n\t"
    "v_mfma_f32_16x16x32_bf16 a[40:43], a[236:239], v[108:111], a[40:43]\n\t"
    "buffer_load_dwordx4 v245, %[srdV], s46 offen offset:3072 lds\n\t"
    "v_mfma_f32_16x16x32_bf16 a[44:47], a[224:227], v[112:115], a[44:47]\n\t"
    "v_exp_f32 v35, v35\n\t"
    "v_mfma_f32_16x16x32_bf16 a[44:47], a[228:231], v[116:119], a[44:47]\n\t"
    "ds_read_b64_tr_b16 a[252:253], %[vb] offset:25344\n\t"
    "ds_read_b64_tr_b16 a[254:255], %[vb] offset:29440\n\t"
    "v_exp_f32 v40, v40\n\t"
    "v_mfma_f32_16x16x32_bf16 a[44:47], a[232:235], v[120:123], a[44:47]\n\t"
    "v_exp_f32 v41, v41\n\t"
    "v_mfma_f32_16x16x32_bf16 a[44:47], a[236:239], v[124:127], a[44:47]\n\t"
    "v_exp_f32 v42, v42\n\t"
    "v_mfma_f32_16x16x32_bf16 v[212:215], v[224:227], v[80:83], v[212:215]\n\t"
    "v_exp_f32 v43, v43\n\t"
    "v_cvt_pk_bf16_f32 v132, v32, v33\n\t"
    "v_mfma_f32_16x16x32_bf16 v[212:215], v[224:227], v[84:87], v[212:215]\n\t"
    "v_cvt_pk_bf16_f32 v133, v34, v35\n\t"
    "v_cvt_pk_bf16_f32 v164, v40, v41\n\t"
    "v_cvt_pk_bf16_f32 v165, v42, v43\n\t"
    "s_waitcnt lgkmcnt(8)\n\t"
    "v_mfma_f32_16x16x32_bf16 v[48:51], a[208:211], a[128:131], 0\n\t"
    "v_cvt_pk_bf16_f32 v36, 0, v36\n\t"
    "v_cvt_pk_bf16_f32 v37, 0, v37\n\t"
    "v_cvt_pk_bf16_f32 v38, 0, v38\n\t"
    "v_mfma_f32_16x16x32_bf16 v[48:51], a[212:215], a[132:135], v[48:51]\n\t"
    "ds_read_b128 a[192:195], v228 offset:49152\n\t"
    "v_cvt_pk_bf16_f32 v39, 0, v39\n\t"
    "v_mul_f32 v36, s56, v36\n\t"
    "v_mfma_f32_16x16x32_bf16 v[48:51], a[216:219], a[136:139], v[48:51]\n\t"
    "v_mul_f32 v37, s56, v37\n\t"
    "v_mul_f32 v38, s56, v38\n\t"
    "v_mul_f32 v39, s56, v39\n\t"
    "v_mfma_f32_16x16x32_bf16 v[48:51], a[220:223], a[140:143], v[48:51]\n\t"
    "s_add_u32 s41, s41, 0x8000\n\t"
    "s_cmp_gt_u32 s44, 5\n\t"
    "s_cselect_b32 s47, 0x7fffff00, s51\n\t"
    "s_cselect_b32 s48, 0, %[pfstep]\n\t"
    "buffer_load_dword v246, %[pfv], %[srdPF], s47 offen\n\t"
    "v_mfma_f32_16x16x32_bf16 v[52:55], a[208:211], a[144:147], 0\n\t"
    "v_mfma_f32_16x16x32_bf16 v[52:55], a[212:215], a[148:151], v[52:55]\n\t"
    "ds_read_b128 a[196:199], v229 offset:49152\n\t"
    "v_cvt_pk_bf16_f32 v36, 0, v36\n\t"
    "v_cvt_pk_bf16_f32 v37, 0, v37\n\t"
    "v_mfma_f32_16x16x32_bf16 v[52:55], a[216:219], a[152:155], v[52:55]\n\t"
    "v_cvt_pk_bf16_f32 v38, 0, v38\n\t"
    "v_cvt_pk_bf16_f32 v39, 0, v39\n\t"
    "v_fma_f32 v36, v36, s58, v193\n\t"
    "v_mfma_f32_16x16x32_bf16 v[52:55], a[220:223], a[156:159], v[52:55]\n\t"
    "v_fma_f32 v37, v37, s58, v193\n\t"
    "v_fma_f32 v38, v38, s58, v193\n\t"
    "v_fma_f32 v39, v39, s58, v193\n\t"
    "v_mfma_f32_16x16x32_bf16 v[56:59], a[208:211], a[160:163], v[196:199]\n\t"
    "v_exp_f32 v36, v36\n\t"
    "v_mfma_f32_16x16x32_bf16 v[56:59], a[212:215], a[164:167], v[56:59]\n\t"
    "ds_read_b128 a[200:203], v230 offset:49152\n\t"
    "v_exp_f32 v37, v37\n\t"
    "v_mfma_f32_16x16x32_bf16 v[56:59], a[216:219], a[168:171], v[56:59]\n\t"
    "v_exp_f32 v38, v38\n\t"
    "v_exp_f32 v39, v39\n\t"
    "v_mfma_f32_16x16x32_bf16 v[56:59], a[220:223], a[172:175], v[56:59]\n\t"
    "s_add_u32 s51, s51, s48\n\t"
    "v_exp_f32 v44, v44\n\t"
    "v_mfma_f32_16x16x32_bf16 v[60:63], a[208:211], a[176:179], v[200:203]\n\t"
    "v_exp_f32 v45, v45\n\t"
    "v_mfma_f32_16x16x32_bf16 v[60:63], a[212:215], a[180:183], v[60:63]\n\t"
    "ds_read_b128 a[204:207], v231 offset:49152\n\t"
    "v_exp_f32 v46, v46\n\t"
    "v_mfma_f32_16x16x32_bf16 v[60:63], a[216:219], a[184:187], v[60:63]\n\t"
    "v_exp_f32 v47, v47\n\t"
    "v_cvt_pk_bf16_f32 v148, v36, v37\n\t"
    "v_mfma_f32_16x16x32_bf16 v[60:63], a[220:223], a[188:191], v[60:63]\n\t"
    "v_cvt_pk_bf16_f32 v149, v38, v39\n\t"
    "v_cvt_pk_bf16_f32 v180, v44, v45\n\t"
    "v_cvt_pk_bf16_f32 v181, v46, v47\n\t"
    "s_waitcnt lgkmcnt(4)\n\t"
    "v_mfma_f32_16x16x32_bf16 a[48:51], a[240:243], v[64:67], a[48:51]\n\t"
    "v_cvt_pk_bf16_f32 v48, 0, v48\n\t"
    "v_cvt_pk_bf16_f32 v49, 0, v49\n\t"
    "v_mfma_f32_16x16x32_bf16 a[48:51], a[244:247], v[68:71], a[48:51]\n\t"
    "ds_read_b64_tr_b16 a[224:225], %[vb] offset:1024\n\t"
    "ds_read_b64_tr_b16 a[226:227], %[vb] offset:5120\n\t"
    "v_cvt_pk_bf16_f32 v50, 0, v50\n\t"
    "v_mfma_f32_16x16x32_bf16 a[48:51], a[248:251], v[72:75], a[48:51]\n\t"
    "v_cvt_pk_bf16_f32 v51, 0, v51\n\t"
    "v_mul_f32 v48, s56, v48\n\t"
    "v_mfma_f32_16x16x32_bf16 a[48:51], a[252:255], v[76:79], a[48:51]\n\t"
    "v_mul_f32 v49, s56, v49\n\t"
    "v_mul_f32 v50, s56, v50\n\t"
    "v_mul_f32 v51, s56, v51\n\t"
    "v_mfma_f32_16x16x32_bf16 a[52:55], a[240:243], v[80:83], a[52:55]\n\t"
    "v_cvt_pk_bf16_f32 v48, 0, v48\n\t"
    "v_cvt_pk_bf16_f32 v49, 0, v49\n\t"
    "v_mfma_f32_16x16x32_bf16 a[52:55], a[244:247], v[84:87], a[52:55]\n\t"
    "ds_read_b64_tr_b16 a[228:229], %[vb] offset:9216\n\t"
    "ds_read_b64_tr_b16 a[230:231], %[vb] offset:13312\n\t"
    "v_cvt_pk_bf16_f32 v50, 0, v50\n\t"
    "v_mfma_f32_16x16x32_bf16 a[52:55], a[248:251], v[88:91], a[52:55]\n\t"
    "v_cvt_pk_bf16_f32 v51, 0, v51\n\t"
    "v_fma_f32 v48, v48, s58, v192\n\t"
    "v_mfma_f32_16x16x32_bf16 a[52:55], a[252:255], v[92:95], a[52:55]\n\t"
    "v_fma_f32 v49, v49, s58, v192\n\t"
    "v_fma_f32 v50, v50, s58, v192\n\t"
    "v_fma_f32 v51, v51, s58, v192\n\t"
    "v_mfma_f32_16x16x32_bf16 a[56:59], a[240:243], v[96:99], a[56:59]\n\t"
    "v_exp_f32 v48, v48\n\t"
    "v_mfma_f32_16x16x32_bf16 a[56:59], a[244:247], v[100:103], a[56:59]\n\t"
    "ds_read_b64_tr_b16 a[232:233], %[vb] offset:17408\n\t"
    "ds_read_b64_tr_b16 a[234:235], %[vb] offset:21504\n\t"
    "v_mfma_f32_16x16x32_bf16 a[56:59], a[248:251], v[104:107], a[56:59]\n\t"
    "v_exp_f32 v49, v49\n\t"
    "v_mfma_f32_16x16x32_bf16 a[56:59], a[252:255], v[108:111], a[56:59]\n\t"
    "v_exp_f32 v50, v50\n\t"
    "v_exp_f32 v51, v51\n\t"
    "v_mfma_f32_16x16x32_bf16 a[60:63], a[240:243], v[112:115], a[60:63]\n\t"
    "v_exp_f32 v56, v56\n\t"
    "v_mfma_f32_16x16x32_bf16 a[60:63], a[244:247], v[116:119], a[60:63]\n\t"
    "ds_read_b64_tr_b16 a[236:237], %[vb] offset:25600\n\t"
    "ds_read_b64_tr_b16 a[238:239], %[vb] offset:29696\n\t"
    "v_mfma_f32_16x16x32_bf16 a[60:63], a[248:251], v[120:123], a[60:63]\n\t"
    "v_exp_f32 v57, v57\n\t"
    "v_mfma_f32_16x16x32_bf16 a[60:63], a[252:255], v[124:127], a[60:63]\n\t"
    "v_exp_f32 v58, v58\n\t"
    "v_exp_f32 v59, v59\n\t"
    "v_mfma_f32_16x16x32_bf16 v[212:215], v[224:227], v[88:91], v[212:215]\n\t"
    "v_cvt_pk_bf16_f32 v134, v48, v49\n\t"
    "v_cvt_pk_bf16_f32 v135, v50, v51\n\t"
    "v_mfma_f32_16x16x32_bf16 v[212:215], v[224:227], v[92:95], v[212:215]\n\t"
    "v_cvt_pk_bf16_f32 v166, v56, v57\n\t"
    "v_cvt_pk_bf16_f32 v167, v58, v59\n\t"
    "s_waitcnt lgkmcnt(8)\n\t"
    "v_mfma_f32_16x16x32_bf16 v[32:35], a[192:195], a[128:131], 0\n\t"
    "v_cvt_pk_bf16_f32 v52, 0, v52\n\t"
    "v_cvt_pk_bf16_f32 v53, 0, v53\n\t"
    "v_cvt_pk_bf16_f32 v54, 0, v54\n\t"
    "v_mfma_f32_16x16x32_bf16 v[32:35], a[196:199], a[132:135], v[32:35]\n\t"
    "ds_read_b128 a[208:211], v228 offset:53248\n\t"
    "v_cvt_pk_bf16_f32 v55, 0, v55\n\t"
    "v_mfma_f32_16x16x32_bf16 v[32:35], a[200:203], a[136:139], v[32:35]\n\t"
    "v_mul_f32 v52, s56, v52\n\t"
    "v_mul_f32 v53, s56, v53\n\t"
    "v_mul_f32 v54, s56, v54\n\t"
    "v_mfma_f32_16x16x32_bf16 v[32:35], a[204:207], a[140:143], v[32:35]\n\t"
    "v_mul_f32 v55, s56, v55\n\t"
    "v_cvt_pk_bf16_f32 v52, 0, v52\n\t"
    "v_mfma_f32_16x16x32_bf16 v[36:39], a[192:195], a[144:147], 0\n\t"
    "v_cvt_pk_bf16_f32 v53, 0, v53\n\t"
    "v_cvt_pk_bf16_f32 v54, 0, v54\n\t"
    "v_cvt_pk_bf16_f32 v55, 0, v55\n\t"
    "v_mfma_f32_16x16x32_bf16 v[36:39], a[196:199], a[148:151], v[36:39]\n\t"
    "ds_read_b128 a[212:215], v229 offset:53248\n\t"
    "v_fma_f32 v52, v52, s58, v193\n\t"
    "v_mfma_f32_16x16x32_bf16 v[36:39], a[200:203], a[152:155], v[36:39]\n\t"
    "v_fma_f32 v53, v53, s58, v193\n\t"
    "v_fma_f32 v54, v54, s58, v193\n\t"
    "v_fma_f32 v55, v55, s58, v193\n\t"
    "v_mfma_f32_16x16x32_bf16 v[36:39], a[204:207], a[156:159], v[36:39]\n\t"
    "v_exp_f32 v52, v52\n\t"
    "v_mfma_f32_16x16x32_bf16 v[40:43], a[192:195], a[160:163], v[196:199]\n\t"
    "v_exp_f32 v53, v53\n\t"
    "v_mfma_f32_16x16x32_bf16 v[40:43], a[196:199], a[164:167], v[40:43]\n\t"
    "ds_read_b128 a[216:219], v230 offset:53248\n\t"
    "v_exp_f32 v54, v54\n\t"
    "v_mfma_f32_16x16x32_bf16 v[40:43], a[200:203], a[168:171], v[40:43]\n\t"
    "v_exp_f32 v55, v55\n\t"
    "v_mfma_f32_16x16x32_bf16 v[40:43], a[204:207], a[172:175], v[40:43]\n\t"
    "v_exp_f32 v60, v60\n\t"
    "v_exp_f32 v61, v61\n\t"
    "v_mfma_f32_16x16x32_bf16 v[44:47], a[192:195], a[176:179], v[200:203]\n\t"
    "v_exp_f32 v62, v62\n\t"
    "v_mfma_f32_16x16x32_bf16 v[44:47], a[196:199], a[180:183], v[44:47]\n\t"
    "ds_read_b128 a[220:223], v231 offset:53248\n\t"
    "v_exp_f32 v63, v63\n\t"
    "v_mfma_f32_16x16x32_bf16 v[44:47], a[200:203], a[184:187], v[44:47]\n\t"
    "v_cvt_pk_bf16_f32 v150, v52, v53\n\t"
    "v_cvt_pk_bf16_f32 v151, v54, v55\n\t"
    "v_mfma_f32_16x16x32_bf16 v[44:47], a[204:207], a[188:191], v[44:47]\n\t"
    "v_cvt_pk_bf16_f32 v182, v60, v61\n\t"
    "v_cvt_pk_bf16_f32 v183, v62, v63\n\t"
    "s_waitcnt lgkmcnt(4)\n\t"
    "v_mfma_f32_16x16x32_bf16 a[64:67], a[224:227], v[64:67], a[64:67]\n\t"
    "v_cvt_pk_bf16_f32 v32, 0, v32\n\t"
    "v_cvt_pk_bf16_f32 v33, 0, v33\n\t"
    "v_mfma_f32_16x16x32_bf16 a[64:67], a[228:231], v[68:71], a[64:67]\n\t"
    "ds_read_b64_tr_b16 a[240:241], %[vb] offset:1280\n\t"
    "ds_read_b64_tr_b16 a[242:243], %[vb] offset:5376\n\t"
    "v_cvt_pk_bf16_f32 v34, 0, v34\n\t"
    "v_mfma_f32_16x16x32_bf16 a[64:67], a[232:235], v[72:75], a[64:67]\n\t"
    "v_cvt_pk_bf16_f32 v35, 0, v35\n\t"
    "v_mul_f32 v32, s56, v32\n\t"
    "v_mfma_f32_16x16x32_bf16 a[64:67], a[236:239], v[76:79], a[64:67]\n\t"
    "v_mul_f32 v33, s56, v33\n\t"
    "v_mul_f32 v34, s56, v34\n\t"
    "v_mul_f32 v35, s56, v35\n\t"
    "v_mfma_f32_16x16x32_bf16 a[68:71], a[224:227], v[80:83], a[68:71]\n\t"
    "v_cvt_pk_bf16_f32 v32, 0, v32\n\t"
    "v_cvt_pk_bf16_f32 v33, 0, v33\n\t"
    "v_mfma_f32_16x16x32_bf16 a[68:71], a[228:231], v[84:87], a[68:71]\n\t"
    "ds_read_b64_tr_b16 a[244:245], %[vb] offset:9472\n\t"
    "ds_read_b64_tr_b16 a[246:247], %[vb] offset:13568\n\t"
    "v_cvt_pk_bf16_f32 v34, 0, v34\n\t"
    "v_mfma_f32_16x16x32_bf16 a[68:71], a[232:235], v[88:91], a[68:71]\n\t"
    "v_cvt_pk_bf16_f32 v35, 0, v35\n\t"
    "v_fma_f32 v32, v32, s58, v192\n\t"
    "v_mfma_f32_16x16x32_bf16 a[68:71], a[236:239], v[92:95], a[68:71]\n\t"
    "v_fma_f32 v33, v33, s58, v192\n\t"
    "v_fma_f32 v34, v34, s58, v192\n\t"
    "v_fma_f32 v35, v35, s58, v192\n\t"
    "v_mfma_f32_16x16x32_bf16 a[72:75], a[224:227], v[96:99], a[72:75]\n\t"
    "v_exp_f32 v32, v32\n\t"
    "v_mfma_f32_16x16x32_bf16 a[72:75], a[228:231], v[100:103], a[72:75]\n\t"
    "ds_read_b64_tr_b16 a[248:249], %[vb] offset:17664\n\t"
    "ds_read_b64_tr_b16 a[250:251], %[vb] offset:21760\n\t"
    "v_mfma_f32_16x16x32_bf16 a[72:75], a[232:235], v[104:107], a[72:75]\n\t"
    "v_exp_f32 v33, v33\n\t"
    "v_mfma_f32_16x16x32_bf16 a[72:75], a[236:239], v[108:111], a[72:75]\n\t"
    "v_exp_f32 v34, v34\n\t"
    "v_exp_f32 v35, v35\n\t"
    "v_mfma_f32_16x16x32_bf16 a[76:79], a[224:227], v[112:115], a[76:79]\n\t"
    "v_exp_f32 v40, v40\n\t"
    "v_mfma_f32_16x16x32_bf16 a[76:79], a[228:231], v[116:119], a[76:79]\n\t"
    "ds_read_b64_tr_b16 a[252:253], %[vb] offset:25856\n\t"
    "ds_read_b64_tr_b16 a[254:255], %[vb] offset:29952\n\t"
    "v_mfma_f32_16x16x32_bf16 a[76:79], a[232:235], v[120:123], a[76:79]\n\t"
    "v_exp_f32 v41, v41\n\t"
    "v_mfma_f32_16x16x32_bf16 a[76:79], a[236:239], v[124:127], a[76:79]\n\t"
    "v_exp_f32 v42, v42\n\t"
    "v_exp_f32 v43, v43\n\t"
    "v_mfma_f32_16x16x32_bf16 v[216:219], v[224:227], v[96:99], v[216:219]\n\t"
    "v_cvt_pk_bf16_f32 v136, v32, v33\n\t"
    "v_cvt_pk_bf16_f32 v137, v34, v35\n\t"
    "v_mfma_f32_16x16x32_bf16 v[216:219], v[224:227], v[100:103], v[216:219]\n\t"
    "v_cvt_pk_bf16_f32 v168, v40, v41\n\t"
    "v_cvt_pk_bf16_f32 v169, v42, v43\n\t"
    "s_waitcnt lgkmcnt(8)\n\t"
    "v_mfma_f32_16x16x32_bf16 v[48:51], a[208:211], a[128:131], 0\n\t"
    "v_cvt_pk_bf16_f32 v36, 0, v36\n\t"
    "v_cvt_pk_bf16_f32 v37, 0, v37\n\t"
    "v_cvt_pk_bf16_f32 v38, 0, v38\n\t"
    "v_mfma_f32_16x16x32_bf16 v[48:51], a[212:215], a[132:135], v[48:51]\n\t"
    "ds_read_b128 a[192:195], v228 offset:57344\n\t"
    "v_cvt_pk_bf16_f32 v39, 0, v39\n\t"
    "v_mfma_f32_16x16x32_bf16 v[48:51], a[216:219], a[136:139], v[48:51]\n\t"
    "v_mul_f32 v36, s56, v36\n\t"
    "v_mul_f32 v37, s56, v37\n\t"
    "v_mul_f32 v38, s56, v38\n\t"
    "v_mfma_f32_16x16x32_bf16 v[48:51], a[220:223], a[140:143], v[48:51]\n\t"
    "v_mul_f32 v39, s56, v39\n\t"
    "v_cvt_pk_bf16_f32 v36, 0, v36\n\t"
    "v_mfma_f32_16x16x32_bf16 v[52:55], a[208:211], a[144:147], 0\n\t"
    "v_cvt_pk_bf16_f32 v37, 0, v37\n\t"
    "v_cvt_pk_bf16_f32 v38, 0, v38\n\t"
    "v_cvt_pk_bf16_f32 v39, 0, v39\n\t"
    "v_mfma_f32_16x16x32_bf16 v[52:55], a[212:215], a[148:151], v[52:55]\n\t"
    "ds_read_b128 a[196:199], v229 offset:57344\n\t"
    "v_fma_f32 v36, v36, s58, v193\n\t"
    "v_mfma_f32_16x16x32_bf16 v[52:55], a[216:219], a[152:155], v[52:55]\n\t"
    "v_fma_f32 v37, v37, s58, v193\n\t"
    "v_fma_f32 v38, v38, s58, v193\n\t"
    "v_fma_f32 v39, v39, s58, v193\n\t"
    "v_mfma_f32_16x16x32_bf16 v[52:55], a[220:223], a[156:159], v[52:55]\n\t"
    "v_exp_f32 v36, v36\n\t"
    "v_mfma_f32_16x16x32_bf16 v[56:59], a[208:211], a[160:163], v[196:199]\n\t"
    "v_exp_f32 v37, v37\n\t"
    "v_mfma_f32_16x16x32_bf16 v[56:59], a[212:215], a[164:167], v[56:59]\n\t"
    "ds_read_b128 a[200:203], v230 offset:57344\n\t"
    "v_exp_f32 v38, v38\n\t"
    "v_mfma_f32_16x16x32_bf16 v[56:59], a[216:219], a[168:171], v[56:59]\n\t"
    "v_exp_f32 v39, v39\n\t"
    "v_mfma_f32_16x16x32_bf16 v[56:59], a[220:223], a[172:175], v[56:59]\n\t"
    "v_exp_f32 v44, v44\n\t"
    "v_exp_f32 v45, v45\n\t"
    "v_mfma_f32_16x16x32_bf16 v[60:63], a[208:211], a[176:179], v[200:203]\n\t"
    "v_exp_f32 v46, v46\n\t"
    "v_mfma_f32_16x16x32_bf16 v[60:63], a[212:215], a[180:183], v[60:63]\n\t"
    "ds_read_b128 a[204:207], v231 offset:57344\n\t"
    "v_exp_f32 v47, v47\n\t"
    "v_mfma_f32_16x16x32_bf16 v[60:63], a[216:219], a[184:187], v[60:63]\n\t"
    "v_cvt_pk_bf16_f32 v152, v36, v37\n\t"
    "v_cvt_pk_bf16_f32 v153, v38, v39\n\t"
    "v_mfma_f32_16x16x32_bf16 v[60:63], a[220:223], a[188:191], v[60:63]\n\t"
    "v_cvt_pk_bf16_f32 v184, v44, v45\n\t"
    "v_cvt_pk_bf16_f32 v185, v46, v47\n\t"
    "s_waitcnt lgkmcnt(4)\n\t"
    "v_mfma_f32_16x16x32_bf16 a[80:83], a[240:243], v[64:67], a[80:83]\n\t"
    "v_cvt_pk_bf16_f32 v48, 0, v48\n\t"
    "v_cvt_pk_bf16_f32 v49, 0, v49\n\t"
    "v_mfma_f32_16x16x32_bf16 a[80:83], a[244:247], v[68:71], a[80:83]\n\t"
    "ds_read_b64_tr_b16 a[224:225], %[vb] offset:1536\n\t"
    "ds_read_b64_tr_b16 a[226:227], %[vb] offset:5632\n\t"
    "v_cvt_pk_bf16_f32 v50, 0, v50\n\t"
    "v_mfma_f32_16x16x32_bf16 a[80:83], a[248:251], v[72:75], a[80:83]\n\t"
    "v_cvt_pk_bf16_f32 v51, 0, v51\n\t"
    "v_mul_f32 v48, s56, v48\n\t"
    "v_mfma_f32_16x16x32_bf16 a[80:83], a[252:255], v[76:79], a[80:83]\n\t"
    "v_mul_f32 v49, s56, v49\n\t"
    "v_mul_f32 v50, s56, v50\n\t"
    "v_mul_f32 v51, s56, v51\n\t"
    "v_mfma_f32_16x16x32_bf16 a[84:87], a[240:243], v[80:83], a[84:87]\n\t"
    "v_cvt_pk_bf16_f32 v48, 0, v48\n\t"
    "v_cvt_pk_bf16_f32 v49, 0, v49\n\t"
    "v_mfma_f32_16x16x32_bf16 a[84:87], a[244:247], v[84:87], a[84:87]\n\t"
    "ds_read_b64_tr_b16 a[228:229], %[vb] offset:9728\n\t"
    "ds_read_b64_tr_b16 a[230:231], %[vb] offset:13824\n\t"
    "v_cvt_pk_bf16_f32 v50, 0, v50\n\t"
    "v_mfma_f32_16x16x32_bf16 a[84:87], a[248:251], v[88:91], a[84:87]\n\t"
    "v_cvt_pk_bf16_f32 v51, 0, v51\n\t"
    "v_fma_f32 v48, v48, s58, v192\n\t"
    "v_mfma_f32_16x16x32_bf16 a[84:87], a[252:255], v[92:95], a[84:87]\n\t"
    "v_fma_f32 v49, v49, s58, v192\n\t"
    "v_fma_f32 v50, v50, s58, v192\n\t"
    "v_fma_f32 v51, v51, s58, v192\n\t"
    "v_mfma_f32_16x16x32_bf16 a[88:91], a[240:243], v[96:99], a[88:91]\n\t"
    "v_exp_f32 v48, v48\n\t"
    "v_mfma_f32_16x16x32_bf16 a[88:91], a[244:247], v[100:103], a[88:91]\n\t"
    "ds_read_b64_tr_b16 a[232:233], %[vb] offset:17920\n\t"
    "ds_read_b64_tr_b16 a[234:235], %[vb] offset:22016\n\t"
    "v_mfma_f32_16x16x32_bf16 a[88:91], a[248:251], v[104:107], a[88:91]\n\t"
    "v_exp_f32 v49, v49\n\t"
    "v_mfma_f32_16x16x32_bf16 a[88:91], a[252:255], v[108:111], a[88:91]\n\t"
    "v_exp_f32 v50, v50\n\t"
    "v_exp_f32 v51, v51\n\t"
    "v_mfma_f32_16x16x32_bf16 a[92:95], a[240:243], v[112:115], a[92:95]\n\t"
    "v_exp_f32 v56, v56\n\t"
    "v_mfma_f32_16x16x32_bf16 a[92:95], a[244:247], v[116:119], a[92:95]\n\t"
    "ds_read_b64_tr_b16 a[236:237], %[vb] offset:26112\n\t"
    "ds_read_b64_tr_b16 a[238:239], %[vb] offset:30208\n\t"
    "v_mfma_f32_16x16x32_bf16 a[92:95], a[248:251], v[120:123], a[92:95]\n\t"
    "v_exp_f32 v57, v57\n\t"
    "v_mfma_f32_16x16x32_bf16 a[92:95], a[252:255], v[124:127], a[92:95]\n\t"
    "v_exp_f32 v58, v58\n\t"
    "v_exp_f32 v59, v59\n\t"
    "v_mfma_f32_16x16x32_bf16 v[216:219], v[224:227], v[104:107], v[216:219]\n\t"
    "v_cvt_pk_bf16_f32 v138, v48, v49\n\t"
    "v_cvt_pk_bf16_f32 v139, v50, v51\n\t"
    "v_mfma_f32_16x16x32_bf16 v[216:219], v[224:227], v[108:111], v[216:219]\n\t"
    "v_cvt_pk_bf16_f32 v170, v56, v57\n\t"
    "v_cvt_pk_bf16_f32 v171, v58, v59\n\t"
    "s_waitcnt lgkmcnt(8)\n\t"
    "v_mfma_f32_16x16x32_bf16 v[32:35], a[192:195], a[128:131], 0\n\t"
    "v_cvt_pk_bf16_f32 v52, 0, v52\n\t"
    "v_cvt_pk_bf16_f32 v53, 0, v53\n\t"
    "v_cvt_pk_bf16_f32 v54, 0, v54\n\t"
    "v_mfma_f32_16x16x32_bf16 v[32:35], a[196:199], a[132:135], v[32:35]\n\t"
    "ds_read_b128 a[208:211], v228 offset:61440\n\t"
    "v_cvt_pk_bf16_f32 v55, 0, v55\n\t"
    "v_mfma_f32_16x16x32_bf16 v[32:35], a[200:203], a[136:139], v[32:35]\n\t"
    "v_mul_f32 v52, s56, v52\n\t"
    "v_mul_f32 v53, s56, v53\n\t"
    "v_mul_f32 v54, s56, v54\n\t"
    "v_mfma_f32_16x16x32_bf16 v[32:35], a[204:207], a[140:143], v[32:35]\n\t"
    "v_mul_f32 v55, s56, v55\n\t"
    "v_cvt_pk_bf16_f32 v52, 0, v52\n\t"
    "v_mfma_f32_16x16x32_bf16 v[36:39], a[192:195], a[144:147], 0\n\t"
    "v_cvt_pk_bf16_f32 v53, 0, v53\n\t"
    "v_cvt_pk_bf16_f32 v54, 0, v54\n\t"
    "v_cvt_pk_bf16_f32 v55, 0, v55\n\t"
    "v_mfma_f32_16x16x32_bf16 v[36:39], a[196:199], a[148:151], v[36:39]\n\t"
    "ds_read_b128 a[212:215], v229 offset:61440\n\t"
    "v_fma_f32 v52, v52, s58, v193\n\t"
    "v_mfma_f32_16x16x32_bf16 v[36:39], a[200:203], a[152:155], v[36:39]\n\t"
    "v_fma_f32 v53, v53, s58, v193\n\t"
    "v_fma_f32 v54, v54, s58, v193\n\t"
    "v_fma_f32 v55, v55, s58, v193\n\t"
    "v_mfma_f32_16x16x32_bf16 v[36:39], a[204:207], a[156:159], v[36:39]\n\t"
    "v_exp_f32 v52, v52\n\t"
    "v_mfma_f32_16x16x32_bf16 v[40:43], a[192:195], a[160:163], v[196:199]\n\t"
    "v_exp_f32 v53, v53\n\t"
    "v_mfma_f32_16x16x32_bf16 v[40:43], a[196:199], a[164:167], v[40:43]\n\t"
    "ds_read_b128 a[216:219], v230 offset:61440\n\t"
    "v_exp_f32 v54, v54\n\t"
    "v_mfma_f32_16x16x32_bf16 v[40:43], a[200:203], a[168:171], v[40:43]\n\t"
    "v_exp_f32 v55, v55\n\t"
    "v_mfma_f32_16x16x32_bf16 v[40:43], a[204:207], a[172:175], v[40:43]\n\t"
    "v_exp_f32 v60, v60\n\t"
    "v_exp_f32 v61, v61\n\t"
    "v_mfma_f32_16x16x32_bf16 v[44:47], a[192:195], a[176:179], v[200:203]\n\t"
    "v_exp_f32 v62, v62\n\t"
    "v_mfma_f32_16x16x32_bf16 v[44:47], a[196:199], a[180:183], v[44:47]\n\t"
    "ds_read_b128 a[220:223], v231 offset:61440\n\t"
    "v_exp_f32 v63, v63\n\t"
    "v_mfma_f32_16x16x32_bf16 v[44:47], a[200:203], a[184:187], v[44:47]\n\t"
    "v_cvt_pk_bf16_f32 v154, v52, v53\n\t"
    "v_cvt_pk_bf16_f32 v155, v54, v55\n\t"
    "v_mfma_f32_16x16x32_bf16 v[44:47], a[204:207], a[188:191], v[44:47]\n\t"
    "v_cvt_pk_bf16_f32 v186, v60, v61\n\t"
    "v_cvt_pk_bf16_f32 v187, v62, v63\n\t"
    "s_waitcnt lgkmcnt(4)\n\t"
    "v_mfma_f32_16x16x32_bf16 a[96:99], a[224:227], v[64:67], a[96:99]\n\t"
    "v_cvt_pk_bf16_f32 v32, 0, v32\n\t"
    "v_cvt_pk_bf16_f32 v33, 0, v33\n\t"
    "v_mfma_f32_16x16x32_bf16 a[96:99], a[228:231], v[68:71], a[96:99]\n\t"
    "ds_read_b64_tr_b16 a[240:241], %[vb] offset:1792\n\t"
    "ds_read_b64_tr_b16 a[242:243], %[vb] offset:5888\n\t"
    "v_cvt_pk_bf16_f32 v34, 0, v34\n\t"
    "v_mfma_f32_16x16x32_bf16 a[96:99], a[232:235], v[72:75], a[96:99]\n\t"
    "v_cvt_pk_bf16_f32 v35, 0, v35\n\t"
    "v_mul_f32 v32, s56, v32\n\t"
    "v_mfma_f32_16x16x32_bf16 a[96:99], a[236:239], v[76:79], a[96:99]\n\t"
    "v_mul_f32 v33, s56, v33\n\t"
    "v_mul_f32 v34, s56, v34\n\t"
    "v_mul_f32 v35, s56, v35\n\t"
    "v_mfma_f32_16x16x32_bf16 a[100:103], a[224:227], v[80:83], a[100:103]\n\t"
    "v_cvt_pk_bf16_f32 v32, 0, v32\n\t"
    "v_cvt_pk_bf16_f32 v33, 0, v33\n\t"
    "v_mfma_f32_16x16x32_bf16 a[100:103], a[228:231], v[84:87], a[100:103]\n\t"
    "ds_read_b64_tr_b16 a[244:245], %[vb] offset:9984\n\t"
    "ds_read_b64_tr_b16 a[246:247], %[vb] offset:14080\n\t"
    "v_cvt_pk_bf16_f32 v34, 0, v34\n\t"
    "v_mfma_f32_16x16x32_bf16 a[100:103], a[232:235], v[88:91], a[100:103]\n\t"
    "v_cvt_pk_bf16_f32 v35, 0, v35\n\t"
    "v_fma_f32 v32, v32, s58, v192\n\t"
    "v_mfma_f32_16x16x32_bf16 a[100:103], a[236:239], v[92:95], a[100:103]\n\t"
    "v_fma_f32 v33, v33, s58, v192\n\t"
    "v_fma_f32 v34, v34, s58, v192\n\t"
    "v_fma_f32 v35, v35, s58, v192\n\t"
    "v_mfma_f32_16x16x32_bf16 a[104:107], a[224:227], v[96:99], a[104:107]\n\t"
    "v_exp_f32 v32, v32\n\t"
    "v_mfma_f32_16x16x32_bf16 a[104:107], a[228:231], v[100:103], a[104:107]\n\t"
    "ds_read_b64_tr_b16 a[248:249], %[vb] offset:18176\n\t"
    "ds_read_b64_tr_b16 a[250:251], %[vb] offset:22272\n\t"
    "v_mfma_f32_16x16x32_bf16 a[104:107], a[232:235], v[104:107], a[104:107]\n\t"
    "v_exp_f32 v33, v33\n\t"
    "v_mfma_f32_16x16x32_bf16 a[104:107], a[236:239], v[108:111], a[104:107]\n\t"
    "v_exp_f32 v34, v34\n\t"
    "v_exp_f32 v35, v35\n\t"
    "v_mfma_f32_16x16x32_bf16 a[108:111], a[224:227], v[112:115], a[108:111]\n\t"
    "v_exp_f32 v40, v40\n\t"
    "v_mfma_f32_16x16x32_bf16 a[108:111], a[228:231], v[116:119], a[108:111]\n\t"
    "ds_read_b64_tr_b16 a[252:253], %[vb] offset:26368\n\t"
    "ds_read_b64_tr_b16 a[254:255], %[vb] offset:30464\n\t"
    "v_mfma_f32_16x16x32_bf16 a[108:111], a[232:235], v[120:123], a[108:111]\n\t"
    "v_exp_f32 v41, v41\n\t"
    "v_mfma_f32_16x16x32_bf16 a[108:111], a[236:239], v[124:127], a[108:111]\n\t"
    "v_exp_f32 v42, v42\n\t"
    "v_exp_f32 v43, v43\n\t"
    "v_mfma_f32_16x16x32_bf16 v[220:223], v[224:227], v[112:115], v[220:223]\n\t"
    "v_cvt_pk_bf16_f32 v140, v32, v33\n\t"
    "v_cvt_pk_bf16_f32 v141, v34, v35\n\t"
    "v_mfma_f32_16x16x32_bf16 v[220:223], v[224:227], v[116:119], v[220:223]\n\t"
    "v_cvt_pk_bf16_f32 v172, v40, v41\n\t"
    "v_cvt_pk_bf16_f32 v173, v42, v43\n\t"
    "s_waitcnt vmcnt(0) lgkmcnt(0)\n\t"
    "s_barrier\n\t"
    "v_mfma_f32_16x16x32_bf16 v[48:51], a[208:211], a[128:131], 0\n\t"
    "v_cvt_pk_bf16_f32 v36, 0, v36\n\t"
    "v_cvt_pk_bf16_f32 v37, 0, v37\n\t"
    "v_cvt_pk_bf16_f32 v38, 0, v38\n\t"
    "v_mfma_f32_16x16x32_bf16 v[48:51], a[212:215], a[132:135], v[48:51]\n\t"
    "v_cvt_pk_bf16_f32 v39, 0, v39\n\t"
    "v_mul_f32 v36, s56, v36\n\t"
    "v_mfma_f32_16x16x32_bf16 v[48:51], a[216:219], a[136:139], v[48:51]\n\t"
    "v_mul_f32 v37, s56, v37\n\t"
    "v_mul_f32 v38, s56, v38\n\t"
    "v_mul_f32 v39, s56, v39\n\t"
    "v_mfma_f32_16x16x32_bf16 v[48:51], a[220:223], a[140:143], v[48:51]\n\t"
    "s_add_u32 m0, s49, 0\n\t"
    "s_mov_b32 s48, 0\n\t"
    "buffer_load_dwordx4 %[osrc0], %[srdWo], s48 offen offset:0 lds\n\t"
    "v_mfma_f32_16x16x32_bf16 v[52:55], a[208:211], a[144:147], 0\n\t"
    "v_cvt_pk_bf16_f32 v36, 0, v36\n\t"
    "v_mfma_f32_16x16x32_bf16 v[52:55], a[212:215], a[148:151], v[52:55]\n\t"
    "v_cvt_pk_bf16_f32 v37, 0, v37\n\t"
    "v_cvt_pk_bf16_f32 v38, 0, v38\n\t"
    "v_cvt_pk_bf16_f32 v39, 0, v39\n\t"
    "v_mfma_f32_16x16x32_bf16 v[52:55], a[216:219], a[152:155], v[52:55]\n\t"
    "v_fma_f32 v36, v36, s58, v193\n\t"
    "v_fma_f32 v37, v37, s58, v193\n\t"
    "v_mfma_f32_16x16x32_bf16 v[52:55], a[220:223], a[156:159], v[52:55]\n\t"
    "v_fma_f32 v38, v38, s58, v193\n\t"
    "v_fma_f32 v39, v39, s58, v193\n\t"
    "v_exp_f32 v36, v36\n\t"
    "v_mfma_f32_16x16x32_bf16 v[56:59], a[208:211], a[160:163], v[196:199]\n\t"
    "v_exp_f32 v37, v37\n\t"
    "v_mfma_f32_16x16x32_bf16 v[56:59], a[212:215], a[164:167], v[56:59]\n\t"
    "v_exp_f32 v38, v38\n\t"
    "v_mfma_f32_16x16x32_bf16 v[56:59], a[216:219], a[168:171], v[56:59]\n\t"
    "v_exp_f32 v39, v39\n\t"
    "v_mfma_f32_16x16x32_bf16 v[56:59], a[220:223], a[172:175], v[56:59]\n\t"
    "buffer_load_dwordx4 v236, %[srdWo], s48 offen offset:1024 lds\n\t"
    "v_exp_f32 v44, v44\n\t"
    "v_mfma_f32_16x16x32_bf16 v[60:63], a[208:211], a[176:179], v[200:203]\n\t"
    "v_exp_f32 v45, v45\n\t"
    "v_mfma_f32_16x16x32_bf16 v[60:63], a[212:215], a[180:183], v[60:63]\n\t"
    "v_exp_f32 v46, v46\n\t"
    "v_mfma_f32_16x16x32_bf16 v[60:63], a[216:219], a[184:187], v[60:63]\n\t"
    "v_exp_f32 v47, v47\n\t"
    "v_cvt_pk_bf16_f32 v156, v36, v37\n\t"
    "v_mfma_f32_16x16x32_bf16 v[60:63], a[220:223], a[188:191], v[60:63]\n\t"
    "v_cvt_pk_bf16_f32 v157, v38, v39\n\t"
    "v_cvt_pk_bf16_f32 v188, v44, v45\n\t"
    "v_cvt_pk_bf16_f32 v189, v46, v47\n\t"
    "s_waitcnt lgkmcnt(0)\n\t"
    "v_mfma_f32_16x16x32_bf16 a[112:115], a[240:243], v[64:67], a[112:115]\n\t"
    "v_cvt_pk_bf16_f32 v48, 0, v48\n\t"
    "v_cvt_pk_bf16_f32 v49, 0, v49\n\t"
    "v_cvt_pk_bf16_f32 v50, 0, v50\n\t"
    "v_mfma_f32_16x16x32_bf16 a[112:115], a[244:247], v[68:71], a[112:115]\n\t"
    "ds_read_b64_tr_b16 a[224:225], %[vb] offset:32768\n\t"
    "ds_read_b64_tr_b16 a[226:227], %[vb] offset:36864\n\t"
    "v_mfma_f32_16x16x32_bf16 a[112:115], a[248:251], v[72:75], a[112:115]\n\t"
    "v_cvt_pk_bf16_f32 v51, 0, v51\n\t"
    "v_mul_f32 v48, s56, v48\n\t"
    "v_mul_f32 v49, s56, v49\n\t"
    "v_mfma_f32_16x16x32_bf16 a[112:115], a[252:255], v[76:79], a[112:115]\n\t"
    "buffer_load_dwordx4 v237, %[srdWo], s48 offen offset:2048 lds\n\t"
    "v_mul_f32 v50, s56, v50\n\t"
    "v_mfma_f32_16x16x32_bf16 a[116:119], a[240:243], v[80:83], a[116:119]\n\t"
    "v_mul_f32 v51, s56, v51\n\t"
    "v_cvt_pk_bf16_f32 v48, 0, v48\n\t"
    "v_mfma_f32_16x16x32_bf16 a[116:119], a[244:247], v[84:87], a[116:119]\n\t"
    "ds_read_b64_tr_b16 a[228:229], %[vb] offset:40960\n\t"
    "ds_read_b64_tr_b16 a[230:231], %[vb] offset:45056\n\t"
    "v_cvt_pk_bf16_f32 v49, 0, v49\n\t"
    "v_mfma_f32_16x16x32_bf16 a[116:119], a[248:251], v[88:91], a[116:119]\n\t"
    "v_cvt_pk_bf16_f32 v50, 0, v50\n\t"
    "v_cvt_pk_bf16_f32 v51, 0, v51\n\t"
    "v_fma_f32 v48, v48, s58, v192\n\t"
    "v_mfma_f32_16x16x32_bf16 a[116:119], a[252:255], v[92:95], a[116:119]\n\t"
    "v_fma_f32 v49, v49, s58, v192\n\t"
    "v_fma_f32 v50, v50, s58, v192\n\t"
    "v_mfma_f32_16x16x32_bf16 a[120:123], a[240:243], v[96:99], a[120:123]\n\t"
    "v_fma_f32 v51, v51, s58, v192\n\t"
    "v_exp_f32 v48, v48\n\t"
    "v_mfma_f32_16x16x32_bf16 a[120:123], a[244:247], v[100:103], a[120:123]\n\t"
    "ds_read_b64_tr_b16 a[232:233], %[vb] offset:49152\n\t"
    "ds_read_b64_tr_b16 a[234:235], %[vb] offset:53248\n\t"
    "v_mfma_f32_16x16x32_bf16 a[120:123], a[248:251], v[104:107], a[120:123]\n\t"
    "v_exp_f32 v49, v49\n\t"
    "v_exp_f32 v50, v50\n\t"
    "v_mfma_f32_16x16x32_bf16 a[120:123], a[252:255], v[108:111], a[120:123]\n\t"
    "buffer_load_dwordx4 v238, %[srdWo], s48 offen offset:3072 lds\n\t"
    "v_mfma_f32_16x16x32_bf16 a[124:127], a[240:243], v[112:115], a[124:127]\n\t"
    "v_exp_f32 v51, v51\n\t"
    "v_mfma_f32_16x16x32_bf16 a[124:127], a[244:247], v[116:119], a[124:127]\n\t"
    "ds_read_b64_tr_b16 a[236:237], %[vb] offset:57344\n\t"
    "ds_read_b64_tr_b16 a[238:239], %[vb] offset:61440\n\t"
    "v_exp_f32 v56, v56\n\t"
    "v_mfma_f32_16x16x32_bf16 a[124:127], a[248:251], v[120:123], a[124:127]\n\t"
    "v_exp_f32 v57, v57\n\t"
    "v_mfma_f32_16x16x32_bf16 a[124:127], a[252:255], v[124:127], a[124:127]\n\t"
    "v_exp_f32 v58, v58\n\t"
    "v_mfma_f32_16x16x32_bf16 v[220:223], v[224:227], v[120:123], v[220:223]\n\t"
    "v_exp_f32 v59, v59\n\t"
    "v_cvt_pk_bf16_f32 v142, v48, v49\n\t"
    "v_mfma_f32_16x16x32_bf16 v[220:223], v[224:227], v[124:127], v[220:223]\n\t"
    "v_cvt_pk_bf16_f32 v143, v50, v51\n\t"
    "v_cvt_pk_bf16_f32 v174, v56, v57\n\t"
    "v_cvt_pk_bf16_f32 v175, v58, v59\n\t"
    "v_cvt_pk_bf16_f32 v52, 0, v52\n\t"
    "v_cvt_pk_bf16_f32 v53, 0, v53\n\t"
    "v_cvt_pk_bf16_f32 v54, 0, v54\n\t"
    "v_cvt_pk_bf16_f32 v55, 0, v55\n\t"
    "v_mul_f32 v52, s56, v52\n\t"
    "v_mul_f32 v53, s56, v53\n\t"
    "v_mul_f32 v54, s56, v54\n\t"
    "v_mul_f32 v55, s56, v55\n\t"
    "v_cvt_pk_bf16_f32 v52, 0, v52\n\t"
    "v_cvt_pk_bf16_f32 v53, 0, v53\n\t"
    "v_cvt_pk_bf16_f32 v54, 0, v54\n\t"
    "v_cvt_pk_bf16_f32 v55, 0, v55\n\t"
    "v_fma_f32 v52, v52, s58, v193\n\t"
    "v_fma_f32 v53, v53, s58, v193\n\t"
    "v_fma_f32 v54, v54, s58, v193\n\t"
    "v_fma_f32 v55, v55, s58, v193\n\t"
    "v_exp_f32 v52, v52\n\t"
    "v_exp_f32 v53, v53\n\t"
    "v_exp_f32 v54, v54\n\t"
    "v_exp_f32 v55, v55\n\t"
    "v_exp_f32 v60, v60\n\t"
    "v_exp_f32 v61, v61\n\t"
    "v_exp_f32 v62, v62\n\t"
    "v_exp_f32 v63, v63\n\t"
    "v_cvt_pk_bf16_f32 v158, v52, v53\n\t"
    "v_cvt_pk_bf16_f32 v159, v54, v55\n\t"
    "v_cvt_pk_bf16_f32 v190, v60, v61\n\t"
    "v_cvt_pk_bf16_f32 v191, v62, v63\n\t"
    "s_waitcnt lgkmcnt(0)\n\t"
    "s_nop 1\n\t"
    "v_mfma_f32_16x16x32_bf16 a[0:3], a[224:227], v[128:131], a[0:3]\n\t"
    "v_mfma_f32_16x16x32_bf16 a[0:3], a[228:231], v[132:135], a[0:3]\n\t"
    "ds_read_b64_tr_b16 a[240:241], %[vb] offset:33024\n\t"
    "v_mfma_f32_16x16x32_bf16 a[0:3], a[232:235], v[136:139], a[0:3]\n\t"
    "v_mfma_f32_16x16x32_bf16 a[0:3], a[236:239], v[140:143], a[0:3]\n\t"
    "ds_read_b64_tr_b16 a[242:243], %[vb] offset:37120\n\t"
    "s_add_u32 m0, s49, 4096\n\t"
    "s_mov_b32 s48, 36864\n\t"
    "buffer_load_dwordx4 %[osrc0], %[srdWo], s48 offen offset:0 lds\n\t"
    "v_mfma_f32_16x16x32_bf16 a[4:7], a[224:227], v[144:147], a[4:7]\n\t"
    "v_mfma_f32_16x16x32_bf16 a[4:7], a[228:231], v[148:151], a[4:7]\n\t"
    "ds_read_b64_tr_b16 a[244:245], %[vb] offset:41216\n\t"
    "v_mfma_f32_16x16x32_bf16 a[4:7], a[232:235], v[152:155], a[4:7]\n\t"
    "v_mfma_f32_16x16x32_bf16 a[4:7], a[236:239], v[156:159], a[4:7]\n\t"
    "ds_read_b64_tr_b16 a[246:247], %[vb] offset:45312\n\t"
    "buffer_load_dwordx4 v236, %[srdWo], s48 offen offset:1024 lds\n\t"
    "v_mfma_f32_16x16x32_bf16 a[8:11], a[224:227], v[160:163], a[8:11]\n\t"
    "v_mfma_f32_16x16x32_bf16 a[8:11], a[228:231], v[164:167], a[8:11]\n\t"
    "ds_read_b64_tr_b16 a[248:249], %[vb] offset:49408\n\t"
    "v_mfma_f32_16x16x32_bf16 a[8:11], a[232:235], v[168:171], a[8:11]\n\t"
    "v_mfma_f32_16x16x32_bf16 a[8:11], a[236:239], v[172:175], a[8:11]\n\t"
    "ds_read_b64_tr_b16 a[250:251], %[vb] offset:53504\n\t"
    "buffer_load_dwordx4 v237, %[srdWo], s48 offen offset:2048 lds\n\t"
    "v_mfma_f32_16x16x32_bf16 a[12:15], a[224:227], v[176:179], a[12:15]\n\t"
    "v_mfma_f32_16x16x32_bf16 a[12:15], a[228:231], v[180:183], a[12:15]\n\t"
    "ds_read_b64_tr_b16 a[252:253], %[vb] offset:57600\n\t"
    "v_mfma_f32_16x16x32_bf16 a[12:15], a[232:235], v[184:187], a[12:15]\n\t"
    "v_mfma_f32_16x16x32_bf16 a[12:15], a[236:239], v[188:191], a[12:15]\n\t"
    "ds_read_b64_tr_b16 a[254:255], %[vb] offset:61696\n\t"
    "buffer_load_dwordx4 v238, %[srdWo], s48 offen offset:3072 lds\n\t"
    "v_mfma_f32_16x16x32_bf16 v[208:211], v[224:227], v[128:131], v[208:211]\n\t"
    "v_mfma_f32_16x16x32_bf16 v[208:211], v[224:227], v[132:135], v[208:211]\n\t"
    "s_waitcnt lgkmcnt(0)\n\t"
    "v_mfma_f32_16x16x32_bf16 a[16:19], a[240:243], v[128:131], a[16:19]\n\t"
    "v_mfma_f32_16x16x32_bf16 a[16:19], a[244:247], v[132:135], a[16:19]\n\t"
    "ds_read_b64_tr_b16 a[224:225], %[vb] offset:33280\n\t"
    "v_mfma_f32_16x16x32_bf16 a[16:19], a[248:251], v[136:139], a[16:19]\n\t"
    "v_mfma_f32_16x16x32_bf16 a[16:19], a[252:255], v[140:143], a[16:19]\n\t"
    "ds_read_b64_tr_b16 a[226:227], %[vb] offset:37376\n\t"
    "s_add_u32 m0, s49, 32768\n\t"
    "s_mov_b32 s48, 128\n\t"
    "buffer_load_dwordx4 %[osrc0], %[srdWo], s48 offen offset:0 lds\n\t"
    "v_mfma_f32_16x16x32_bf16 a[20:23], a[240:243], v[144:147], a[20:23]\n\t"
    "v_mfma_f32_16x16x32_bf16 a[20:23], a[244:247], v[148:151], a[20:23]\n\t"
    "ds_read_b64_tr_b16 a[228:229], %[vb] offset:41472\n\t"
    "v_mfma_f32_16x16x32_bf16 a[20:23], a[248:251], v[152:155], a[20:23]\n\t"
    "v_mfma_f32_16x16x32_bf16 a[20:23], a[252:255], v[156:159], a[20:23]\n\t"
    "ds_read_b64_tr_b16 a[230:231], %[vb] offset:45568\n\t"
    "buffer_load_dwordx4 v236, %[srdWo], s48 offen offset:1024 lds\n\t"
    "v_mfma_f32_16x16x32_bf16 a[24:27], a[240:243], v[160:163], a[24:27]\n\t"
    "v_mfma_f32_16x16x32_bf16 a[24:27], a[244:247], v[164:167], a[24:27]\n\t"
    "ds_read_b64_tr_b16 a[232:233], %[vb] offset:49664\n\t"
    "v_mfma_f32_16x16x32_bf16 a[24:27], a[248:251], v[168:171], a[24:27]\n\t"
    "v_mfma_f32_16x16x32_bf16 a[24:27], a[252:255], v[172:175], a[24:27]\n\t"
    "ds_read_b64_tr_b16 a[234:235], %[vb] offset:53760\n\t"
    "buffer_load_dwordx4 v237, %[srdWo], s48 offen offset:2048 lds\n\t"
    "v_mfma_f32_16x16x32_bf16 a[28:31], a[240:243], v[176:179], a[28:31]\n\t"
    "v_mfma_f32_16x16x32_bf16 a[28:31], a[244:247], v[180:183], a[28:31]\n\t"
    "ds_read_b64_tr_b16 a[236:237], %[vb] offset:57856\n\t"
    "v_mfma_f32_16x16x32_bf16 a[28:31], a[248:251], v[184:187], a[28:31]\n\t"
    "v_mfma_f32_16x16x32_bf16 a[28:31], a[252:255], v[188:191], a[28:31]\n\t"
    "ds_read_b64_tr_b16 a[238:239], %[vb] offset:61952\n\t"
    "buffer_load_dwordx4 v238, %[srdWo], s48 offen offset:3072 lds\n\t"
    "v_mfma_f32_16x16x32_bf16 v[208:211], v[224:227], v[136:139], v[208:211]\n\t"
    "v_mfma_f32_16x16x32_bf16 v[208:211], v[224:227], v[140:143], v[208:211]\n\t"
    "s_waitcnt lgkmcnt(0)\n\t"
    "v_mfma_f32_16x16x32_bf16 a[32:35], a[224:227], v[128:131], a[32:35]\n\t"
    "v_mfma_f32_16x16x32_bf16 a[32:35], a[228:231], v[132:135], a[32:35]\n\t"
    "ds_read_b64_tr_b16 a[240:241], %[vb] offset:33536\n\t"
    "v_mfma_f32_16x16x32_bf16 a[32:35], a[232:235], v[136:139], a[32:35]\n\t"
    "v_mfma_f32_16x16x32_bf16 a[32:35], a[236:239], v[140:143], a[32:35]\n\t"
    "ds_read_b64_tr_b16 a[242:243], %[vb] offset:37632\n\t"
    "s_add_u32 m0, s49, 36864\n\t"
    "s_mov_b32 s48, 36992\n\t"
    "buffer_load_dwordx4 %[osrc0], %[srdWo], s48 offen offset:0 lds\n\t"
    "v_mfma_f32_16x16x32_bf16 a[36:39], a[224:227], v[144:147], a[36:39]\n\t"
    "v_mfma_f32_16x16x32_bf16 a[36:39], a[228:231], v[148:151], a[36:39]\n\t"
    "ds_read_b64_tr_b16 a[244:245], %[vb] offset:41728\n\t"
    "v_mfma_f32_16x16x32_bf16 a[36:39], a[232:235], v[152:155], a[36:39]\n\t"
    "v_mfma_f32_16x16x32_bf16 a[36:39], a[236:239], v[156:159], a[36:39]\n\t"
    "ds_read_b64_tr_b16 a[246:247], %[vb] offset:45824\n\t"
    "buffer_load_dwordx4 v236, %[srdWo], s48 offen offset:1024 lds\n\t"
    "v_mfma_f32_16x16x32_bf16 a[40:43], a[224:227], v[160:163], a[40:43]\n\t"
    "v_mfma_f32_16x16x32_bf16 a[40:43], a[228:231], v[164:167], a[40:43]\n\t"
    "ds_read_b64_tr_b16 a[248:249], %[vb] offset:49920\n\t"
    "v_mfma_f32_16x16x32_bf16 a[40:43], a[232:235], v[168:171], a[40:43]\n\t"
    "v_mfma_f32_16x16x32_bf16 a[40:43], a[236:239], v[172:175], a[40:43]\n\t"
    "ds_read_b64_tr_b16 a[250:251], %[vb] offset:54016\n\t"
    "buffer_load_dwordx4 v237, %[srdWo], s48 offen offset:2048 lds\n\t"
    "v_mfma_f32_16x16x32_bf16 a[44:47], a[224:227], v[176:179], a[44:47]\n\t"
    "v_mfma_f32_16x16x32_bf16 a[44:47], a[228:231], v[180:183], a[44:47]\n\t"
    "ds_read_b64_tr_b16 a[252:253], %[vb] offset:58112\n\t"
    "v_mfma_f32_16x16x32_bf16 a[44:47], a[232:235], v[184:187], a[44:47]\n\t"
    "v_mfma_f32_16x16x32_bf16 a[44:47], a[236:239], v[188:191], a[44:47]\n\t"
    "ds_read_b64_tr_b16 a[254:255], %[vb] offset:62208\n\t"
    "buffer_load_dwordx4 v238, %[srdWo], s48 offen offset:3072 lds\n\t"
    "v_mfma_f32_16x16x32_bf16 v[212:215], v[224:227], v[144:147], v[212:215]\n\t"
    "v_mfma_f32_16x16x32_bf16 v[212:215], v[224:227], v[148:151], v[212:215]\n\t"
    "s_waitcnt lgkmcnt(0)\n\t"
    "v_mfma_f32_16x16x32_bf16 a[48:51], a[240:243], v[128:131], a[48:51]\n\t"
    "v_mfma_f32_16x16x32_bf16 a[48:51], a[244:247], v[132:135], a[48:51]\n\t"
    "ds_read_b64_tr_b16 a[224:225], %[vb] offset:33792\n\t"
    "v_mfma_f32_16x16x32_bf16 a[48:51], a[248:251], v[136:139], a[48:51]\n\t"
    "v_mfma_f32_16x16x32_bf16 a[48:51], a[252:255], v[140:143], a[48:51]\n\t"
    "ds_read_b64_tr_b16 a[226:227], %[vb] offset:37888\n\t"
    "s_add_u32 m0, s49, 65536\n\t"
    "s_mov_b32 s48, 256\n\t"
    "buffer_load_dwordx4 %[osrc0], %[srdWo], s48 offen offset:0 lds\n\t"
    "v_mfma_f32_16x16x32_bf16 a[52:55], a[240:243], v[144:147], a[52:55]\n\t"
    "v_mfma_f32_16x16x32_bf16 a[52:55], a[244:247], v[148:151], a[52:55]\n\t"
    "ds_read_b64_tr_b16 a[228:229], %[vb] offset:41984\n\t"
    "v_mfma_f32_16x16x32_bf16 a[52:55], a[248:251], v[152:155], a[52:55]\n\t"
    "v_mfma_f32_16x16x32_bf16 a[52:55], a[252:255], v[156:159], a[52:55]\n\t"
    "ds_read_b64_tr_b16 a[230:231], %[vb] offset:46080\n\t"
    "buffer_load_dwordx4 v236, %[srdWo], s48 offen offset:1024 lds\n\t"
    "v_mfma_f32_16x16x32_bf16 a[56:59], a[240:243], v[160:163], a[56:59]\n\t"
    "v_mfma_f32_16x16x32_bf16 a[56:59], a[244:247], v[164:167], a[56:59]\n\t"
    "ds_read_b64_tr_b16 a[232:233], %[vb] offset:50176\n\t"
    "v_mfma_f32_16x16x32_bf16 a[56:59], a[248:251], v[168:171], a[56:59]\n\t"
    "v_mfma_f32_16x16x32_bf16 a[56:59], a[252:255], v[172:175], a[56:59]\n\t"
    "ds_read_b64_tr_b16 a[234:235], %[vb] offset:54272\n\t"
    "buffer_load_dwordx4 v237, %[srdWo], s48 offen offset:2048 lds\n\t"
    "v_mfma_f32_16x16x32_bf16 a[60:63], a[240:243], v[176:179], a[60:63]\n\t"
    "v_mfma_f32_16x16x32_bf16 a[60:63], a[244:247], v[180:183], a[60:63]\n\t"
    "ds_read_b64_tr_b16 a[236:237], %[vb] offset:58368\n\t"
    "v_mfma_f32_16x16x32_bf16 a[60:63], a[248:251], v[184:187], a[60:63]\n\t"
    "v_mfma_f32_16x16x32_bf16 a[60:63], a[252:255], v[188:191], a[60:63]\n\t"
    "ds_read_b64_tr_b16 a[238:239], %[vb] offset:62464\n\t"
    "buffer_load_dwordx4 v238, %[srdWo], s48 offen offset:3072 lds\n\t"
    "v_mfma_f32_16x16x32_bf16 v[212:215], v[224:227], v[152:155], v[212:215]\n\t"
    "v_mfma_f32_16x16x32_bf16 v[212:215], v[224:227], v[156:159], v[212:215]\n\t"
    "s_waitcnt lgkmcnt(0)\n\t"
    "v_mfma_f32_16x16x32_bf16 a[64:67], a[224:227], v[128:131], a[64:67]\n\t"
    "v_mfma_f32_16x16x32_bf16 a[64:67], a[228:231], v[132:135], a[64:67]\n\t"
    "ds_read_b64_tr_b16 a[240:241], %[vb] offset:34048\n\t"
    "v_mfma_f32_16x16x32_bf16 a[64:67], a[232:235], v[136:139], a[64:67]\n\t"
    "v_mfma_f32_16x16x32_bf16 a[64:67], a[236:239], v[140:143], a[64:67]\n\t"
    "ds_read_b64_tr_b16 a[242:243], %[vb] offset:38144\n\t"
    "s_add_u32 m0, s49, 69632\n\t"
    "s_mov_b32 s48, 37120\n\t"
    "buffer_load_dwordx4 %[osrc0], %[srdWo], s48 offen offset:0 lds\n\t"
    "v_mfma_f32_16x16x32_bf16 a[68:71], a[224:227], v[144:147], a[68:71]\n\t"
    "v_mfma_f32_16x16x32_bf16 a[68:71], a[228:231], v[148:151], a[68:71]\n\t"
    "ds_read_b64_tr_b16 a[244:245], %[vb] offset:42240\n\t"
    "v_mfma_f32_16x16x32_bf16 a[68:71], a[232:235], v[152:155], a[68:71]\n\t"
    "v_mfma_f32_16x16x32_bf16 a[68:71], a[236:239], v[156:159], a[68:71]\n\t"
    "ds_read_b64_tr_b16 a[246:247], %[vb] offset:46336\n\t"
    "buffer_load_dwordx4 v236, %[srdWo], s48 offen offset:1024 lds\n\t"
    "v_mfma_f32_16x16x32_bf16 a[72:75], a[224:227], v[160:163], a[72:75]\n\t"
    "v_mfma_f32_16x16x32_bf16 a[72:75], a[228:231], v[164:167], a[72:75]\n\t"
    "ds_read_b64_tr_b16 a[248:249], %[vb] offset:50432\n\t"
    "v_mfma_f32_16x16x32_bf16 a[72:75], a[232:235], v[168:171], a[72:75]\n\t"
    "v_mfma_f32_16x16x32_bf16 a[72:75], a[236:239], v[172:175], a[72:75]\n\t"
    "ds_read_b64_tr_b16 a[250:251], %[vb] offset:54528\n\t"
    "buffer_load_dwordx4 v237, %[srdWo], s48 offen offset:2048 lds\n\t"
    "v_mfma_f32_16x16x32_bf16 a[76:79], a[224:227], v[176:179], a[76:79]\n\t"
    "v_mfma_f32_16x16x32_bf16 a[76:79], a[228:231], v[180:183], a[76:79]\n\t"
    "ds_read_b64_tr_b16 a[252:253], %[vb] offset:58624\n\t"
    "v_mfma_f32_16x16x32_bf16 a[76:79], a[232:235], v[184:187], a[76:79]\n\t"
    "v_mfma_f32_16x16x32_bf16 a[76:79], a[236:239], v[188:191], a[76:79]\n\t"
    "ds_read_b64_tr_b16 a[254:255], %[vb] offset:62720\n\t"
    "buffer_load_dwordx4 v238, %[srdWo], s48 offen offset:3072 lds\n\t"
    "v_mfma_f32_16x16x32_bf16 v[216:219], v[224:227], v[160:163], v[216:219]\n\t"
    "v_mfma_f32_16x16x32_bf16 v[216:219], v[224:227], v[164:167], v[216:219]\n\t"
    "s_waitcnt lgkmcnt(0)\n\t"
    "v_mfma_f32_16x16x32_bf16 a[80:83], a[240:243], v[128:131], a[80:83]\n\t"
    "v_mfma_f32_16x16x32_bf16 a[80:83], a[244:247], v[132:135], a[80:83]\n\t"
    "ds_read_b64_tr_b16 a[224:225], %[vb] offset:34304\n\t"
    "v_mfma_f32_16x16x32_bf16 a[80:83], a[248:251], v[136:139], a[80:83]\n\t"
    "v_mfma_f32_16x16x32_bf16 a[80:83], a[252:255], v[140:143], a[80:83]\n\t"
    "ds_read_b64_tr_b16 a[226:227], %[vb] offset:38400\n\t"
    "s_add_u32 m0, s49, 131072\n\t"
    "s_mov_b32 s48, 384\n\t"
    "buffer_load_dwordx4 %[osrc0], %[srdWo], s48 offen offset:0 lds\n\t"
    "v_mfma_f32_16x16x32_bf16 a[84:87], a[240:243], v[144:147], a[84:87]\n\t"
    "v_mfma_f32_16x16x32_bf16 a[84:87], a[244:247], v[148:151], a[84:87]\n\t"
    "ds_read_b64_tr_b16 a[228:229], %[vb] offset:42496\n\t"
    "v_mfma_f32_16x16x32_bf16 a[84:87], a[248:251], v[152:155], a[84:87]\n\t"
    "v_mfma_f32_16x16x32_bf16 a[84:87], a[252:255], v[156:159], a[84:87]\n\t"
    "ds_read_b64_tr_b16 a[230:231], %[vb] offset:46592\n\t"
    "buffer_load_dwordx4 v236, %[srdWo], s48 offen offset:1024 lds\n\t"
    "v_mfma_f32_16x16x32_bf16 a[88:91], a[240:243], v[160:163], a[88:91]\n\t"
    "v_mfma_f32_16x16x32_bf16 a[88:91], a[244:247], v[164:167], a[88:91]\n\t"
    "ds_read_b64_tr_b16 a[232:233], %[vb] offset:50688\n\t"
    "v_mfma_f32_16x16x32_bf16 a[88:91], a[248:251], v[168:171], a[88:91]\n\t"
    "v_mfma_f32_16x16x32_bf16 a[88:91], a[252:255], v[172:175], a[88:91]\n\t"
    "ds_read_b64_tr_b16 a[234:235], %[vb] offset:54784\n\t"
    "buffer_load_dwordx4 v237, %[srdWo], s48 offen offset:2048 lds\n\t"
    "v_mfma_f32_16x16x32_bf16 a[92:95], a[240:243], v[176:179], a[92:95]\n\t"
    "v_mfma_f32_16x16x32_bf16 a[92:95], a[244:247], v[180:183], a[92:95]\n\t"
    "ds_read_b64_tr_b16 a[236:237], %[vb] offset:58880\n\t"
    "v_mfma_f32_16x16x32_bf16 a[92:95], a[248:251], v[184:187], a[92:95]\n\t"
    "v_mfma_f32_16x16x32_bf16 a[92:95], a[252:255], v[188:191], a[92:95]\n\t"
    "ds_read_b64_tr_b16 a[238:239], %[vb] offset:62976\n\t"
    "buffer_load_dwordx4 v238, %[srdWo], s48 offen offset:3072 lds\n\t"
    "v_mfma_f32_16x16x32_bf16 v[216:219], v[224:227], v[168:171], v[216:219]\n\t"
    "v_mfma_f32_16x16x32_bf16 v[216:219], v[224:227], v[172:175], v[216:219]\n\t"
    "s_waitcnt lgkmcnt(0)\n\t"
    "v_mfma_f32_16x16x32_bf16 a[96:99], a[224:227], v[128:131], a[96:99]\n\t"
    "v_mfma_f32_16x16x32_bf16 a[96:99], a[228:231], v[132:135], a[96:99]\n\t"
    "ds_read_b64_tr_b16 a[240:241], %[vb] offset:34560\n\t"
    "v_mfma_f32_16x16x32_bf16 a[96:99], a[232:235], v[136:139], a[96:99]\n\t"
    "v_mfma_f32_16x16x32_bf16 a[96:99], a[236:239], v[140:143], a[96:99]\n\t"
    "ds_read_b64_tr_b16 a[242:243], %[vb] offset:38656\n\t"
    "s_add_u32 m0, s49, 135168\n\t"
    "s_mov_b32 s48, 37248\n\t"
    "buffer_load_dwordx4 %[osrc0], %[srdWo], s48 offen offset:0 lds\n\t"
    "v_mfma_f32_16x16x32_bf16 a[100:103], a[224:227], v[144:147], a[100:103]\n\t"
    "v_mfma_f32_16x16x32_bf16 a[100:103], a[228:231], v[148:151], a[100:103]\n\t"
    "ds_read_b64_tr_b16 a[244:245], %[vb] offset:42752\n\t"
    "v_mfma_f32_16x16x32_bf16 a[100:103], a[232:235], v[152:155], a[100:103]\n\t"
    "v_mfma_f32_16x16x32_bf16 a[100:103], a[236:239], v[156:159], a[100:103]\n\t"
    "ds_read_b64_tr_b16 a[246:247], %[vb] offset:46848\n\t"
    "buffer_load_dwordx4 v236, %[srdWo], s48 offen offset:1024 lds\n\t"
    "v_mfma_f32_16x16x32_bf16 a[104:107], a[224:227], v[160:163], a[104:107]\n\t"
    "v_mfma_f32_16x16x32_bf16 a[104:107], a[228:231], v[164:167], a[104:107]\n\t"
    "ds_read_b64_tr_b16 a[248:249], %[vb] offset:50944\n\t"
    "v_mfma_f32_16x16x32_bf16 a[104:107], a[232:235], v[168:171], a[104:107]\n\t"
    "v_mfma_f32_16x16x32_bf16 a[104:107], a[236:239], v[172:175], a[104:107]\n\t"
    "ds_read_b64_tr_b16 a[250:251], %[vb] offset:55040\n\t"
    "buffer_load_dwordx4 v237, %[srdWo], s48 offen offset:2048 lds\n\t"
    "v_mfma_f32_16x16x32_bf16 a[108:111], a[224:227], v[176:179], a[108:111]\n\t"
    "v_mfma_f32_16x16x32_bf16 a[108:111], a[228:231], v[180:183], a[108:111]\n\t"
    "ds_read_b64_tr_b16 a[252:253], %[vb] offset:59136\n\t"
    "v_mfma_f32_16x16x32_bf16 a[108:111], a[232:235], v[184:187], a[108:111]\n\t"
    "v_mfma_f32_16x16x32_bf16 a[108:111], a[236:239], v[188:191], a[108:111]\n\t"
    "ds_read_b64_tr_b16 a[254:255], %[vb] offset:63232\n\t"
    "buffer_load_dwordx4 v238, %[srdWo], s48 offen offset:3072 lds\n\t"
    "v_mfma_f32_16x16x32_bf16 v[220:223], v[224:227], v[176:179], v[220:223]\n\t"
    "v_mfma_f32_16x16x32_bf16 v[220:223], v[224:227], v[180:183], v[220:223]\n\t"
    "s_waitcnt lgkmcnt(0)\n\t"
    "v_mfma_f32_16x16x32_bf16 a[112:115], a[240:243], v[128:131], a[112:115]\n\t"
    "v_mfma_f32_16x16x32_bf16 a[112:115], a[244:247], v[132:135], a[112:115]\n\t"
    "v_mfma_f32_16x16x32_bf16 a[112:115], a[248:251], v[136:139], a[112:115]\n\t"
    "v_mfma_f32_16x16x32_bf16 a[112:115], a[252:255], v[140:143], a[112:115]\n\t"
    "v_mfma_f32_16x16x32_bf16 a[116:119], a[240:243], v[144:147], a[116:119]\n\t"
    "v_mfma_f32_16x16x32_bf16 a[116:119], a[244:247], v[148:151], a[116:119]\n\t"
    "v_mfma_f32_16x16x32_bf16 a[116:119], a[248:251], v[152:155], a[116:119]\n\t"
    "v_mfma_f32_16x16x32_bf16 a[116:119], a[252:255], v[156:159], a[116:119]\n\t"
    "v_mfma_f32_16x16x32_bf16 a[120:123], a[240:243], v[160:163], a[120:123]\n\t"
    "v_mfma_f32_16x16x32_bf16 a[120:123], a[244:247], v[164:167], a[120:123]\n\t"
    "v_mfma_f32_16x16x32_bf16 a[120:123], a[248:251], v[168:171], a[120:123]\n\t"
    "v_mfma_f32_16x16x32_bf16 a[120:123], a[252:255], v[172:175], a[120:123]\n\t"
    "v_mfma_f32_16x16x32_bf16 a[124:127], a[240:243], v[176:179], a[124:127]\n\t"
    "v_mfma_f32_16x16x32_bf16 a[124:127], a[244:247], v[180:183], a[124:127]\n\t"
    "v_mfma_f32_16x16x32_bf16 a[124:127], a[248:251], v[184:187], a[124:127]\n\t"
    "v_mfma_f32_16x16x32_bf16 a[124:127], a[252:255], v[188:191], a[124:127]\n\t"
    "v_mfma_f32_16x16x32_bf16 v[220:223], v[224:227], v[184:187], v[220:223]\n\t"
    "v_mfma_f32_16x16x32_bf16 v[220:223], v[224:227], v[188:191], v[220:223]\n\t"
    "s_waitcnt lgkmcnt(0)\n\t"
    "s_barrier\n\t"
    "NORM%=:\n\t"
    "s_nop 7\n\t"
    "v_rcp_f32 v232, v208\n\t"
    "v_rcp_f32 v233, v212\n\t"
    "v_rcp_f32 v234, v216\n\t"
    "v_rcp_f32 v235, v220\n\t"
    "v_mov_b32 v252, 0\n\t"
    "v_mov_b32 v253, 0\n\t"
    "v_mov_b32 v254, 0\n\t"
    "v_mov_b32 v255, 0\n\t"
    "v_accvgpr_read_b32 v32, a0\n\t"
    "v_accvgpr_read_b32 v33, a1\n\t"
    "v_accvgpr_read_b32 v34, a2\n\t"
    "v_accvgpr_read_b32 v35, a3\n\t"
    "v_accvgpr_read_b32 v36, a16\n\t"
    "v_accvgpr_read_b32 v37, a17\n\t"
    "v_accvgpr_read_b32 v38, a18\n\t"
    "v_accvgpr_read_b32 v39, a19\n\t"
    "v_accvgpr_read_b32 v40, a4\n\t"
    "v_accvgpr_read_b32 v41, a5\n\t"
    "v_accvgpr_read_b32 v42, a6\n\t"
    "v_accvgpr_read_b32 v43, a7\n\t"
    "v_accvgpr_read_b32 v44, a20\n\t"
    "v_accvgpr_read_b32 v45, a21\n\t"
    "v_accvgpr_read_b32 v46, a22\n\t"
    "v_accvgpr_read_b32 v47, a23\n\t"
    "v_accvgpr_read_b32 v48, a8\n\t"
    "v_accvgpr_read_b32 v49, a9\n\t"
    "v_accvgpr_read_b32 v50, a10\n\t"
    "v_accvgpr_read_b32 v51, a11\n\t"
    "v_accvgpr_read_b32 v52, a24\n\t"
    "v_accvgpr_read_b32 v53, a25\n\t"
    "v_accvgpr_read_b32 v54, a26\n\t"
    "v_accvgpr_read_b32 v55, a27\n\t"
    "v_accvgpr_read_b32 v56, a12\n\t"
    "v_accvgpr_read_b32 v57, a13\n\t"
    "v_accvgpr_read_b32 v58, a14\n\t"
    "v_accvgpr_read_b32 v59, a15\n\t"
    "v_accvgpr_read_b32 v60, a28\n\t"
    "v_accvgpr_read_b32 v61, a29\n\t"
    "v_accvgpr_read_b32 v62, a30\n\t"
    "v_accvgpr_read_b32 v63, a31\n\t"
    "v_max3_f32 v252, |v32|, |v33|, v252\n\t"
    "v_max3_f32 v252, |v34|, |v35|, v252\n\t"
    "v_max3_f32 v252, |v36|, |v37|, v252\n\t"
    "v_max3_f32 v252, |v38|, |v39|, v252\n\t"
    "v_mul_f32 v32, v32, v232\n\t"
    "v_mul_f32 v33, v33, v232\n\t"
    "v_mul_f32 v34, v34, v232\n\t"
    "v_mul_f32 v35, v35, v232\n\t"
    "v_mul_f32 v36, v36, v232\n\t"
    "v_mul_f32 v37, v37, v232\n\t"
    "v_mul_f32 v38, v38, v232\n\t"
    "v_mul_f32 v39, v39, v232\n\t"
    "v_cvt_pk_bf16_f32 v64, v32, v33\n\t"
    "v_cvt_pk_bf16_f32 v65, v34, v35\n\t"
    "v_cvt_pk_bf16_f32 v66, v36, v37\n\t"
    "v_cvt_pk_bf16_f32 v67, v38, v39\n\t"
    "v_max3_f32 v253, |v40|, |v41|, v253\n\t"
    "v_max3_f32 v253, |v42|, |v43|, v253\n\t"
    "v_max3_f32 v253, |v44|, |v45|, v253\n\t"
    "v_max3_f32 v253, |v46|, |v47|, v253\n\t"
    "v_mul_f32 v40, v40, v233\n\t"
    "v_mul_f32 v41, v41, v233\n\t"
    "v_mul_f32 v42, v42, v233\n\t"
    "v_mul_f32 v43, v43, v233\n\t"
    "v_mul_f32 v44, v44, v233\n\t"
    "v_mul_f32 v45, v45, v233\n\t"
    "v_mul_f32 v46, v46, v233\n\t"
    "v_mul_f32 v47, v47, v233\n\t"
    "v_cvt_pk_bf16_f32 v80, v40, v41\n\t"
    "v_cvt_pk_bf16_f32 v81, v42, v43\n\t"
    "v_cvt_pk_bf16_f32 v82, v44, v45\n\t"
    "v_cvt_pk_bf16_f32 v83, v46, v47\n\t"
    "v_max3_f32 v254, |v48|, |v49|, v254\n\t"
    "v_max3_f32 v254, |v50|, |v51|, v254\n\t"
    "v_max3_f32 v254, |v52|, |v53|, v254\n\t"
    "v_max3_f32 v254, |v54|, |v55|, v254\n\t"
    "v_mul_f32 v48, v48, v234\n\t"
    "v_mul_f32 v49, v49, v234\n\t"
    "v_mul_f32 v50, v50, v234\n\t"
    "v_mul_f32 v51, v51, v234\n\t"
    "v_mul_f32 v52, v52, v234\n\t"
    "v_mul_f32 v53, v53, v234\n\t"
    "v_mul_f32 v54, v54, v234\n\t"
    "v_mul_f32 v55, v55, v234\n\t"
    "v_cvt_pk_bf16_f32 v96, v48, v49\n\t"
    "v_cvt_pk_bf16_f32 v97, v50, v51\n\t"
    "v_cvt_pk_bf16_f32 v98, v52, v53\n\t"
    "v_cvt_pk_bf16_f32 v99, v54, v55\n\t"
    "v_max3_f32 v255, |v56|, |v57|, v255\n\t"
    "v_max3_f32 v255, |v58|, |v59|, v255\n\t"
    "v_max3_f32 v255, |v60|, |v61|, v255\n\t"
    "v_max3_f32 v255, |v62|, |v63|, v255\n\t"
    "v_mul_f32 v56, v56, v235\n\t"
    "v_mul_f32 v57, v57, v235\n\t"
    "v_mul_f32 v58, v58, v235\n\t"
    "v_mul_f32 v59, v59, v235\n\t"
    "v_mul_f32 v60, v60, v235\n\t"
    "v_mul_f32 v61, v61, v235\n\t"
    "v_mul_f32 v62, v62, v235\n\t"
    "v_mul_f32 v63, v63, v235\n\t"
    "v_cvt_pk_bf16_f32 v112, v56, v57\n\t"
    "v_cvt_pk_bf16_f32 v113, v58, v59\n\t"
    "v_cvt_pk_bf16_f32 v114, v60, v61\n\t"
    "v_cvt_pk_bf16_f32 v115, v62, v63\n\t"
    "v_accvgpr_read_b32 v32, a32\n\t"
    "v_accvgpr_read_b32 v33, a33\n\t"
    "v_accvgpr_read_b32 v34, a34\n\t"
    "v_accvgpr_read_b32 v35, a35\n\t"
    "v_accvgpr_read_b32 v36, a48\n\t"
    "v_accvgpr_read_b32 v37, a49\n\t"
    "v_accvgpr_read_b32 v38, a50\n\t"
    "v_accvgpr_read_b32 v39, a51\n\t"
    "v_accvgpr_read_b32 v40, a36\n\t"
    "v_accvgpr_read_b32 v41, a37\n\t"
    "v_accvgpr_read_b32 v42, a38\n\t"
    "v_accvgpr_read_b32 v43, a39\n\t"
    "v_accvgpr_read_b32 v44, a52\n\t"
    "v_accvgpr_read_b32 v45, a53\n\t"
    "v_accvgpr_read_b32 v46, a54\n\t"
    "v_accvgpr_read_b32 v47, a55\n\t"
    "v_accvgpr_read_b32 v48, a40\n\t"
    "v_accvgpr_read_b32 v49, a41\n\t"
    "v_accvgpr_read_b32 v50, a42\n\t"
    "v_accvgpr_read_b32 v51, a43\n\t"
    "v_accvgpr_read_b32 v52, a56\n\t"
    "v_accvgpr_read_b32 v53, a57\n\t"
    "v_accvgpr_read_b32 v54, a58\n\t"
    "v_accvgpr_read_b32 v55, a59\n\t"
    "v_accvgpr_read_b32 v56, a44\n\t"
    "v_accvgpr_read_b32 v57, a45\n\t"
    "v_accvgpr_read_b32 v58, a46\n\t"
    "v_accvgpr_read_b32 v59, a47\n\t"
    "v_accvgpr_read_b32 v60, a60\n\t"
    "v_accvgpr_read_b32 v61, a61\n\t"
    "v_accvgpr_read_b32 v62, a62\n\t"
    "v_accvgpr_read_b32 v63, a63\n\t"
    "v_max3_f32 v252, |v32|, |v33|, v252\n\t"
    "v_max3_f32 v252, |v34|, |v35|, v252\n\t"
    "v_max3_f32 v252, |v36|, |v37|, v252\n\t"
    "v_max3_f32 v252, |v38|, |v39|, v252\n\t"
    "v_mul_f32 v32, v32, v232\n\t"
    "v_mul_f32 v33, v33, v232\n\t"
    "v_mul_f32 v34, v34, v232\n\t"
    "v_mul_f32 v35, v35, v232\n\t"
    "v_mul_f32 v36, v36, v232\n\t"
    "v_mul_f32 v37, v37, v232\n\t"
    "v_mul_f32 v38, v38, v232\n\t"
    "v_mul_f32 v39, v39, v232\n\t"
    "v_cvt_pk_bf16_f32 v68, v32, v33\n\t"
    "v_cvt_pk_bf16_f32 v69, v34, v35\n\t"
    "v_cvt_pk_bf16_f32 v70, v36, v37\n\t"
    "v_cvt_pk_bf16_f32 v71, v38, v39\n\t"
    "v_max3_f32 v253, |v40|, |v41|, v253\n\t"
    "v_max3_f32 v253, |v42|, |v43|, v253\n\t"
    "v_max3_f32 v253, |v44|, |v45|, v253\n\t"
    "v_max3_f32 v253, |v46|, |v47|, v253\n\t"
    "v_mul_f32 v40, v40, v233\n\t"
    "v_mul_f32 v41, v41, v233\n\t"
    "v_mul_f32 v42, v42, v233\n\t"
    "v_mul_f32 v43, v43, v233\n\t"
    "v_mul_f32 v44, v44, v233\n\t"
    "v_mul_f32 v45, v45, v233\n\t"
    "v_mul_f32 v46, v46, v233\n\t"
    "v_mul_f32 v47, v47, v233\n\t"
    "v_cvt_pk_bf16_f32 v84, v40, v41\n\t"
    "v_cvt_pk_bf16_f32 v85, v42, v43\n\t"
    "v_cvt_pk_bf16_f32 v86, v44, v45\n\t"
    "v_cvt_pk_bf16_f32 v87, v46, v47\n\t"
    "v_max3_f32 v254, |v48|, |v49|, v254\n\t"
    "v_max3_f32 v254, |v50|, |v51|, v254\n\t"
    "v_max3_f32 v254, |v52|, |v53|, v254\n\t"
    "v_max3_f32 v254, |v54|, |v55|, v254\n\t"
    "v_mul_f32 v48, v48, v234\n\t"
    "v_mul_f32 v49, v49, v234\n\t"
    "v_mul_f32 v50, v50, v234\n\t"
    "v_mul_f32 v51, v51, v234\n\t"
    "v_mul_f32 v52, v52, v234\n\t"
    "v_mul_f32 v53, v53, v234\n\t"
    "v_mul_f32 v54, v54, v234\n\t"
    "v_mul_f32 v55, v55, v234\n\t"
    "v_cvt_pk_bf16_f32 v100, v48, v49\n\t"
    "v_cvt_pk_bf16_f32 v101, v50, v51\n\t"
    "v_cvt_pk_bf16_f32 v102, v52, v53\n\t"
    "v_cvt_pk_bf16_f32 v103, v54, v55\n\t"
    "v_max3_f32 v255, |v56|, |v57|, v255\n\t"
    "v_max3_f32 v255, |v58|, |v59|, v255\n\t"
    "v_max3_f32 v255, |v60|, |v61|, v255\n\t"
    "v_max3_f32 v255, |v62|, |v63|, v255\n\t"
    "v_mul_f32 v56, v56, v235\n\t"
    "v_mul_f32 v57, v57, v235\n\t"
    "v_mul_f32 v58, v58, v235\n\t"
    "v_mul_f32 v59, v59, v235\n\t"
    "v_mul_f32 v60, v60, v235\n\t"
    "v_mul_f32 v61, v61, v235\n\t"
    "v_mul_f32 v62, v62, v235\n\t"
    "v_mul_f32 v63, v63, v235\n\t"
    "v_cvt_pk_bf16_f32 v116, v56, v57\n\t"
    "v_cvt_pk_bf16_f32 v117, v58, v59\n\t"
    "v_cvt_pk_bf16_f32 v118, v60, v61\n\t"
    "v_cvt_pk_bf16_f32 v119, v62, v63\n\t"
    "v_accvgpr_read_b32 v32, a64\n\t"
    "v_accvgpr_read_b32 v33, a65\n\t"
    "v_accvgpr_read_b32 v34, a66\n\t"
    "v_accvgpr_read_b32 v35, a67\n\t"
    "v_accvgpr_read_b32 v36, a80\n\t"
    "v_accvgpr_read_b32 v37, a81\n\t"
    "v_accvgpr_read_b32 v38, a82\n\t"
    "v_accvgpr_read_b32 v39, a83\n\t"
    "v_accvgpr_read_b32 v40, a68\n\t"
    "v_accvgpr_read_b32 v41, a69\n\t"
    "v_accvgpr_read_b32 v42, a70\n\t"
    "v_accvgpr_read_b32 v43, a71\n\t"
    "v_accvgpr_read_b32 v44, a84\n\t"
    "v_accvgpr_read_b32 v45, a85\n\t"
    "v_accvgpr_read_b32 v46, a86\n\t"
    "v_accvgpr_read_b32 v47, a87\n\t"
    "v_accvgpr_read_b32 v48, a72\n\t"
    "v_accvgpr_read_b32 v49, a73\n\t"
    "v_accvgpr_read_b32 v50, a74\n\t"
    "v_accvgpr_read_b32 v51, a75\n\t"
    "v_accvgpr_read_b32 v52, a88\n\t"
    "v_accvgpr_read_b32 v53, a89\n\t"
    "v_accvgpr_read_b32 v54, a90\n\t"
    "v_accvgpr_read_b32 v55, a91\n\t"
    "v_accvgpr_read_b32 v56, a76\n\t"
    "v_accvgpr_read_b32 v57, a77\n\t"
    "v_accvgpr_read_b32 v58, a78\n\t"
    "v_accvgpr_read_b32 v59, a79\n\t"
    "v_accvgpr_read_b32 v60, a92\n\t"
    "v_accvgpr_read_b32 v61, a93\n\t"
    "v_accvgpr_read_b32 v62, a94\n\t"
    "v_accvgpr_read_b32 v63, a95\n\t"
    "v_max3_f32 v252, |v32|, |v33|, v252\n\t"
    "v_max3_f32 v252, |v34|, |v35|, v252\n\t"
    "v_max3_f32 v252, |v36|, |v37|, v252\n\t"
    "v_max3_f32 v252, |v38|, |v39|, v252\n\t"
    "v_mul_f32 v32, v32, v232\n\t"
    "v_mul_f32 v33, v33, v232\n\t"
    "v_mul_f32 v34, v34, v232\n\t"
    "v_mul_f32 v35, v35, v232\n\t"
    "v_mul_f32 v36, v36, v232\n\t"
    "v_mul_f32 v37, v37, v232\n\t"
    "v_mul_f32 v38, v38, v232\n\t"
    "v_mul_f32 v39, v39, v232\n\t"
    "v_cvt_pk_bf16_f32 v72, v32, v33\n\t"
    "v_cvt_pk_bf16_f32 v73, v34, v35\n\t"
    "v_cvt_pk_bf16_f32 v74, v36, v37\n\t"
    "v_cvt_pk_bf16_f32 v75, v38, v39\n\t"
    "v_max3_f32 v253, |v40|, |v41|, v253\n\t"
    "v_max3_f32 v253, |v42|, |v43|, v253\n\t"
    "v_max3_f32 v253, |v44|, |v45|, v253\n\t"
    "v_max3_f32 v253, |v46|, |v47|, v253\n\t"
    "v_mul_f32 v40, v40, v233\n\t"
    "v_mul_f32 v41, v41, v233\n\t"
    "v_mul_f32 v42, v42, v233\n\t"
    "v_mul_f32 v43, v43, v233\n\t"
    "v_mul_f32 v44, v44, v233\n\t"
    "v_mul_f32 v45, v45, v233\n\t"
    "v_mul_f32 v46, v46, v233\n\t"
    "v_mul_f32 v47, v47, v233\n\t"
    "v_cvt_pk_bf16_f32 v88, v40, v41\n\t"
    "v_cvt_pk_bf16_f32 v89, v42, v43\n\t"
    "v_cvt_pk_bf16_f32 v90, v44, v45\n\t"
    "v_cvt_pk_bf16_f32 v91, v46, v47\n\t"
    "v_max3_f32 v254, |v48|, |v49|, v254\n\t"
    "v_max3_f32 v254, |v50|, |v51|, v254\n\t"
    "v_max3_f32 v254, |v52|, |v53|, v254\n\t"
    "v_max3_f32 v254, |v54|, |v55|, v254\n\t"
    "v_mul_f32 v48, v48, v234\n\t"
    "v_mul_f32 v49, v49, v234\n\t"
    "v_mul_f32 v50, v50, v234\n\t"
    "v_mul_f32 v51, v51, v234\n\t"
    "v_mul_f32 v52, v52, v234\n\t"
    "v_mul_f32 v53, v53, v234\n\t"
    "v_mul_f32 v54, v54, v234\n\t"
    "v_mul_f32 v55, v55, v234\n\t"
    "v_cvt_pk_bf16_f32 v104, v48, v49\n\t"
    "v_cvt_pk_bf16_f32 v105, v50, v51\n\t"
    "v_cvt_pk_bf16_f32 v106, v52, v53\n\t"
    "v_cvt_pk_bf16_f32 v107, v54, v55\n\t"
    "v_max3_f32 v255, |v56|, |v57|, v255\n\t"
    "v_max3_f32 v255, |v58|, |v59|, v255\n\t"
    "v_max3_f32 v255, |v60|, |v61|, v255\n\t"
    "v_max3_f32 v255, |v62|, |v63|, v255\n\t"
    "v_mul_f32 v56, v56, v235\n\t"
    "v_mul_f32 v57, v57, v235\n\t"
    "v_mul_f32 v58, v58, v235\n\t"
    "v_mul_f32 v59, v59, v235\n\t"
    "v_mul_f32 v60, v60, v235\n\t"
    "v_mul_f32 v61, v61, v235\n\t"
    "v_mul_f32 v62, v62, v235\n\t"
    "v_mul_f32 v63, v63, v235\n\t"
    "v_cvt_pk_bf16_f32 v120, v56, v57\n\t"
    "v_cvt_pk_bf16_f32 v121, v58, v59\n\t"
    "v_cvt_pk_bf16_f32 v122, v60, v61\n\t"
    "v_cvt_pk_bf16_f32 v123, v62, v63\n\t"
    "v_accvgpr_read_b32 v32, a96\n\t"
    "v_accvgpr_read_b32 v33, a97\n\t"
    "v_accvgpr_read_b32 v34, a98\n\t"
    "v_accvgpr_read_b32 v35, a99\n\t"
    "v_accvgpr_read_b32 v36, a112\n\t"
    "v_accvgpr_read_b32 v37, a113\n\t"
    "v_accvgpr_read_b32 v38, a114\n\t"
    "v_accvgpr_read_b32 v39, a115\n\t"
    "v_accvgpr_read_b32 v40, a100\n\t"
    "v_accvgpr_read_b32 v41, a101\n\t"
    "v_accvgpr_read_b32 v42, a102\n\t"
    "v_accvgpr_read_b32 v43, a103\n\t"
    "v_accvgpr_read_b32 v44, a116\n\t"
    "v_accvgpr_read_b32 v45, a117\n\t"
    "v_accvgpr_read_b32 v46, a118\n\t"
    "v_accvgpr_read_b32 v47, a119\n\t"
    "v_accvgpr_read_b32 v48, a104\n\t"
    "v_accvgpr_read_b32 v49, a105\n\t"
    "v_accvgpr_read_b32 v50, a106\n\t"
    "v_accvgpr_read_b32 v51, a107\n\t"
    "v_accvgpr_read_b32 v52, a120\n\t"
    "v_accvgpr_read_b32 v53, a121\n\t"
    "v_accvgpr_read_b32 v54, a122\n\t"
    "v_accvgpr_read_b32 v55, a123\n\t"
    "v_accvgpr_read_b32 v56, a108\n\t"
    "v_accvgpr_read_b32 v57, a109\n\t"
    "v_accvgpr_read_b32 v58, a110\n\t"
    "v_accvgpr_read_b32 v59, a111\n\t"
    "v_accvgpr_read_b32 v60, a124\n\t"
    "v_accvgpr_read_b32 v61, a125\n\t"
    "v_accvgpr_read_b32 v62, a126\n\t"
    "v_accvgpr_read_b32 v63, a127\n\t"
    "v_max3_f32 v252, |v32|, |v33|, v252\n\t"
    "v_max3_f32 v252, |v34|, |v35|, v252\n\t"
    "v_max3_f32 v252, |v36|, |v37|, v252\n\t"
    "v_max3_f32 v252, |v38|, |v39|, v252\n\t"
    "v_mul_f32 v32, v32, v232\n\t"
    "v_mul_f32 v33, v33, v232\n\t"
    "v_mul_f32 v34, v34, v232\n\t"
    "v_mul_f32 v35, v35, v232\n\t"
    "v_mul_f32 v36, v36, v232\n\t"
    "v_mul_f32 v37, v37, v232\n\t"
    "v_mul_f32 v38, v38, v232\n\t"
    "v_mul_f32 v39, v39, v232\n\t"
    "v_cvt_pk_bf16_f32 v76, v32, v33\n\t"
    "v_cvt_pk_bf16_f32 v77, v34, v35\n\t"
    "v_cvt_pk_bf16_f32 v78, v36, v37\n\t"
    "v_cvt_pk_bf16_f32 v79, v38, v39\n\t"
    "v_max3_f32 v253, |v40|, |v41|, v253\n\t"
    "v_max3_f32 v253, |v42|, |v43|, v253\n\t"
    "v_max3_f32 v253, |v44|, |v45|, v253\n\t"
    "v_max3_f32 v253, |v46|, |v47|, v253\n\t"
    "v_mul_f32 v40, v40, v233\n\t"
    "v_mul_f32 v41, v41, v233\n\t"
    "v_mul_f32 v42, v42, v233\n\t"
    "v_mul_f32 v43, v43, v233\n\t"
    "v_mul_f32 v44, v44, v233\n\t"
    "v_mul_f32 v45, v45, v233\n\t"
    "v_mul_f32 v46, v46, v233\n\t"
    "v_mul_f32 v47, v47, v233\n\t"
    "v_cvt_pk_bf16_f32 v92, v40, v41\n\t"
    "v_cvt_pk_bf16_f32 v93, v42, v43\n\t"
    "v_cvt_pk_bf16_f32 v94, v44, v45\n\t"
    "v_cvt_pk_bf16_f32 v95, v46, v47\n\t"
    "v_max3_f32 v254, |v48|, |v49|, v254\n\t"
    "v_max3_f32 v254, |v50|, |v51|, v254\n\t"
    "v_max3_f32 v254, |v52|, |v53|, v254\n\t"
    "v_max3_f32 v254, |v54|, |v55|, v254\n\t"
    "v_mul_f32 v48, v48, v234\n\t"
    "v_mul_f32 v49, v49, v234\n\t"
    "v_mul_f32 v50, v50, v234\n\t"
    "v_mul_f32 v51, v51, v234\n\t"
    "v_mul_f32 v52, v52, v234\n\t"
    "v_mul_f32 v53, v53, v234\n\t"
    "v_mul_f32 v54, v54, v234\n\t"
    "v_mul_f32 v55, v55, v234\n\t"
    "v_cvt_pk_bf16_f32 v108, v48, v49\n\t"
    "v_cvt_pk_bf16_f32 v109, v50, v51\n\t"
    "v_cvt_pk_bf16_f32 v110, v52, v53\n\t"
    "v_cvt_pk_bf16_f32 v111, v54, v55\n\t"
    "v_max3_f32 v255, |v56|, |v57|, v255\n\t"
    "v_max3_f32 v255, |v58|, |v59|, v255\n\t"
    "v_max3_f32 v255, |v60|, |v61|, v255\n\t"
    "v_max3_f32 v255, |v62|, |v63|, v255\n\t"
    "v_mul_f32 v56, v56, v235\n\t"
    "v_mul_f32 v57, v57, v235\n\t"
    "v_mul_f32 v58, v58, v235\n\t"
    "v_mul_f32 v59, v59, v235\n\t"
    "v_mul_f32 v60, v60, v235\n\t"
    "v_mul_f32 v61, v61, v235\n\t"
    "v_mul_f32 v62, v62, v235\n\t"
    "v_mul_f32 v63, v63, v235\n\t"
    "v_cvt_pk_bf16_f32 v124, v56, v57\n\t"
    "v_cvt_pk_bf16_f32 v125, v58, v59\n\t"
    "v_cvt_pk_bf16_f32 v126, v60, v61\n\t"
    "v_cvt_pk_bf16_f32 v127, v62, v63\n\t"
    "v_max3_f32 v32, v208, v212, v216\n\t"
    "v_max_f32 v32, v32, v220\n\t"
    "v_max3_f32 v33, v252, v253, v254\n\t"
    "v_max_f32 v33, v33, v255\n\t"
    "v_min3_f32 v34, v252, v253, v254\n\t"
    "v_min_f32 v34, v34, v255\n\t"
    "v_cmp_gt_f32 vcc, 0x71800000, v32\n\t"
    "s_nop 1\n\t"
    "s_mov_b64 s[64:65], vcc\n\t"
    "v_cmp_ge_f32 vcc, 0x7f7fffff, v33\n\t"
    "s_nop 1\n\t"
    "s_and_b64 s[64:65], s[64:65], vcc\n\t"
    "v_cmp_le_f32 vcc, 0x800000, v34\n\t"
    "s_nop 1\n\t"
    "s_and_b64 s[64:65], s[64:65], vcc\n\t"
    "s_cmp_eq_u64 s[64:65], -1\n\t"
    "s_cselect_b32 s62, 0, 1\n\t"
    "s_cmp_eq_u32 s61, 1\n\t"
    "s_cselect_b32 s62, 0, s62\n\t"
    "v_and_b32 v32, 0x30, %[poff]\n\t"
    "v_lshlrev_b32 v32, 9, v32\n\t"
    "s_lshl_b32 s45, %[wid], 2\n\t"
    "s_add_u32 s45, s45, %[ldsb]\n\t"
    "s_add_u32 s45, s45, 98304\n\t"
    "v_add_u32 v32, s45, v32\n\t"
    "v_mov_b32 v33, s62\n\t"
    "s_add_u32 s45, s49, 98304\n\t"
    "v_mov_b32 v34, s45\n\t"
    "s_mov_b64 exec, 15\n\t"
    "ds_write_b32 v32, v33\n\t"
    "s_mov_b64 exec, -1\n\t"
    "s_waitcnt vmcnt(24)\n\t"
    "s_waitcnt lgkmcnt(0)\n\t"
    "s_barrier\n\t"
    "ds_read_b128 v[36:39], v34\n\t"
    "s_waitcnt lgkmcnt(0)\n\t"
    "v_or3_b32 v36, v36, v37, v38\n\t"
    "v_or_b32 v36, v36, v39\n\t"
    "s_nop 1\n\t"
    "v_readfirstlane_b32 s45, v36\n\t"
    "s_nop 3\n\t"
    "s_cmp_lg_u32 s45, 0\n\t"
    "s_cbranch_scc1 FB%=\n\t"
    "ds_read_b64_tr_b16 a[128:129], %[ob0] offset:0\n\t"
    "ds_read_b64_tr_b16 a[130:131], %[ob0] offset:2048\n\t"
    "ds_read_b64_tr_b16 a[132:133], %[ob0] offset:4096\n\t"
    "ds_read_b64_tr_b16 a[134:135], %[ob0] offset:6144\n\t"
    "ds_read_b64_tr_b16 a[136:137], %[ob0] offset:8192\n\t"
    "ds_read_b64_tr_b16 a[138:139], %[ob0] offset:10240\n\t"
    "ds_read_b64_tr_b16 a[140:141], %[ob0] offset:12288\n\t"
    "ds_read_b64_tr_b16 a[142:143], %[ob0] offset:14336\n\t"
    "ds_read_b64_tr_b16 a[144:145], %[ob0] offset:16384\n\t"
    "ds_read_b64_tr_b16 a[146:147], %[ob0] offset:18432\n\t"
    "ds_read_b64_tr_b16 a[148:149], %[ob0] offset:20480\n\t"
    "ds_read_b64_tr_b16 a[150:151], %[ob0] offset:22528\n\t"
    "ds_read_b64_tr_b16 a[152:153], %[ob0] offset:24576\n\t"
    "ds_read_b64_tr_b16 a[154:155], %[ob0] offset:26624\n\t"
    "ds_read_b64_tr_b16 a[156:157], %[ob0] offset:28672\n\t"
    "ds_read_b64_tr_b16 a[158:159], %[ob0] offset:30720\n\t"
    "s_waitcnt lgkmcnt(8)\n\t"
    "v_mfma_f32_16x16x32_bf16 v[128:131], a[128:131], v[64:67], 0\n\t"
    "ds_read_b64_tr_b16 a[160:161], %[ob0] offset:256\n\t"
    "s_add_u32 m0, s49, 98304\n\t"
    "s_mov_b32 s48, 512\n\t"
    "v_mfma_f32_16x16x32_bf16 v[128:131], a[132:135], v[68:71], v[128:131]\n\t"
    "ds_read_b64_tr_b16 a[162:163], %[ob0] offset:2304\n\t"
    "buffer_load_dwordx4 %[osrc0], %[srdWo], s48 offen offset:0 lds\n\t"
    "v_mfma_f32_16x16x32_bf16 v[128:131], a[136:139], v[72:75], v[128:131]\n\t"
    "ds_read_b64_tr_b16 a[164:165], %[ob0] offset:4352\n\t"
    "v_mfma_f32_16x16x32_bf16 v[128:131], a[140:143], v[76:79], v[128:131]\n\t"
    "ds_read_b64_tr_b16 a[166:167], %[ob0] offset:6400\n\t"
    "v_mfma_f32_16x16x32_bf16 v[132:135], a[128:131], v[80:83], 0\n\t"
    "ds_read_b64_tr_b16 a[168:169], %[ob0] offset:8448\n\t"
    "v_mfma_f32_16x16x32_bf16 v[132:135], a[132:135], v[84:87], v[132:135]\n\t"
    "ds_read_b64_tr_b16 a[170:171], %[ob0] offset:10496\n\t"
    "v_mfma_f32_16x16x32_bf16 v[132:135], a[136:139], v[88:91], v[132:135]\n\t"
    "ds_read_b64_tr_b16 a[172:173], %[ob0] offset:12544\n\t"
    "v_mfma_f32_16x16x32_bf16 v[132:135], a[140:143], v[92:95], v[132:135]\n\t"
    "ds_read_b64_tr_b16 a[174:175], %[ob0] offset:14592\n\t"
    "s_waitcnt lgkmcnt(8)\n\t"
    "v_mfma_f32_16x16x32_bf16 v[128:131], a[144:147], v[96:99], v[128:131]\n\t"
    "ds_read_b64_tr_b16 a[176:177], %[ob0] offset:16640\n\t"
    "v_mfma_f32_16x16x32_bf16 v[128:131], a[148:151], v[100:103], v[128:131]\n\t"
    "ds_read_b64_tr_b16 a[178:179], %[ob0] offset:18688\n\t"
    "buffer_load_dwordx4 v236, %[srdWo], s48 offen offset:1024 lds\n\t"
    "v_mfma_f32_16x16x32_bf16 v[128:131], a[152:155], v[104:107], v[128:131]\n\t"
    "ds_read_b64_tr_b16 a[180:181], %[ob0] offset:20736\n\t"
    "v_mfma_f32_16x16x32_bf16 v[128:131], a[156:159], v[108:111], v[128:131]\n\t"
    "ds_read_b64_tr_b16 a[182:183], %[ob0] offset:22784\n\t"
    "v_mfma_f32_16x16x32_bf16 v[132:135], a[144:147], v[112:115], v[132:135]\n\t"
    "ds_read_b64_tr_b16 a[184:185], %[ob0] offset:24832\n\t"
    "v_mfma_f32_16x16x32_bf16 v[132:135], a[148:151], v[116:119], v[132:135]\n\t"
    "ds_read_b64_tr_b16 a[186:187], %[ob0] offset:26880\n\t"
    "v_mfma_f32_16x16x32_bf16 v[132:135], a[152:155], v[120:123], v[132:135]\n\t"
    "ds_read_b64_tr_b16 a[188:189], %[ob0] offset:28928\n\t"
    "v_mfma_f32_16x16x32_bf16 v[132:135], a[156:159], v[124:127], v[132:135]\n\t"
    "ds_read_b64_tr_b16 a[190:191], %[ob0] offset:30976\n\t"
    "s_waitcnt lgkmcnt(8)\n\t"
    "v_mfma_f32_16x16x32_bf16 v[136:139], a[160:163], v[64:67], 0\n\t"
    "ds_read_b64_tr_b16 a[128:129], %[ob0] offset:512\n\t"
    "v_mfma_f32_16x16x32_bf16 v[136:139], a[164:167], v[68:71], v[136:139]\n\t"
    "ds_read_b64_tr_b16 a[130:131], %[ob0] offset:2560\n\t"
    "buffer_load_dwordx4 v237, %[srdWo], s48 offen offset:2048 lds\n\t"
    "v_mfma_f32_16x16x32_bf16 v[136:139], a[168:171], v[72:75], v[136:139]\n\t"
    "ds_read_b64_tr_b16 a[132:133], %[ob0] offset:4608\n\t"
    "v_mfma_f32_16x16x32_bf16 v[136:139], a[172:175], v[76:79], v[136:139]\n\t"
    "ds_read_b64_tr_b16 a[134:135], %[ob0] offset:6656\n\t"
    "v_mfma_f32_16x16x32_bf16 v[140:143], a[160:163], v[80:83], 0\n\t"
    "ds_read_b64_tr_b16 a[136:137], %[ob0] offset:8704\n\t"
    "v_mfma_f32_16x16x32_bf16 v[140:143], a[164:167], v[84:87], v[140:143]\n\t"
    "ds_read_b64_tr_b16 a[138:139], %[ob0] offset:10752\n\t"
    "v_mfma_f32_16x16x32_bf16 v[140:143], a[168:171], v[88:91], v[140:143]\n\t"
    "ds_read_b64_tr_b16 a[140:141], %[ob0] offset:12800\n\t"
    "v_mfma_f32_16x16x32_bf16 v[140:143], a[172:175], v[92:95], v[140:143]\n\t"
    "ds_read_b64_tr_b16 a[142:143], %[ob0] offset:14848\n\t"
    "s_waitcnt lgkmcnt(8)\n\t"
    "v_mfma_f32_16x16x32_bf16 v[136:139], a[176:179], v[96:99], v[136:139]\n\t"
    "ds_read_b64_tr_b16 a[144:145], %[ob0] offset:16896\n\t"
    "v_mfma_f32_16x16x32_bf16 v[136:139], a[180:183], v[100:103], v[136:139]\n\t"
    "ds_read_b64_tr_b16 a[146:147], %[ob0] offset:18944\n\t"
    "buffer_load_dwordx4 v238, %[srdWo], s48 offen offset:3072 lds\n\t"
    "v_mfma_f32_16x16x32_bf16 v[136:139], a[184:187], v[104:107], v[136:139]\n\t"
    "ds_read_b64_tr_b16 a[148:149], %[ob0] offset:20992\n\t"
    "v_mfma_f32_16x16x32_bf16 v[136:139], a[188:191], v[108:111], v[136:139]\n\t"
    "ds_read_b64_tr_b16 a[150:151], %[ob0] offset:23040\n\t"
    "v_mfma_f32_16x16x32_bf16 v[140:143], a[176:179], v[112:115], v[140:143]\n\t"
    "ds_read_b64_tr_b16 a[152:153], %[ob0] offset:25088\n\t"
    "v_mfma_f32_16x16x32_bf16 v[140:143], a[180:183], v[116:119], v[140:143]\n\t"
    "ds_read_b64_tr_b16 a[154:155], %[ob0] offset:27136\n\t"
    "v_mfma_f32_16x16x32_bf16 v[140:143], a[184:187], v[120:123], v[140:143]\n\t"
    "ds_read_b64_tr_b16 a[156:157], %[ob0] offset:29184\n\t"
    "v_mfma_f32_16x16x32_bf16 v[140:143], a[188:191], v[124:127], v[140:143]\n\t"
    "ds_read_b64_tr_b16 a[158:159], %[ob0] offset:31232\n\t"
    "s_waitcnt lgkmcnt(8)\n\t"
    "v_mfma_f32_16x16x32_bf16 v[144:147], a[128:131], v[64:67], 0\n\t"
    "ds_read_b64_tr_b16 a[160:161], %[ob0] offset:768\n\t"
    "s_add_u32 m0, s49, 102400\n\t"
    "s_mov_b32 s48, 37376\n\t"
    "v_mfma_f32_16x16x32_bf16 v[144:147], a[132:135], v[68:71], v[144:147]\n\t"
    "ds_read_b64_tr_b16 a[162:163], %[ob0] offset:2816\n\t"
    "buffer_load_dwordx4 %[osrc0], %[srdWo], s48 offen offset:0 lds\n\t"
    "v_mfma_f32_16x16x32_bf16 v[144:147], a[136:139], v[72:75], v[144:147]\n\t"
    "ds_read_b64_tr_b16 a[164:165], %[ob0] offset:4864\n\t"
    "v_mfma_f32_16x16x32_bf16 v[144:147], a[140:143], v[76:79], v[144:147]\n\t"
    "ds_read_b64_tr_b16 a[166:167], %[ob0] offset:6912\n\t"
    "v_mfma_f32_16x16x32_bf16 v[148:151], a[128:131], v[80:83], 0\n\t"
    "ds_read_b64_tr_b16 a[168:169], %[ob0] offset:8960\n\t"
    "v_mfma_f32_16x16x32_bf16 v[148:151], a[132:135], v[84:87], v[148:151]\n\t"
    "ds_read_b64_tr_b16 a[170:171], %[ob0] offset:11008\n\t"
    "v_mfma_f32_16x16x32_bf16 v[148:151], a[136:139], v[88:91], v[148:151]\n\t"
    "ds_read_b64_tr_b16 a[172:173], %[ob0] offset:13056\n\t"
    "v_mfma_f32_16x16x32_bf16 v[148:151], a[140:143], v[92:95], v[148:151]\n\t"
    "ds_read_b64_tr_b16 a[174:175], %[ob0] offset:15104\n\t"
    "s_waitcnt lgkmcnt(8)\n\t"
    "v_mfma_f32_16x16x32_bf16 v[144:147], a[144:147], v[96:99], v[144:147]\n\t"
    "ds_read_b64_tr_b16 a[176:177], %[ob0] offset:17152\n\t"
    "v_mfma_f32_16x16x32_bf16 v[144:147], a[148:151], v[100:103], v[144:147]\n\t"
    "ds_read_b64_tr_b16 a[178:179], %[ob0] offset:19200\n\t"
    "buffer_load_dwordx4 v236, %[srdWo], s48 offen offset:1024 lds\n\t"
    "v_mfma_f32_16x16x32_bf16 v[144:147], a[152:155], v[104:107], v[144:147]\n\t"
    "ds_read_b64_tr_b16 a[180:181], %[ob0] offset:21248\n\t"
    "v_mfma_f32_16x16x32_bf16 v[144:147], a[156:159], v[108:111], v[144:147]\n\t"
    "ds_read_b64_tr_b16 a[182:183], %[ob0] offset:23296\n\t"
    "v_mfma_f32_16x16x32_bf16 v[148:151], a[144:147], v[112:115], v[148:151]\n\t"
    "ds_read_b64_tr_b16 a[184:185], %[ob0] offset:25344\n\t"
    "v_mfma_f32_16x16x32_bf16 v[148:151], a[148:151], v[116:119], v[148:151]\n\t"
    "ds_read_b64_tr_b16 a[186:187], %[ob0] offset:27392\n\t"
    "v_mfma_f32_16x16x32_bf16 v[148:151], a[152:155], v[120:123], v[148:151]\n\t"
    "ds_read_b64_tr_b16 a[188:189], %[ob0] offset:29440\n\t"
    "v_mfma_f32_16x16x32_bf16 v[148:151], a[156:159], v[124:127], v[148:151]\n\t"
    "ds_read_b64_tr_b16 a[190:191], %[ob0] offset:31488\n\t"
    "s_waitcnt vmcnt(22)\n\t"
    "s_waitcnt lgkmcnt(0)\n\t"
    "s_barrier\n\t"
    "s_waitcnt lgkmcnt(8)\n\t"
    "v_mfma_f32_16x16x32_bf16 v[152:155], a[160:163], v[64:67], 0\n\t"
    "ds_read_b64_tr_b16 a[128:129], %[ob0] offset:32768\n\t"
    "v_mfma_f32_16x16x32_bf16 v[152:155], a[164:167], v[68:71], v[152:155]\n\t"
    "ds_read_b64_tr_b16 a[130:131], %[ob0] offset:34816\n\t"
    "buffer_load_dwordx4 v237, %[srdWo], s48 offen offset:2048 lds\n\t"
    "v_mfma_f32_16x16x32_bf16 v[152:155], a[168:171], v[72:75], v[152:155]\n\t"
    "ds_read_b64_tr_b16 a[132:133], %[ob0] offset:36864\n\t"
    "v_mfma_f32_16x16x32_bf16 v[152:155], a[172:175], v[76:79], v[152:155]\n\t"
    "ds_read_b64_tr_b16 a[134:135], %[ob0] offset:38912\n\t"
    "v_mfma_f32_16x16x32_bf16 v[156:159], a[160:163], v[80:83], 0\n\t"
    "ds_read_b64_tr_b16 a[136:137], %[ob0] offset:40960\n\t"
    "v_mfma_f32_16x16x32_bf16 v[156:159], a[164:167], v[84:87], v[156:159]\n\t"
    "ds_read_b64_tr_b16 a[138:139], %[ob0] offset:43008\n\t"
    "v_mfma_f32_16x16x32_bf16 v[156:159], a[168:171], v[88:91], v[156:159]\n\t"
    "ds_read_b64_tr_b16 a[140:141], %[ob0] offset:45056\n\t"
    "v_mfma_f32_16x16x32_bf16 v[156:159], a[172:175], v[92:95], v[156:159]\n\t"
    "ds_read_b64_tr_b16 a[142:143], %[ob0] offset:47104\n\t"
    "s_waitcnt lgkmcnt(8)\n\t"
    "v_mfma_f32_16x16x32_bf16 v[152:155], a[176:179], v[96:99], v[152:155]\n\t"
    "ds_read_b64_tr_b16 a[144:145], %[ob0] offset:49152\n\t"
    "v_mfma_f32_16x16x32_bf16 v[152:155], a[180:183], v[100:103], v[152:155]\n\t"
    "ds_read_b64_tr_b16 a[146:147], %[ob0] offset:51200\n\t"
    "buffer_load_dwordx4 v238, %[srdWo], s48 offen offset:3072 lds\n\t"
    "v_mfma_f32_16x16x32_bf16 v[152:155], a[184:187], v[104:107], v[152:155]\n\t"
    "ds_read_b64_tr_b16 a[148:149], %[ob0] offset:53248\n\t"
    "v_mfma_f32_16x16x32_bf16 v[152:155], a[188:191], v[108:111], v[152:155]\n\t"
    "ds_read_b64_tr_b16 a[150:151], %[ob0] offset:55296\n\t"
    "v_mfma_f32_16x16x32_bf16 v[156:159], a[176:179], v[112:115], v[156:159]\n\t"
    "ds_read_b64_tr_b16 a[152:153], %[ob0] offset:57344\n\t"
    "v_mfma_f32_16x16x32_bf16 v[156:159], a[180:183], v[116:119], v[156:159]\n\t"
    "ds_read_b64_tr_b16 a[154:155], %[ob0] offset:59392\n\t"
    "v_mfma_f32_16x16x32_bf16 v[156:159], a[184:187], v[120:123], v[156:159]\n\t"
    "ds_read_b64_tr_b16 a[156:157], %[ob0] offset:61440\n\t"
    "v_mfma_f32_16x16x32_bf16 v[156:159], a[188:191], v[124:127], v[156:159]\n\t"
    "ds_read_b64_tr_b16 a[158:159], %[ob0] offset:63488\n\t"
    "s_waitcnt lgkmcnt(8)\n\t"
    "v_mfma_f32_16x16x32_bf16 v[160:163], a[128:131], v[64:67], 0\n\t"
    "ds_read_b64_tr_b16 a[160:161], %[ob0] offset:33024\n\t"
    "s_add_u32 m0, s49, 0\n\t"
    "s_mov_b32 s48, 640\n\t"
    "v_mfma_f32_16x16x32_bf16 v[160:163], a[132:135], v[68:71], v[160:163]\n\t"
    "ds_read_b64_tr_b16 a[162:163], %[ob0] offset:35072\n\t"
    "buffer_load_dwordx4 %[osrc0], %[srdWo], s48 offen offset:0 lds\n\t"
    "v_mfma_f32_16x16x32_bf16 v[160:163], a[136:139], v[72:75], v[160:163]\n\t"
    "ds_read_b64_tr_b16 a[164:165], %[ob0] offset:37120\n\t"
    "v_cvt_pk_bf16_f32 v32, v128, v129\n\t"
    "v_mfma_f32_16x16x32_bf16 v[160:163], a[140:143], v[76:79], v[160:163]\n\t"
    "ds_read_b64_tr_b16 a[166:167], %[ob0] offset:39168\n\t"
    "v_cvt_pk_bf16_f32 v33, v130, v131\n\t"
    "v_mfma_f32_16x16x32_bf16 v[164:167], a[128:131], v[80:83], 0\n\t"
    "ds_read_b64_tr_b16 a[168:169], %[ob0] offset:41216\n\t"
    "v_cvt_pk_bf16_f32 v34, v136, v137\n\t"
    "v_mfma_f32_16x16x32_bf16 v[164:167], a[132:135], v[84:87], v[164:167]\n\t"
    "ds_read_b64_tr_b16 a[170:171], %[ob0] offset:43264\n\t"
    "v_cvt_pk_bf16_f32 v35, v138, v139\n\t"
    "s_mov_b32 s47, 0\n\t"
    "v_mfma_f32_16x16x32_bf16 v[164:167], a[136:139], v[88:91], v[164:167]\n\t"
    "ds_read_b64_tr_b16 a[172:173], %[ob0] offset:45312\n\t"
    "buffer_store_dwordx4 v[32:35], %[poff], %[srdP], s47 offen sc1\n\t"
    "v_mfma_f32_16x16x32_bf16 v[164:167], a[140:143], v[92:95], v[164:167]\n\t"
    "ds_read_b64_tr_b16 a[174:175], %[ob0] offset:47360\n\t"
    "s_waitcnt lgkmcnt(8)\n\t"
    "v_mfma_f32_16x16x32_bf16 v[160:163], a[144:147], v[96:99], v[160:163]\n\t"
    "ds_read_b64_tr_b16 a[176:177], %[ob0] offset:49408\n\t"
    "v_mfma_f32_16x16x32_bf16 v[160:163], a[148:151], v[100:103], v[160:163]\n\t"
    "ds_read_b64_tr_b16 a[178:179], %[ob0] offset:51456\n\t"
    "buffer_load_dwordx4 v236, %[srdWo], s48 offen offset:1024 lds\n\t"
    "v_mfma_f32_16x16x32_bf16 v[160:163], a[152:155], v[104:107], v[160:163]\n\t"
    "ds_read_b64_tr_b16 a[180:181], %[ob0] offset:53504\n\t"
    "v_cvt_pk_bf16_f32 v36, v132, v133\n\t"
    "v_mfma_f32_16x16x32_bf16 v[160:163], a[156:159], v[108:111], v[160:163]\n\t"
    "ds_read_b64_tr_b16 a[182:183], %[ob0] offset:55552\n\t"
    "v_cvt_pk_bf16_f32 v37, v134, v135\n\t"
    "v_mfma_f32_16x16x32_bf16 v[164:167], a[144:147], v[112:115], v[164:167]\n\t"
    "ds_read_b64_tr_b16 a[184:185], %[ob0] offset:57600\n\t"
    "v_cvt_pk_bf16_f32 v38, v140, v141\n\t"
    "v_mfma_f32_16x16x32_bf16 v[164:167], a[148:151], v[116:119], v[164:167]\n\t"
    "ds_read_b64_tr_b16 a[186:187], %[ob0] offset:59648\n\t"
    "v_cvt_pk_bf16_f32 v39, v142, v143\n\t"
    "s_mov_b32 s47, 18432\n\t"
    "v_mfma_f32_16x16x32_bf16 v[164:167], a[152:155], v[120:123], v[164:167]\n\t"
    "ds_read_b64_tr_b16 a[188:189], %[ob0] offset:61696\n\t"
    "buffer_store_dwordx4 v[36:39], %[poff], %[srdP], s47 offen sc1\n\t"
    "v_mfma_f32_16x16x32_bf16 v[164:167], a[156:159], v[124:127], v[164:167]\n\t"
    "ds_read_b64_tr_b16 a[190:191], %[ob0] offset:63744\n\t"
    "s_waitcnt lgkmcnt(8)\n\t"
    "v_mfma_f32_16x16x32_bf16 v[168:171], a[160:163], v[64:67], 0\n\t"
    "ds_read_b64_tr_b16 a[128:129], %[ob0] offset:33280\n\t"
    "v_mfma_f32_16x16x32_bf16 v[168:171], a[164:167], v[68:71], v[168:171]\n\t"
    "ds_read_b64_tr_b16 a[130:131], %[ob0] offset:35328\n\t"
    "buffer_load_dwordx4 v237, %[srdWo], s48 offen offset:2048 lds\n\t"
    "v_mfma_f32_16x16x32_bf16 v[168:171], a[168:171], v[72:75], v[168:171]\n\t"
    "ds_read_b64_tr_b16 a[132:133], %[ob0] offset:37376\n\t"
    "v_cvt_pk_bf16_f32 v40, v144, v145\n\t"
    "v_mfma_f32_16x16x32_bf16 v[168:171], a[172:175], v[76:79], v[168:171]\n\t"
    "ds_read_b64_tr_b16 a[134:135], %[ob0] offset:39424\n\t"
    "v_cvt_pk_bf16_f32 v41, v146, v147\n\t"
    "v_mfma_f32_16x16x32_bf16 v[172:175], a[160:163], v[80:83], 0\n\t"
    "ds_read_b64_tr_b16 a[136:137], %[ob0] offset:41472\n\t"
    "v_cvt_pk_bf16_f32 v42, v152, v153\n\t"
    "v_mfma_f32_16x16x32_bf16 v[172:175], a[164:167], v[84:87], v[172:175]\n\t"
    "ds_read_b64_tr_b16 a[138:139], %[ob0] offset:43520\n\t"
    "v_cvt_pk_bf16_f32 v43, v154, v155\n\t"
    "s_mov_b32 s47, 1024\n\t"
    "v_mfma_f32_16x16x32_bf16 v[172:175], a[168:171], v[88:91], v[172:175]\n\t"
    "ds_read_b64_tr_b16 a[140:141], %[ob0] offset:45568\n\t"
    "buffer_store_dwordx4 v[40:43], %[poff], %[srdP], s47 offen sc1\n\t"
    "v_mfma_f32_16x16x32_bf16 v[172:175], a[172:175], v[92:95], v[172:175]\n\t"
    "ds_read_b64_tr_b16 a[142:143], %[ob0] offset:47616\n\t"
    "s_waitcnt lgkmcnt(8)\n\t"
    "v_mfma_f32_16x16x32_bf16 v[168:171], a[176:179], v[96:99], v[168:171]\n\t"
    "ds_read_b64_tr_b16 a[144:145], %[ob0] offset:49664\n\t"
    "v_mfma_f32_16x16x32_bf16 v[168:171], a[180:183], v[100:103], v[168:171]\n\t"
    "ds_read_b64_tr_b16 a[146:147], %[ob0] offset:51712\n\t"
    "buffer_load_dwordx4 v238, %[srdWo], s48 offen offset:3072 lds\n\t"
    "v_mfma_f32_16x16x32_bf16 v[168:171], a[184:187], v[104:107], v[168:171]\n\t"
    "ds_read_b64_tr_b16 a[148:149], %[ob0] offset:53760\n\t"
    "v_cvt_pk_bf16_f32 v44, v148, v149\n\t"
    "v_mfma_f32_16x16x32_bf16 v[168:171], a[188:191], v[108:111], v[168:171]\n\t"
    "ds_read_b64_tr_b16 a[150:151], %[ob0] offset:55808\n\t"
    "v_cvt_pk_bf16_f32 v45, v150, v151\n\t"
    "v_mfma_f32_16x16x32_bf16 v[172:175], a[176:179], v[112:115], v[172:175]\n\t"
    "ds_read_b64_tr_b16 a[152:153], %[ob0] offset:57856\n\t"
    "v_cvt_pk_bf16_f32 v46, v156, v157\n\t"
    "v_mfma_f32_16x16x32_bf16 v[172:175], a[180:183], v[116:119], v[172:175]\n\t"
    "ds_read_b64_tr_b16 a[154:155], %[ob0] offset:59904\n\t"
    "v_cvt_pk_bf16_f32 v47, v158, v159\n\t"
    "s_mov_b32 s47, 19456\n\t"
    "v_mfma_f32_16x16x32_bf16 v[172:175], a[184:187], v[120:123], v[172:175]\n\t"
    "ds_read_b64_tr_b16 a[156:157], %[ob0] offset:61952\n\t"
    "buffer_store_dwordx4 v[44:47], %[poff], %[srdP], s47 offen sc1\n\t"
    "v_mfma_f32_16x16x32_bf16 v[172:175], a[188:191], v[124:127], v[172:175]\n\t"
    "ds_read_b64_tr_b16 a[158:159], %[ob0] offset:64000\n\t"
    "s_waitcnt lgkmcnt(8)\n\t"
    "v_mfma_f32_16x16x32_bf16 v[176:179], a[128:131], v[64:67], 0\n\t"
    "ds_read_b64_tr_b16 a[160:161], %[ob0] offset:33536\n\t"
    "s_add_u32 m0, s49, 4096\n\t"
    "s_mov_b32 s48, 37504\n\t"
    "v_mfma_f32_16x16x32_bf16 v[176:179], a[132:135], v[68:71], v[176:179]\n\t"
    "ds_read_b64_tr_b16 a[162:163], %[ob0] offset:35584\n\t"
    "buffer_load_dwordx4 %[osrc0], %[srdWo], s48 offen offset:0 lds\n\t"
    "v_mfma_f32_16x16x32_bf16 v[176:179], a[136:139], v[72:75], v[176:179]\n\t"
    "ds_read_b64_tr_b16 a[164:165], %[ob0] offset:37632\n\t"
    "v_mfma_f32_16x16x32_bf16 v[176:179], a[140:143], v[76:79], v[176:179]\n\t"
    "ds_read_b64_tr_b16 a[166:167], %[ob0] offset:39680\n\t"
    "v_mfma_f32_16x16x32_bf16 v[180:183], a[128:131], v[80:83], 0\n\t"
    "ds_read_b64_tr_b16 a[168:169], %[ob0] offset:41728\n\t"
    "v_mfma_f32_16x16x32_bf16 v[180:183], a[132:135], v[84:87], v[180:183]\n\t"
    "ds_read_b64_tr_b16 a[170:171], %[ob0] offset:43776\n\t"
    "v_mfma_f32_16x16x32_bf16 v[180:183], a[136:139], v[88:91], v[180:183]\n\t"
    "ds_read_b64_tr_b16 a[172:173], %[ob0] offset:45824\n\t"
    "v_mfma_f32_16x16x32_bf16 v[180:183], a[140:143], v[92:95], v[180:183]\n\t"
    "ds_read_b64_tr_b16 a[174:175], %[ob0] offset:47872\n\t"
    "s_waitcnt lgkmcnt(8)\n\t"
    "v_mfma_f32_16x16x32_bf16 v[176:179], a[144:147], v[96:99], v[176:179]\n\t"
    "ds_read_b64_tr_b16 a[176:177], %[ob0] offset:49920\n\t"
    "v_mfma_f32_16x16x32_bf16 v[176:179], a[148:151], v[100:103], v[176:179]\n\t"
    "ds_read_b64_tr_b16 a[178:179], %[ob0] offset:51968\n\t"
    "buffer_load_dwordx4 v236, %[srdWo], s48 offen offset:1024 lds\n\t"
    "v_mfma_f32_16x16x32_bf16 v[176:179], a[152:155], v[104:107], v[176:179]\n\t"
    "ds_read_b64_tr_b16 a[180:181], %[ob0] offset:54016\n\t"
    "v_mfma_f32_16x16x32_bf16 v[176:179], a[156:159], v[108:111], v[176:179]\n\t"
    "ds_read_b64_tr_b16 a[182:183], %[ob0] offset:56064\n\t"
    "v_mfma_f32_16x16x32_bf16 v[180:183], a[144:147], v[112:115], v[180:183]\n\t"
    "ds_read_b64_tr_b16 a[184:185], %[ob0] offset:58112\n\t"
    "v_mfma_f32_16x16x32_bf16 v[180:183], a[148:151], v[116:119], v[180:183]\n\t"
    "ds_read_b64_tr_b16 a[186:187], %[ob0] offset:60160\n\t"
    "v_mfma_f32_16x16x32_bf16 v[180:183], a[152:155], v[120:123], v[180:183]\n\t"
    "ds_read_b64_tr_b16 a[188:189], %[ob0] offset:62208\n\t"
    "v_mfma_f32_16x16x32_bf16 v[180:183], a[156:159], v[124:127], v[180:183]\n\t"
    "ds_read_b64_tr_b16 a[190:191], %[ob0] offset:64256\n\t"
    "s_waitcnt vmcnt(26)\n\t"
    "s_waitcnt lgkmcnt(0)\n\t"
    "s_barrier\n\t"
    "s_waitcnt lgkmcnt(8)\n\t"
    "v_mfma_f32_16x16x32_bf16 v[184:187], a[160:163], v[64:67], 0\n\t"
    "ds_read_b64_tr_b16 a[128:129], v239 offset:0\n\t"
    "v_mfma_f32_16x16x32_bf16 v[184:187], a[164:167], v[68:71], v[184:187]\n\t"
    "ds_read_b64_tr_b16 a[130:131], v239 offset:2048\n\t"
    "buffer_load_dwordx4 v237, %[srdWo], s48 offen offset:2048 lds\n\t"
    "v_mfma_f32_16x16x32_bf16 v[184:187], a[168:171], v[72:75], v[184:187]\n\t"
    "ds_read_b64_tr_b16 a[132:133], v239 offset:4096\n\t"
    "v_mfma_f32_16x16x32_bf16 v[184:187], a[172:175], v[76:79], v[184:187]\n\t"
    "ds_read_b64_tr_b16 a[134:135], v239 offset:6144\n\t"
    "v_mfma_f32_16x16x32_bf16 v[188:191], a[160:163], v[80:83], 0\n\t"
    "ds_read_b64_tr_b16 a[136:137], v239 offset:8192\n\t"
    "v_mfma_f32_16x16x32_bf16 v[188:191], a[164:167], v[84:87], v[188:191]\n\t"
    "ds_read_b64_tr_b16 a[138:139], v239 offset:10240\n\t"
    "v_mfma_f32_16x16x32_bf16 v[188:191], a[168:171], v[88:91], v[188:191]\n\t"
    "ds_read_b64_tr_b16 a[140:141], v239 offset:12288\n\t"
    "v_mfma_f32_16x16x32_bf16 v[188:191], a[172:175], v[92:95], v[188:191]\n\t"
    "ds_read_b64_tr_b16 a[142:143], v239 offset:14336\n\t"
    "s_waitcnt lgkmcnt(8)\n\t"
    "v_mfma_f32_16x16x32_bf16 v[184:187], a[176:179], v[96:99], v[184:187]\n\t"
    "ds_read_b64_tr_b16 a[144:145], v239 offset:16384\n\t"
    "v_mfma_f32_16x16x32_bf16 v[184:187], a[180:183], v[100:103], v[184:187]\n\t"
    "ds_read_b64_tr_b16 a[146:147], v239 offset:18432\n\t"
    "buffer_load_dwordx4 v238, %[srdWo], s48 offen offset:3072 lds\n\t"
    "v_mfma_f32_16x16x32_bf16 v[184:187], a[184:187], v[104:107], v[184:187]\n\t"
    "ds_read_b64_tr_b16 a[148:149], v239 offset:20480\n\t"
    "v_mfma_f32_16x16x32_bf16 v[184:187], a[188:191], v[108:111], v[184:187]\n\t"
    "ds_read_b64_tr_b16 a[150:151], v239 offset:22528\n\t"
    "v_mfma_f32_16x16x32_bf16 v[188:191], a[176:179], v[112:115], v[188:191]\n\t"
    "ds_read_b64_tr_b16 a[152:153], v239 offset:24576\n\t"
    "v_mfma_f32_16x16x32_bf16 v[188:191], a[180:183], v[116:119], v[188:191]\n\t"
    "ds_read_b64_tr_b16 a[154:155], v239 offset:26624\n\t"
    "v_mfma_f32_16x16x32_bf16 v[188:191], a[184:187], v[120:123], v[188:191]\n\t"
    "ds_read_b64_tr_b16 a[156:157], v239 offset:28672\n\t"
    "v_mfma_f32_16x16x32_bf16 v[188:191], a[188:191], v[124:127], v[188:191]\n\t"
    "ds_read_b64_tr_b16 a[158:159], v239 offset:30720\n\t"
    "s_waitcnt lgkmcnt(8)\n\t"
    "v_mfma_f32_16x16x32_bf16 v[128:131], a[128:131], v[64:67], 0\n\t"
    "ds_read_b64_tr_b16 a[160:161], v239 offset:256\n\t"
    "s_add_u32 m0, s49, 32768\n\t"
    "s_mov_b32 s48, 768\n\t"
    "v_mfma_f32_16x16x32_bf16 v[128:131], a[132:135], v[68:71], v[128:131]\n\t"
    "ds_read_b64_tr_b16 a[162:163], v239 offset:2304\n\t"
    "buffer_load_dwordx4 %[osrc0], %[srdWo], s48 offen offset:0 lds\n\t"
    "v_mfma_f32_16x16x32_bf16 v[128:131], a[136:139], v[72:75], v[128:131]\n\t"
    "ds_read_b64_tr_b16 a[164:165], v239 offset:4352\n\t"
    "v_cvt_pk_bf16_f32 v48, v160, v161\n\t"
    "v_mfma_f32_16x16x32_bf16 v[128:131], a[140:143], v[76:79], v[128:131]\n\t"
    "ds_read_b64_tr_b16 a[166:167], v239 offset:6400\n\t"
    "v_cvt_pk_bf16_f32 v49, v162, v163\n\t"
    "v_mfma_f32_16x16x32_bf16 v[132:135], a[128:131], v[80:83], 0\n\t"
    "ds_read_b64_tr_b16 a[168:169], v239 offset:8448\n\t"
    "v_cvt_pk_bf16_f32 v50, v168, v169\n\t"
    "v_mfma_f32_16x16x32_bf16 v[132:135], a[132:135], v[84:87], v[132:135]\n\t"
    "ds_read_b64_tr_b16 a[170:171], v239 offset:10496\n\t"
    "v_cvt_pk_bf16_f32 v51, v170, v171\n\t"
    "s_mov_b32 s47, 2048\n\t"
    "v_mfma_f32_16x16x32_bf16 v[132:135], a[136:139], v[88:91], v[132:135]\n\t"
    "ds_read_b64_tr_b16 a[172:173], v239 offset:12544\n\t"
    "buffer_store_dwordx4 v[48:51], %[poff], %[srdP], s47 offen sc1\n\t"
    "v_mfma_f32_16x16x32_bf16 v[132:135], a[140:143], v[92:95], v[132:135]\n\t"
    "ds_read_b64_tr_b16 a[174:175], v239 offset:14592\n\t"
    "s_waitcnt lgkmcnt(8)\n\t"
    "v_mfma_f32_16x16x32_bf16 v[128:131], a[144:147], v[96:99], v[128:131]\n\t"
    "ds_read_b64_tr_b16 a[176:177], v239 offset:16640\n\t"
    "v_mfma_f32_16x16x32_bf16 v[128:131], a[148:151], v[100:103], v[128:131]\n\t"
    "ds_read_b64_tr_b16 a[178:179], v239 offset:18688\n\t"
    "buffer_load_dwordx4 v236, %[srdWo], s48 offen offset:1024 lds\n\t"
    "v_mfma_f32_16x16x32_bf16 v[128:131], a[152:155], v[104:107], v[128:131]\n\t"
    "ds_read_b64_tr_b16 a[180:181], v239 offset:20736\n\t"
    "v_cvt_pk_bf16_f32 v52, v164, v165\n\t"
    "v_mfma_f32_16x16x32_bf16 v[128:131], a[156:159], v[108:111], v[128:131]\n\t"
    "ds_read_b64_tr_b16 a[182:183], v239 offset:22784\n\t"
    "v_cvt_pk_bf16_f32 v53, v166, v167\n\t"
    "v_mfma_f32_16x16x32_bf16 v[132:135], a[144:147], v[112:115], v[132:135]\n\t"
    "ds_read_b64_tr_b16 a[184:185], v239 offset:24832\n\t"
    "v_cvt_pk_bf16_f32 v54, v172, v173\n\t"
    "v_mfma_f32_16x16x32_bf16 v[132:135], a[148:151], v[116:119], v[132:135]\n\t"
    "ds_read_b64_tr_b16 a[186:187], v239 offset:26880\n\t"
    "v_cvt_pk_bf16_f32 v55, v174, v175\n\t"
    "s_mov_b32 s47, 20480\n\t"
    "v_mfma_f32_16x16x32_bf16 v[132:135], a[152:155], v[120:123], v[132:135]\n\t"
    "ds_read_b64_tr_b16 a[188:189], v239 offset:28928\n\t"
    "buffer_store_dwordx4 v[52:55], %[poff], %[srdP], s47 offen sc1\n\t"
    "v_mfma_f32_16x16x32_bf16 v[132:135], a[156:159], v[124:127], v[132:135]\n\t"
    "ds_read_b64_tr_b16 a[190:191], v239 offset:30976\n\t"
    "s_waitcnt lgkmcnt(8)\n\t"
    "v_mfma_f32_16x16x32_bf16 v[136:139], a[160:163], v[64:67], 0\n\t"
    "ds_read_b64_tr_b16 a[128:129], v239 offset:512\n\t"
    "v_mfma_f32_16x16x32_bf16 v[136:139], a[164:167], v[68:71], v[136:139]\n\t"
    "ds_read_b64_tr_b16 a[130:131], v239 offset:2560\n\t"
    "buffer_load_dwordx4 v237, %[srdWo], s48 offen offset:2048 lds\n\t"
    "v_mfma_f32_16x16x32_bf16 v[136:139], a[168:171], v[72:75], v[136:139]\n\t"
    "ds_read_b64_tr_b16 a[132:133], v239 offset:4608\n\t"
    "v_cvt_pk_bf16_f32 v56, v176, v177\n\t"
    "v_mfma_f32_16x16x32_bf16 v[136:139], a[172:175], v[76:79], v[136:139]\n\t"
    "ds_read_b64_tr_b16 a[134:135], v239 offset:6656\n\t"
    "v_cvt_pk_bf16_f32 v57, v178, v179\n\t"
    "v_mfma_f32_16x16x32_bf16 v[140:143], a[160:163], v[80:83], 0\n\t"
    "ds_read_b64_tr_b16 a[136:137], v239 offset:8704\n\t"
    "v_cvt_pk_bf16_f32 v58, v184, v185\n\t"
    "v_mfma_f32_16x16x32_bf16 v[140:143], a[164:167], v[84:87], v[140:143]\n\t"
    "ds_read_b64_tr_b16 a[138:139], v239 offset:10752\n\t"
    "v_cvt_pk_bf16_f32 v59, v186, v187\n\t"
    "s_mov_b32 s47, 3072\n\t"
    "v_mfma_f32_16x16x32_bf16 v[140:143], a[168:171], v[88:91], v[140:143]\n\t"
    "ds_read_b64_tr_b16 a[140:141], v239 offset:12800\n\t"
    "buffer_store_dwordx4 v[56:59], %[poff], %[srdP], s47 offen sc1\n\t"
    "v_mfma_f32_16x16x32_bf16 v[140:143], a[172:175], v[92:95], v[140:143]\n\t"
    "ds_read_b64_tr_b16 a[142:143], v239 offset:14848\n\t"
    "s_waitcnt lgkmcnt(8)\n\t"
    "v_mfma_f32_16x16x32_bf16 v[136:139], a[176:179], v[96:99], v[136:139]\n\t"
    "ds_read_b64_tr_b16 a[144:145], v239 offset:16896\n\t"
    "v_mfma_f32_16x16x32_bf16 v[136:139], a[180:183], v[100:103], v[136:139]\n\t"
    "ds_read_b64_tr_b16 a[146:147], v239 offset:18944\n\t"
    "buffer_load_dwordx4 v238, %[srdWo], s48 offen offset:3072 lds\n\t"
    "v_mfma_f32_16x16x32_bf16 v[136:139], a[184:187], v[104:107], v[136:139]\n\t"
    "ds_read_b64_tr_b16 a[148:149], v239 offset:20992\n\t"
    "v_cvt_pk_bf16_f32 v60, v180, v181\n\t"
    "v_mfma_f32_16x16x32_bf16 v[136:139], a[188:191], v[108:111], v[136:139]\n\t"
    "ds_read_b64_tr_b16 a[150:151], v239 offset:23040\n\t"
    "v_cvt_pk_bf16_f32 v61, v182, v183\n\t"
    "v_mfma_f32_16x16x32_bf16 v[140:143], a[176:179], v[112:115], v[140:143]\n\t"
    "ds_read_b64_tr_b16 a[152:153], v239 offset:25088\n\t"
    "v_cvt_pk_bf16_f32 v62, v188, v189\n\t"
    "v_mfma_f32_16x16x32_bf16 v[140:143], a[180:183], v[116:119], v[140:143]\n\t"
    "ds_read_b64_tr_b16 a[154:155], v239 offset:27136\n\t"
    "v_cvt_pk_bf16_f32 v63, v190, v191\n\t"
    "s_mov_b32 s47, 21504\n\t"
    "v_mfma_f32_16x16x32_bf16 v[140:143], a[184:187], v[120:123], v[140:143]\n\t"
    "ds_read_b64_tr_b16 a[156:157], v239 offset:29184\n\t"
    "buffer_store_dwordx4 v[60:63], %[poff], %[srdP], s47 offen sc1\n\t"
    "v_mfma_f32_16x16x32_bf16 v[140:143], a[188:191], v[124:127], v[140:143]\n\t"
    "ds_read_b64_tr_b16 a[158:159], v239 offset:31232\n\t"
    "s_waitcnt lgkmcnt(8)\n\t"
    "v_mfma_f32_16x16x32_bf16 v[144:147], a[128:131], v[64:67], 0\n\t"
    "ds_read_b64_tr_b16 a[160:161], v239 offset:768\n\t"
    "s_add_u32 m0, s49, 36864\n\t"
    "s_mov_b32 s48, 37632\n\t"
    "v_mfma_f32_16x16x32_bf16 v[144:147], a[132:135], v[68:71], v[144:147]\n\t"
    "ds_read_b64_tr_b16 a[162:163], v239 offset:2816\n\t"
    "buffer_load_dwordx4 %[osrc0], %[srdWo], s48 offen offset:0 lds\n\t"
    "v_mfma_f32_16x16x32_bf16 v[144:147], a[136:139], v[72:75], v[144:147]\n\t"
    "ds_read_b64_tr_b16 a[164:165], v239 offset:4864\n\t"
    "v_mfma_f32_16x16x32_bf16 v[144:147], a[140:143], v[76:79], v[144:147]\n\t"
    "ds_read_b64_tr_b16 a[166:167], v239 offset:6912\n\t"
    "v_mfma_f32_16x16x32_bf16 v[148:151], a[128:131], v[80:83], 0\n\t"
    "ds_read_b64_tr_b16 a[168:169], v239 offset:8960\n\t"
    "v_mfma_f32_16x16x32_bf16 v[148:151], a[132:135], v[84:87], v[148:151]\n\t"
    "ds_read_b64_tr_b16 a[170:171], v239 offset:11008\n\t"
    "v_mfma_f32_16x16x32_bf16 v[148:151], a[136:139], v[88:91], v[148:151]\n\t"
    "ds_read_b64_tr_b16 a[172:173], v239 offset:13056\n\t"
    "v_mfma_f32_16x16x32_bf16 v[148:151], a[140:143], v[92:95], v[148:151]\n\t"
    "ds_read_b64_tr_b16 a[174:175], v239 offset:15104\n\t"
    "s_waitcnt lgkmcnt(8)\n\t"
    "v_mfma_f32_16x16x32_bf16 v[144:147], a[144:147], v[96:99], v[144:147]\n\t"
    "ds_read_b64_tr_b16 a[176:177], v239 offset:17152\n\t"
    "v_mfma_f32_16x16x32_bf16 v[144:147], a[148:151], v[100:103], v[144:147]\n\t"
    "ds_read_b64_tr_b16 a[178:179], v239 offset:19200\n\t"
    "buffer_load_dwordx4 v236, %[srdWo], s48 offen offset:1024 lds\n\t"
    "v_mfma_f32_16x16x32_bf16 v[144:147], a[152:155], v[104:107], v[144:147]\n\t"
    "ds_read_b64_tr_b16 a[180:181], v239 offset:21248\n\t"
    "v_mfma_f32_16x16x32_bf16 v[144:147], a[156:159], v[108:111], v[144:147]\n\t"
    "ds_read_b64_tr_b16 a[182:183], v239 offset:23296\n\t"
    "v_mfma_f32_16x16x32_bf16 v[148:151], a[144:147], v[112:115], v[148:151]\n\t"
    "ds_read_b64_tr_b16 a[184:185], v239 offset:25344\n\t"
    "v_mfma_f32_16x16x32_bf16 v[148:151], a[148:151], v[116:119], v[148:151]\n\t"
    "ds_read_b64_tr_b16 a[186:187], v239 offset:27392\n\t"
    "v_mfma_f32_16x16x32_bf16 v[148:151], a[152:155], v[120:123], v[148:151]\n\t"
    "ds_read_b64_tr_b16 a[188:189], v239 offset:29440\n\t"
    "v_mfma_f32_16x16x32_bf16 v[148:151], a[156:159], v[124:127], v[148:151]\n\t"
    "ds_read_b64_tr_b16 a[190:191], v239 offset:31488\n\t"
    "s_waitcnt vmcnt(30)\n\t"
    "s_waitcnt lgkmcnt(0)\n\t"
    "s_barrier\n\t"
    "s_waitcnt lgkmcnt(8)\n\t"
    "v_mfma_f32_16x16x32_bf16 v[152:155], a[160:163], v[64:67], 0\n\t"
    "ds_read_b64_tr_b16 a[128:129], v240 offset:0\n\t"
    "v_mfma_f32_16x16x32_bf16 v[152:155], a[164:167], v[68:71], v[152:155]\n\t"
    "ds_read_b64_tr_b16 a[130:131], v240 offset:2048\n\t"
    "buffer_load_dwordx4 v237, %[srdWo], s48 offen offset:2048 lds\n\t"
    "v_mfma_f32_16x16x32_bf16 v[152:155], a[168:171], v[72:75], v[152:155]\n\t"
    "ds_read_b64_tr_b16 a[132:133], v240 offset:4096\n\t"
    "v_mfma_f32_16x16x32_bf16 v[152:155], a[172:175], v[76:79], v[152:155]\n\t"
    "ds_read_b64_tr_b16 a[134:135], v240 offset:6144\n\t"
    "v_mfma_f32_16x16x32_bf16 v[156:159], a[160:163], v[80:83], 0\n\t"
    "ds_read_b64_tr_b16 a[136:137], v240 offset:8192\n\t"
    "v_mfma_f32_16x16x32_bf16 v[156:159], a[164:167], v[84:87], v[156:159]\n\t"
    "ds_read_b64_tr_b16 a[138:139], v240 offset:10240\n\t"
    "v_mfma_f32_16x16x32_bf16 v[156:159], a[168:171], v[88:91], v[156:159]\n\t"
    "ds_read_b64_tr_b16 a[140:141], v240 offset:12288\n\t"
    "v_mfma_f32_16x16x32_bf16 v[156:159], a[172:175], v[92:95], v[156:159]\n\t"
    "ds_read_b64_tr_b16 a[142:143], v240 offset:14336\n\t"
    "s_waitcnt lgkmcnt(8)\n\t"
    "v_mfma_f32_16x16x32_bf16 v[152:155], a[176:179], v[96:99], v[152:155]\n\t"
    "ds_read_b64_tr_b16 a[144:145], v240 offset:16384\n\t"
    "v_mfma_f32_16x16x32_bf16 v[152:155], a[180:183], v[100:103], v[152:155]\n\t"
    "ds_read_b64_tr_b16 a[146:147], v240 offset:18432\n\t"
    "buffer_load_dwordx4 v238, %[srdWo], s48 offen offset:3072 lds\n\t"
    "v_mfma_f32_16x16x32_bf16 v[152:155], a[184:187], v[104:107], v[152:155]\n\t"
    "ds_read_b64_tr_b16 a[148:149], v240 offset:20480\n\t"
    "v_mfma_f32_16x16x32_bf16 v[152:155], a[188:191], v[108:111], v[152:155]\n\t"
    "ds_read_b64_tr_b16 a[150:151], v240 offset:22528\n\t"
    "v_mfma_f32_16x16x32_bf16 v[156:159], a[176:179], v[112:115], v[156:159]\n\t"
    "ds_read_b64_tr_b16 a[152:153], v240 offset:24576\n\t"
    "v_mfma_f32_16x16x32_bf16 v[156:159], a[180:183], v[116:119], v[156:159]\n\t"
    "ds_read_b64_tr_b16 a[154:155], v240 offset:26624\n\t"
    "v_mfma_f32_16x16x32_bf16 v[156:159], a[184:187], v[120:123], v[156:159]\n\t"
    "ds_read_b64_tr_b16 a[156:157], v240 offset:28672\n\t"
    "v_mfma_f32_16x16x32_bf16 v[156:159], a[188:191], v[124:127], v[156:159]\n\t"
    "ds_read_b64_tr_b16 a[158:159], v240 offset:30720\n\t"
    "s_waitcnt lgkmcnt(8)\n\t"
    "v_mfma_f32_16x16x32_bf16 v[160:163], a[128:131], v[64:67], 0\n\t"
    "ds_read_b64_tr_b16 a[160:161], v240 offset:256\n\t"
    "s_add_u32 m0, s49, 65536\n\t"
    "s_mov_b32 s48, 896\n\t"
    "v_mfma_f32_16x16x32_bf16 v[160:163], a[132:135], v[68:71], v[160:163]\n\t"
    "ds_read_b64_tr_b16 a[162:163], v240 offset:2304\n\t"
    "buffer_load_dwordx4 %[osrc0], %[srdWo], s48 offen offset:0 lds\n\t"
    "v_mfma_f32_16x16x32_bf16 v[160:163], a[136:139], v[72:75], v[160:163]\n\t"
    "ds_read_b64_tr_b16 a[164:165], v240 offset:4352\n\t"
    "v_cvt_pk_bf16_f32 v32, v128, v129\n\t"
    "v_mfma_f32_16x16x32_bf16 v[160:163], a[140:143], v[76:79], v[160:163]\n\t"
    "ds_read_b64_tr_b16 a[166:167], v240 offset:6400\n\t"
    "v_cvt_pk_bf16_f32 v33, v130, v131\n\t"
    "v_mfma_f32_16x16x32_bf16 v[164:167], a[128:131], v[80:83], 0\n\t"
    "ds_read_b64_tr_b16 a[168:169], v240 offset:8448\n\t"
    "v_cvt_pk_bf16_f32 v34, v136, v137\n\t"
    "v_mfma_f32_16x16x32_bf16 v[164:167], a[132:135], v[84:87], v[164:167]\n\t"
    "ds_read_b64_tr_b16 a[170:171], v240 offset:10496\n\t"
    "v_cvt_pk_bf16_f32 v35, v138, v139\n\t"
    "s_mov_b32 s47, 4096\n\t"
    "v_mfma_f32_16x16x32_bf16 v[164:167], a[136:139], v[88:91], v[164:167]\n\t"
    "ds_read_b64_tr_b16 a[172:173], v240 offset:12544\n\t"
    "buffer_store_dwordx4 v[32:35], %[poff], %[srdP], s47 offen sc1\n\t"
    "v_mfma_f32_16x16x32_bf16 v[164:167], a[140:143], v[92:95], v[164:167]\n\t"
    "ds_read_b64_tr_b16 a[174:175], v240 offset:14592\n\t"
    "s_waitcnt lgkmcnt(8)\n\t"
    "v_mfma_f32_16x16x32_bf16 v[160:163], a[144:147], v[96:99], v[160:163]\n\t"
    "ds_read_b64_tr_b16 a[176:177], v240 offset:16640\n\t"
    "v_mfma_f32_16x16x32_bf16 v[160:163], a[148:151], v[100:103], v[160:163]\n\t"
    "ds_read_b64_tr_b16 a[178:179], v240 offset:18688\n\t"
    "buffer_load_dwordx4 v236, %[srdWo], s48 offen offset:1024 lds\n\t"
    "v_mfma_f32_16x16x32_bf16 v[160:163], a[152:155], v[104:107], v[160:163]\n\t"
    "ds_read_b64_tr_b16 a[180:181], v240 offset:20736\n\t"
    "v_cvt_pk_bf16_f32 v36, v132, v133\n\t"
    "v_mfma_f32_16x16x32_bf16 v[160:163], a[156:159], v[108:111], v[160:163]\n\t"
    "ds_read_b64_tr_b16 a[182:183], v240 offset:22784\n\t"
    "v_cvt_pk_bf16_f32 v37, v134, v135\n\t"
    "v_mfma_f32_16x16x32_bf16 v[164:167], a[144:147], v[112:115], v[164:167]\n\t"
    "ds_read_b64_tr_b16 a[184:185], v240 offset:24832\n\t"
    "v_cvt_pk_bf16_f32 v38, v140, v141\n\t"
    "v_mfma_f32_16x16x32_bf16 v[164:167], a[148:151], v[116:119], v[164:167]\n\t"
    "ds_read_b64_tr_b16 a[186:187], v240 offset:26880\n\t"
    "v_cvt_pk_bf16_f32 v39, v142, v143\n\t"
    "s_mov_b32 s47, 22528\n\t"
    "v_mfma_f32_16x16x32_bf16 v[164:167], a[152:155], v[120:123], v[164:167]\n\t"
    "ds_read_b64_tr_b16 a[188:189], v240 offset:28928\n\t"
    "buffer_store_dwordx4 v[36:39], %[poff], %[srdP], s47 offen sc1\n\t"
    "v_mfma_f32_16x16x32_bf16 v[164:167], a[156:159], v[124:127], v[164:167]\n\t"
    "ds_read_b64_tr_b16 a[190:191], v240 offset:30976\n\t"
    "s_waitcnt lgkmcnt(8)\n\t"
    "v_mfma_f32_16x16x32_bf16 v[168:171], a[160:163], v[64:67], 0\n\t"
    "ds_read_b64_tr_b16 a[128:129], v240 offset:512\n\t"
    "v_mfma_f32_16x16x32_bf16 v[168:171], a[164:167], v[68:71], v[168:171]\n\t"
    "ds_read_b64_tr_b16 a[130:131], v240 offset:2560\n\t"
    "buffer_load_dwordx4 v237, %[srdWo], s48 offen offset:2048 lds\n\t"
    "v_mfma_f32_16x16x32_bf16 v[168:171], a[168:171], v[72:75], v[168:171]\n\t"
    "ds_read_b64_tr_b16 a[132:133], v240 offset:4608\n\t"
    "v_cvt_pk_bf16_f32 v40, v144, v145\n\t"
    "v_mfma_f32_16x16x32_bf16 v[168:171], a[172:175], v[76:79], v[168:171]\n\t"
    "ds_read_b64_tr_b16 a[134:135], v240 offset:6656\n\t"
    "v_cvt_pk_bf16_f32 v41, v146, v147\n\t"
    "v_mfma_f32_16x16x32_bf16 v[172:175], a[160:163], v[80:83], 0\n\t"
    "ds_read_b64_tr_b16 a[136:137], v240 offset:8704\n\t"
    "v_cvt_pk_bf16_f32 v42, v152, v153\n\t"
    "v_mfma_f32_16x16x32_bf16 v[172:175], a[164:167], v[84:87], v[172:175]\n\t"
    "ds_read_b64_tr_b16 a[138:139], v240 offset:10752\n\t"
    "v_cvt_pk_bf16_f32 v43, v154, v155\n\t"
    "s_mov_b32 s47, 5120\n\t"
    "v_mfma_f32_16x16x32_bf16 v[172:175], a[168:171], v[88:91], v[172:175]\n\t"
    "ds_read_b64_tr_b16 a[140:141], v240 offset:12800\n\t"
    "buffer_store_dwordx4 v[40:43], %[poff], %[srdP], s47 offen sc1\n\t"
    "v_mfma_f32_16x16x32_bf16 v[172:175], a[172:175], v[92:95], v[172:175]\n\t"
    "ds_read_b64_tr_b16 a[142:143], v240 offset:14848\n\t"
    "s_waitcnt lgkmcnt(8)\n\t"
    "v_mfma_f32_16x16x32_bf16 v[168:171], a[176:179], v[96:99], v[168:171]\n\t"
    "ds_read_b64_tr_b16 a[144:145], v240 offset:16896\n\t"
    "v_mfma_f32_16x16x32_bf16 v[168:171], a[180:183], v[100:103], v[168:171]\n\t"
    "ds_read_b64_tr_b16 a[146:147], v240 offset:18944\n\t"
    "buffer_load_dwordx4 v238, %[srdWo], s48 offen offset:3072 lds\n\t"
    "v_mfma_f32_16x16x32_bf16 v[168:171], a[184:187], v[104:107], v[168:171]\n\t"
    "ds_read_b64_tr_b16 a[148:149], v240 offset:20992\n\t"
    "v_cvt_pk_bf16_f32 v44, v148, v149\n\t"
    "v_mfma_f32_16x16x32_bf16 v[168:171], a[188:191], v[108:111], v[168:171]\n\t"
    "ds_read_b64_tr_b16 a[150:151], v240 offset:23040\n\t"
    "v_cvt_pk_bf16_f32 v45, v150, v151\n\t"
    "v_mfma_f32_16x16x32_bf16 v[172:175], a[176:179], v[112:115], v[172:175]\n\t"
    "ds_read_b64_tr_b16 a[152:153], v240 offset:25088\n\t"
    "v_cvt_pk_bf16_f32 v46, v156, v157\n\t"
    "v_mfma_f32_16x16x32_bf16 v[172:175], a[180:183], v[116:119], v[172:175]\n\t"
    "ds_read_b64_tr_b16 a[154:155], v240 offset:27136\n\t"
    "v_cvt_pk_bf16_f32 v47, v158, v159\n\t"
    "s_mov_b32 s47, 23552\n\t"
    "v_mfma_f32_16x16x32_bf16 v[172:175], a[184:187], v[120:123], v[172:175]\n\t"
    "ds_read_b64_tr_b16 a[156:157], v240 offset:29184\n\t"
    "buffer_store_dwordx4 v[44:47], %[poff], %[srdP], s47 offen sc1\n\t"
    "v_mfma_f32_16x16x32_bf16 v[172:175], a[188:191], v[124:127], v[172:175]\n\t"
    "ds_read_b64_tr_b16 a[158:159], v240 offset:31232\n\t"
    "s_waitcnt lgkmcnt(8)\n\t"
    "v_mfma_f32_16x16x32_bf16 v[176:179], a[128:131], v[64:67], 0\n\t"
    "ds_read_b64_tr_b16 a[160:161], v240 offset:768\n\t"
    "s_add_u32 m0, s49, 69632\n\t"
    "s_mov_b32 s48, 37760\n\t"
    "v_mfma_f32_16x16x32_bf16 v[176:179], a[132:135], v[68:71], v[176:179]\n\t"
    "ds_read_b64_tr_b16 a[162:163], v240 offset:2816\n\t"
    "buffer_load_dwordx4 %[osrc0], %[srdWo], s48 offen offset:0 lds\n\t"
    "v_mfma_f32_16x16x32_bf16 v[176:179], a[136:139], v[72:75], v[176:179]\n\t"
    "ds_read_b64_tr_b16 a[164:165], v240 offset:4864\n\t"
    "v_mfma_f32_16x16x32_bf16 v[176:179], a[140:143], v[76:79], v[176:179]\n\t"
    "ds_read_b64_tr_b16 a[166:167], v240 offset:6912\n\t"
    "v_mfma_f32_16x16x32_bf16 v[180:183], a[128:131], v[80:83], 0\n\t"
    "ds_read_b64_tr_b16 a[168:169], v240 offset:8960\n\t"
    "v_mfma_f32_16x16x32_bf16 v[180:183], a[132:135], v[84:87], v[180:183]\n\t"
    "ds_read_b64_tr_b16 a[170:171], v240 offset:11008\n\t"
    "v_mfma_f32_16x16x32_bf16 v[180:183], a[136:139], v[88:91], v[180:183]\n\t"
    "ds_read_b64_tr_b16 a[172:173], v240 offset:13056\n\t"
    "v_mfma_f32_16x16x32_bf16 v[180:183], a[140:143], v[92:95], v[180:183]\n\t"
    "ds_read_b64_tr_b16 a[174:175], v240 offset:15104\n\t"
    "s_waitcnt lgkmcnt(8)\n\t"
    "v_mfma_f32_16x16x32_bf16 v[176:179], a[144:147], v[96:99], v[176:179]\n\t"
    "ds_read_b64_tr_b16 a[176:177], v240 offset:17152\n\t"
    "v_mfma_f32_16x16x32_bf16 v[176:179], a[148:151], v[100:103], v[176:179]\n\t"
    "ds_read_b64_tr_b16 a[178:179], v240 offset:19200\n\t"
    "buffer_load_dwordx4 v236, %[srdWo], s48 offen offset:1024 lds\n\t"
    "v_mfma_f32_16x16x32_bf16 v[176:179], a[152:155], v[104:107], v[176:179]\n\t"
    "ds_read_b64_tr_b16 a[180:181], v240 offset:21248\n\t"
    "v_mfma_f32_16x16x32_bf16 v[176:179], a[156:159], v[108:111], v[176:179]\n\t"
    "ds_read_b64_tr_b16 a[182:183], v240 offset:23296\n\t"
    "v_mfma_f32_16x16x32_bf16 v[180:183], a[144:147], v[112:115], v[180:183]\n\t"
    "ds_read_b64_tr_b16 a[184:185], v240 offset:25344\n\t"
    "v_mfma_f32_16x16x32_bf16 v[180:183], a[148:151], v[116:119], v[180:183]\n\t"
    "ds_read_b64_tr_b16 a[186:187], v240 offset:27392\n\t"
    "v_mfma_f32_16x16x32_bf16 v[180:183], a[152:155], v[120:123], v[180:183]\n\t"
    "ds_read_b64_tr_b16 a[188:189], v240 offset:29440\n\t"
    "v_mfma_f32_16x16x32_bf16 v[180:183], a[156:159], v[124:127], v[180:183]\n\t"
    "ds_read_b64_tr_b16 a[190:191], v240 offset:31488\n\t"
    "s_waitcnt vmcnt(34)\n\t"
    "s_waitcnt lgkmcnt(0)\n\t"
    "s_barrier\n\t"
    "s_waitcnt lgkmcnt(8)\n\t"
    "v_mfma_f32_16x16x32_bf16 v[184:187], a[160:163], v[64:67], 0\n\t"
    "ds_read_b64_tr_b16 a[128:129], v239 offset:32768\n\t"
    "v_mfma_f32_16x16x32_bf16 v[184:187], a[164:167], v[68:71], v[184:187]\n\t"
    "ds_read_b64_tr_b16 a[130:131], v239 offset:34816\n\t"
    "buffer_load_dwordx4 v237, %[srdWo], s48 offen offset:2048 lds\n\t"
    "v_mfma_f32_16x16x32_bf16 v[184:187], a[168:171], v[72:75], v[184:187]\n\t"
    "ds_read_b64_tr_b16 a[132:133], v239 offset:36864\n\t"
    "v_mfma_f32_16x16x32_bf16 v[184:187], a[172:175], v[76:79], v[184:187]\n\t"
    "ds_read_b64_tr_b16 a[134:135], v239 offset:38912\n\t"
    "v_mfma_f32_16x16x32_bf16 v[188:191], a[160:163], v[80:83], 0\n\t"
    "ds_read_b64_tr_b16 a[136:137], v239 offset:40960\n\t"
    "v_mfma_f32_16x16x32_bf16 v[188:191], a[164:167], v[84:87], v[188:191]\n\t"
    "ds_read_b64_tr_b16 a[138:139], v239 offset:43008\n\t"
    "v_mfma_f32_16x16x32_bf16 v[188:191], a[168:171], v[88:91], v[188:191]\n\t"
    "ds_read_b64_tr_b16 a[140:141], v239 offset:45056\n\t"
    "v_mfma_f32_16x16x32_bf16 v[188:191], a[172:175], v[92:95], v[188:191]\n\t"
    "ds_read_b64_tr_b16 a[142:143], v239 offset:47104\n\t"
    "s_waitcnt lgkmcnt(8)\n\t"
    "v_mfma_f32_16x16x32_bf16 v[184:187], a[176:179], v[96:99], v[184:187]\n\t"
    "ds_read_b64_tr_b16 a[144:145], v239 offset:49152\n\t"
    "v_mfma_f32_16x16x32_bf16 v[184:187], a[180:183], v[100:103], v[184:187]\n\t"
    "ds_read_b64_tr_b16 a[146:147], v239 offset:51200\n\t"
    "buffer_load_dwordx4 v238, %[srdWo], s48 offen offset:3072 lds\n\t"
    "v_mfma_f32_16x16x32_bf16 v[184:187], a[184:187], v[104:107], v[184:187]\n\t"
    "ds_read_b64_tr_b16 a[148:149], v239 offset:53248\n\t"
    "v_mfma_f32_16x16x32_bf16 v[184:187], a[188:191], v[108:111], v[184:187]\n\t"
    "ds_read_b64_tr_b16 a[150:151], v239 offset:55296\n\t"
    "v_mfma_f32_16x16x32_bf16 v[188:191], a[176:179], v[112:115], v[188:191]\n\t"
    "ds_read_b64_tr_b16 a[152:153], v239 offset:57344\n\t"
    "v_mfma_f32_16x16x32_bf16 v[188:191], a[180:183], v[116:119], v[188:191]\n\t"
    "ds_read_b64_tr_b16 a[154:155], v239 offset:59392\n\t"
    "v_mfma_f32_16x16x32_bf16 v[188:191], a[184:187], v[120:123], v[188:191]\n\t"
    "ds_read_b64_tr_b16 a[156:157], v239 offset:61440\n\t"
    "v_mfma_f32_16x16x32_bf16 v[188:191], a[188:191], v[124:127], v[188:191]\n\t"
    "ds_read_b64_tr_b16 a[158:159], v239 offset:63488\n\t"
    "s_waitcnt lgkmcnt(8)\n\t"
    "v_mfma_f32_16x16x32_bf16 v[128:131], a[128:131], v[64:67], 0\n\t"
    "ds_read_b64_tr_b16 a[160:161], v239 offset:33024\n\t"
    "s_add_u32 m0, s49, 131072\n\t"
    "s_mov_b32 s48, 1024\n\t"
    "v_mfma_f32_16x16x32_bf16 v[128:131], a[132:135], v[68:71], v[128:131]\n\t"
    "ds_read_b64_tr_b16 a[162:163], v239 offset:35072\n\t"
    "buffer_load_dwordx4 %[osrc0], %[srdWo], s48 offen offset:0 lds\n\t"
    "v_mfma_f32_16x16x32_bf16 v[128:131], a[136:139], v[72:75], v[128:131]\n\t"
    "ds_read_b64_tr_b16 a[164:165], v239 offset:37120\n\t"
    "v_cvt_pk_bf16_f32 v48, v160, v161\n\t"
    "v_mfma_f32_16x16x32_bf16 v[128:131], a[140:143], v[76:79], v[128:131]\n\t"
    "ds_read_b64_tr_b16 a[166:167], v239 offset:39168\n\t"
    "v_cvt_pk_bf16_f32 v49, v162, v163\n\t"
    "v_mfma_f32_16x16x32_bf16 v[132:135], a[128:131], v[80:83], 0\n\t"
    "ds_read_b64_tr_b16 a[168:169], v239 offset:41216\n\t"
    "v_cvt_pk_bf16_f32 v50, v168, v169\n\t"
    "v_mfma_f32_16x16x32_bf16 v[132:135], a[132:135], v[84:87], v[132:135]\n\t"
    "ds_read_b64_tr_b16 a[170:171], v239 offset:43264\n\t"
    "v_cvt_pk_bf16_f32 v51, v170, v171\n\t"
    "s_mov_b32 s47, 6144\n\t"
    "v_mfma_f32_16x16x32_bf16 v[132:135], a[136:139], v[88:91], v[132:135]\n\t"
    "ds_read_b64_tr_b16 a[172:173], v239 offset:45312\n\t"
    "buffer_store_dwordx4 v[48:51], %[poff], %[srdP], s47 offen sc1\n\t"
    "v_mfma_f32_16x16x32_bf16 v[132:135], a[140:143], v[92:95], v[132:135]\n\t"
    "ds_read_b64_tr_b16 a[174:175], v239 offset:47360\n\t"
    "s_waitcnt lgkmcnt(8)\n\t"
    "v_mfma_f32_16x16x32_bf16 v[128:131], a[144:147], v[96:99], v[128:131]\n\t"
    "ds_read_b64_tr_b16 a[176:177], v239 offset:49408\n\t"
    "v_mfma_f32_16x16x32_bf16 v[128:131], a[148:151], v[100:103], v[128:131]\n\t"
    "ds_read_b64_tr_b16 a[178:179], v239 offset:51456\n\t"
    "buffer_load_dwordx4 v236, %[srdWo], s48 offen offset:1024 lds\n\t"
    "v_mfma_f32_16x16x32_bf16 v[128:131], a[152:155], v[104:107], v[128:131]\n\t"
    "ds_read_b64_tr_b16 a[180:181], v239 offset:53504\n\t"
    "v_cvt_pk_bf16_f32 v52, v164, v165\n\t"
    "v_mfma_f32_16x16x32_bf16 v[128:131], a[156:159], v[108:111], v[128:131]\n\t"
    "ds_read_b64_tr_b16 a[182:183], v239 offset:55552\n\t"
    "v_cvt_pk_bf16_f32 v53, v166, v167\n\t"
    "v_mfma_f32_16x16x32_bf16 v[132:135], a[144:147], v[112:115], v[132:135]\n\t"
    "ds_read_b64_tr_b16 a[184:185], v239 offset:57600\n\t"
    "v_cvt_pk_bf16_f32 v54, v172, v173\n\t"
    "v_mfma_f32_16x16x32_bf16 v[132:135], a[148:151], v[116:119], v[132:135]\n\t"
    "ds_read_b64_tr_b16 a[186:187], v239 offset:59648\n\t"
    "v_cvt_pk_bf16_f32 v55, v174, v175\n\t"
    "s_mov_b32 s47, 24576\n\t"
    "v_mfma_f32_16x16x32_bf16 v[132:135], a[152:155], v[120:123], v[132:135]\n\t"
    "ds_read_b64_tr_b16 a[188:189], v239 offset:61696\n\t"
    "buffer_store_dwordx4 v[52:55], %[poff], %[srdP], s47 offen sc1\n\t"
    "v_mfma_f32_16x16x32_bf16 v[132:135], a[156:159], v[124:127], v[132:135]\n\t"
    "ds_read_b64_tr_b16 a[190:191], v239 offset:63744\n\t"
    "s_waitcnt lgkmcnt(8)\n\t"
    "v_mfma_f32_16x16x32_bf16 v[136:139], a[160:163], v[64:67], 0\n\t"
    "ds_read_b64_tr_b16 a[128:129], v239 offset:33280\n\t"
    "v_mfma_f32_16x16x32_bf16 v[136:139], a[164:167], v[68:71], v[136:139]\n\t"
    "ds_read_b64_tr_b16 a[130:131], v239 offset:35328\n\t"
    "buffer_load_dwordx4 v237, %[srdWo], s48 offen offset:2048 lds\n\t"
    "v_mfma_f32_16x16x32_bf16 v[136:139], a[168:171], v[72:75], v[136:139]\n\t"
    "ds_read_b64_tr_b16 a[132:133], v239 offset:37376\n\t"
    "v_cvt_pk_bf16_f32 v56, v176, v177\n\t"
    "v_mfma_f32_16x16x32_bf16 v[136:139], a[172:175], v[76:79], v[136:139]\n\t"
    "ds_read_b64_tr_b16 a[134:135], v239 offset:39424\n\t"
    "v_cvt_pk_bf16_f32 v57, v178, v179\n\t"
    "v_mfma_f32_16x16x32_bf16 v[140:143], a[160:163], v[80:83], 0\n\t"
    "ds_read_b64_tr_b16 a[136:137], v239 offset:41472\n\t"
    "v_cvt_pk_bf16_f32 v58, v184, v185\n\t"
    "v_mfma_f32_16x16x32_bf16 v[140:143], a[164:167], v[84:87], v[140:143]\n\t"
    "ds_read_b64_tr_b16 a[138:139], v239 offset:43520\n\t"
    "v_cvt_pk_bf16_f32 v59, v186, v187\n\t"
    "s_mov_b32 s47, 7168\n\t"
    "v_mfma_f32_16x16x32_bf16 v[140:143], a[168:171], v[88:91], v[140:143]\n\t"
    "ds_read_b64_tr_b16 a[140:141], v239 offset:45568\n\t"
    "buffer_store_dwordx4 v[56:59], %[poff], %[srdP], s47 offen sc1\n\t"
    "v_mfma_f32_16x16x32_bf16 v[140:143], a[172:175], v[92:95], v[140:143]\n\t"
    "ds_read_b64_tr_b16 a[142:143], v239 offset:47616\n\t"
    "s_waitcnt lgkmcnt(8)\n\t"
    "v_mfma_f32_16x16x32_bf16 v[136:139], a[176:179], v[96:99], v[136:139]\n\t"
    "ds_read_b64_tr_b16 a[144:145], v239 offset:49664\n\t"
    "v_mfma_f32_16x16x32_bf16 v[136:139], a[180:183], v[100:103], v[136:139]\n\t"
    "ds_read_b64_tr_b16 a[146:147], v239 offset:51712\n\t"
    "buffer_load_dwordx4 v238, %[srdWo], s48 offen offset:3072 lds\n\t"
    "v_mfma_f32_16x16x32_bf16 v[136:139], a[184:187], v[104:107], v[136:139]\n\t"
    "ds_read_b64_tr_b16 a[148:149], v239 offset:53760\n\t"
    "v_cvt_pk_bf16_f32 v60, v180, v181\n\t"
    "v_mfma_f32_16x16x32_bf16 v[136:139], a[188:191], v[108:111], v[136:139]\n\t"
    "ds_read_b64_tr_b16 a[150:151], v239 offset:55808\n\t"
    "v_cvt_pk_bf16_f32 v61, v182, v183\n\t"
    "v_mfma_f32_16x16x32_bf16 v[140:143], a[176:179], v[112:115], v[140:143]\n\t"
    "ds_read_b64_tr_b16 a[152:153], v239 offset:57856\n\t"
    "v_cvt_pk_bf16_f32 v62, v188, v189\n\t"
    "v_mfma_f32_16x16x32_bf16 v[140:143], a[180:183], v[116:119], v[140:143]\n\t"
    "ds_read_b64_tr_b16 a[154:155], v239 offset:59904\n\t"
    "v_cvt_pk_bf16_f32 v63, v190, v191\n\t"
    "s_mov_b32 s47, 25600\n\t"
    "v_mfma_f32_16x16x32_bf16 v[140:143], a[184:187], v[120:123], v[140:143]\n\t"
    "ds_read_b64_tr_b16 a[156:157], v239 offset:61952\n\t"
    "buffer_store_dwordx4 v[60:63], %[poff], %[srdP], s47 offen sc1\n\t"
    "v_mfma_f32_16x16x32_bf16 v[140:143], a[188:191], v[124:127], v[140:143]\n\t"
    "ds_read_b64_tr_b16 a[158:159], v239 offset:64000\n\t"
    "s_waitcnt lgkmcnt(8)\n\t"
    "v_mfma_f32_16x16x32_bf16 v[144:147], a[128:131], v[64:67], 0\n\t"
    "ds_read_b64_tr_b16 a[160:161], v239 offset:33536\n\t"
    "s_add_u32 m0, s49, 135168\n\t"
    "s_mov_b32 s48, 37888\n\t"
    "v_mfma_f32_16x16x32_bf16 v[144:147], a[132:135], v[68:71], v[144:147]\n\t"
    "ds_read_b64_tr_b16 a[162:163], v239 offset:35584\n\t"
    "buffer_load_dwordx4 %[osrc0], %[srdWo], s48 offen offset:0 lds\n\t"
    "v_mfma_f32_16x16x32_bf16 v[144:147], a[136:139], v[72:75], v[144:147]\n\t"
    "ds_read_b64_tr_b16 a[164:165], v239 offset:37632\n\t"
    "v_mfma_f32_16x16x32_bf16 v[144:147], a[140:143], v[76:79], v[144:147]\n\t"
    "ds_read_b64_tr_b16 a[166:167], v239 offset:39680\n\t"
    "v_mfma_f32_16x16x32_bf16 v[148:151], a[128:131], v[80:83], 0\n\t"
    "ds_read_b64_tr_b16 a[168:169], v239 offset:41728\n\t"
    "v_mfma_f32_16x16x32_bf16 v[148:151], a[132:135], v[84:87], v[148:151]\n\t"
    "ds_read_b64_tr_b16 a[170:171], v239 offset:43776\n\t"
    "v_mfma_f32_16x16x32_bf16 v[148:151], a[136:139], v[88:91], v[148:151]\n\t"
    "ds_read_b64_tr_b16 a[172:173], v239 offset:45824\n\t"
    "v_mfma_f32_16x16x32_bf16 v[148:151], a[140:143], v[92:95], v[148:151]\n\t"
    "ds_read_b64_tr_b16 a[174:175], v239 offset:47872\n\t"
    "s_waitcnt lgkmcnt(8)\n\t"
    "v_mfma_f32_16x16x32_bf16 v[144:147], a[144:147], v[96:99], v[144:147]\n\t"
    "ds_read_b64_tr_b16 a[176:177], v239 offset:49920\n\t"
    "v_mfma_f32_16x16x32_bf16 v[144:147], a[148:151], v[100:103], v[144:147]\n\t"
    "ds_read_b64_tr_b16 a[178:179], v239 offset:51968\n\t"
    "buffer_load_dwordx4 v236, %[srdWo], s48 offen offset:1024 lds\n\t"
    "v_mfma_f32_16x16x32_bf16 v[144:147], a[152:155], v[104:107], v[144:147]\n\t"
    "ds_read_b64_tr_b16 a[180:181], v239 offset:54016\n\t"
    "v_mfma_f32_16x16x32_bf16 v[144:147], a[156:159], v[108:111], v[144:147]\n\t"
    "ds_read_b64_tr_b16 a[182:183], v239 offset:56064\n\t"
    "v_mfma_f32_16x16x32_bf16 v[148:151], a[144:147], v[112:115], v[148:151]\n\t"
    "ds_read_b64_tr_b16 a[184:185], v239 offset:58112\n\t"
    "v_mfma_f32_16x16x32_bf16 v[148:151], a[148:151], v[116:119], v[148:151]\n\t"
    "ds_read_b64_tr_b16 a[186:187], v239 offset:60160\n\t"
    "v_mfma_f32_16x16x32_bf16 v[148:151], a[152:155], v[120:123], v[148:151]\n\t"
    "ds_read_b64_tr_b16 a[188:189], v239 offset:62208\n\t"
    "v_mfma_f32_16x16x32_bf16 v[148:151], a[156:159], v[124:127], v[148:151]\n\t"
    "ds_read_b64_tr_b16 a[190:191], v239 offset:64256\n\t"
    "s_waitcnt vmcnt(34)\n\t"
    "s_waitcnt lgkmcnt(0)\n\t"
    "s_barrier\n\t"
    "s_waitcnt lgkmcnt(8)\n\t"
    "v_mfma_f32_16x16x32_bf16 v[152:155], a[160:163], v[64:67], 0\n\t"
    "ds_read_b64_tr_b16 a[128:129], %[ob0] offset:0\n\t"
    "v_mfma_f32_16x16x32_bf16 v[152:155], a[164:167], v[68:71], v[152:155]\n\t"
    "ds_read_b64_tr_b16 a[130:131], %[ob0] offset:2048\n\t"
    "buffer_load_dwordx4 v237, %[srdWo], s48 offen offset:2048 lds\n\t"
    "v_mfma_f32_16x16x32_bf16 v[152:155], a[168:171], v[72:75], v[152:155]\n\t"
    "ds_read_b64_tr_b16 a[132:133], %[ob0] offset:4096\n\t"
    "v_mfma_f32_16x16x32_bf16 v[152:155], a[172:175], v[76:79], v[152:155]\n\t"
    "ds_read_b64_tr_b16 a[134:135], %[ob0] offset:6144\n\t"
    "v_mfma_f32_16x16x32_bf16 v[156:159], a[160:163], v[80:83], 0\n\t"
    "ds_read_b64_tr_b16 a[136:137], %[ob0] offset:8192\n\t"
    "v_mfma_f32_16x16x32_bf16 v[156:159], a[164:167], v[84:87], v[156:159]\n\t"
    "ds_read_b64_tr_b16 a[138:139], %[ob0] offset:10240\n\t"
    "v_mfma_f32_16x16x32_bf16 v[156:159], a[168:171], v[88:91], v[156:159]\n\t"
    "ds_read_b64_tr_b16 a[140:141], %[ob0] offset:12288\n\t"
    "v_mfma_f32_16x16x32_bf16 v[156:159], a[172:175], v[92:95], v[156:159]\n\t"
    "ds_read_b64_tr_b16 a[142:143], %[ob0] offset:14336\n\t"
    "s_waitcnt lgkmcnt(8)\n\t"
    "v_mfma_f32_16x16x32_bf16 v[152:155], a[176:179], v[96:99], v[152:155]\n\t"
    "ds_read_b64_tr_b16 a[144:145], %[ob0] offset:16384\n\t"
    "v_mfma_f32_16x16x32_bf16 v[152:155], a[180:183], v[100:103], v[152:155]\n\t"
    "ds_read_b64_tr_b16 a[146:147], %[ob0] offset:18432\n\t"
    "buffer_load_dwordx4 v238, %[srdWo], s48 offen offset:3072 lds\n\t"
    "v_mfma_f32_16x16x32_bf16 v[152:155], a[184:187], v[104:107], v[152:155]\n\t"
    "ds_read_b64_tr_b16 a[148:149], %[ob0] offset:20480\n\t"
    "v_mfma_f32_16x16x32_bf16 v[152:155], a[188:191], v[108:111], v[152:155]\n\t"
    "ds_read_b64_tr_b16 a[150:151], %[ob0] offset:22528\n\t"
    "v_mfma_f32_16x16x32_bf16 v[156:159], a[176:179], v[112:115], v[156:159]\n\t"
    "ds_read_b64_tr_b16 a[152:153], %[ob0] offset:24576\n\t"
    "v_mfma_f32_16x16x32_bf16 v[156:159], a[180:183], v[116:119], v[156:159]\n\t"
    "ds_read_b64_tr_b16 a[154:155], %[ob0] offset:26624\n\t"
    "v_mfma_f32_16x16x32_bf16 v[156:159], a[184:187], v[120:123], v[156:159]\n\t"
    "ds_read_b64_tr_b16 a[156:157], %[ob0] offset:28672\n\t"
    "v_mfma_f32_16x16x32_bf16 v[156:159], a[188:191], v[124:127], v[156:159]\n\t"
    "ds_read_b64_tr_b16 a[158:159], %[ob0] offset:30720\n\t"
    "s_waitcnt lgkmcnt(8)\n\t"
    "v_mfma_f32_16x16x32_bf16 v[160:163], a[128:131], v[64:67], 0\n\t"
    "ds_read_b64_tr_b16 a[160:161], %[ob0] offset:256\n\t"
    "s_add_u32 m0, s50, 98304\n\t"
    "s_mov_b32 s45, 0\n\t"
    "v_mfma_f32_16x16x32_bf16 v[160:163], a[132:135], v[68:71], v[160:163]\n\t"
    "ds_read_b64_tr_b16 a[162:163], %[ob0] offset:2304\n\t"
    "buffer_load_dwordx4 %[dsrc], %[srdDn], s45 offen lds\n\t"
    "v_mfma_f32_16x16x32_bf16 v[160:163], a[136:139], v[72:75], v[160:163]\n\t"
    "ds_read_b64_tr_b16 a[164:165], %[ob0] offset:4352\n\t"
    "v_cvt_pk_bf16_f32 v32, v128, v129\n\t"
    "v_mfma_f32_16x16x32_bf16 v[160:163], a[140:143], v[76:79], v[160:163]\n\t"
    "ds_read_b64_tr_b16 a[166:167], %[ob0] offset:6400\n\t"
    "v_cvt_pk_bf16_f32 v33, v130, v131\n\t"
    "v_mfma_f32_16x16x32_bf16 v[164:167], a[128:131], v[80:83], 0\n\t"
    "ds_read_b64_tr_b16 a[168:169], %[ob0] offset:8448\n\t"
    "v_cvt_pk_bf16_f32 v34, v136, v137\n\t"
    "v_mfma_f32_16x16x32_bf16 v[164:167], a[132:135], v[84:87], v[164:167]\n\t"
    "ds_read_b64_tr_b16 a[170:171], %[ob0] offset:10496\n\t"
    "v_cvt_pk_bf16_f32 v35, v138, v139\n\t"
    "s_mov_b32 s47, 8192\n\t"
    "v_mfma_f32_16x16x32_bf16 v[164:167], a[136:139], v[88:91], v[164:167]\n\t"
    "ds_read_b64_tr_b16 a[172:173], %[ob0] offset:12544\n\t"
    "buffer_store_dwordx4 v[32:35], %[poff], %[srdP], s47 offen sc1\n\t"
    "v_mfma_f32_16x16x32_bf16 v[164:167], a[140:143], v[92:95], v[164:167]\n\t"
    "ds_read_b64_tr_b16 a[174:175], %[ob0] offset:14592\n\t"
    "s_waitcnt lgkmcnt(8)\n\t"
    "v_mfma_f32_16x16x32_bf16 v[160:163], a[144:147], v[96:99], v[160:163]\n\t"
    "ds_read_b64_tr_b16 a[176:177], %[ob0] offset:16640\n\t"
    "s_add_u32 m0, s50, 102400\n\t"
    "s_mov_b32 s45, 128\n\t"
    "v_mfma_f32_16x16x32_bf16 v[160:163], a[148:151], v[100:103], v[160:163]\n\t"
    "ds_read_b64_tr_b16 a[178:179], %[ob0] offset:18688\n\t"
    "buffer_load_dwordx4 %[dsrc], %[srdDn], s45 offen lds\n\t"
    "v_mfma_f32_16x16x32_bf16 v[160:163], a[152:155], v[104:107], v[160:163]\n\t"
    "ds_read_b64_tr_b16 a[180:181], %[ob0] offset:20736\n\t"
    "v_cvt_pk_bf16_f32 v36, v132, v133\n\t"
    "v_mfma_f32_16x16x32_bf16 v[160:163], a[156:159], v[108:111], v[160:163]\n\t"
    "ds_read_b64_tr_b16 a[182:183], %[ob0] offset:22784\n\t"
    "v_cvt_pk_bf16_f32 v37, v134, v135\n\t"
    "v_mfma_f32_16x16x32_bf16 v[164:167], a[144:147], v[112:115], v[164:167]\n\t"
    "ds_read_b64_tr_b16 a[184:185], %[ob0] offset:24832\n\t"
    "v_cvt_pk_bf16_f32 v38, v140, v141\n\t"
    "v_mfma_f32_16x16x32_bf16 v[164:167], a[148:151], v[116:119], v[164:167]\n\t"
    "ds_read_b64_tr_b16 a[186:187], %[ob0] offset:26880\n\t"
    "v_cvt_pk_bf16_f32 v39, v142, v143\n\t"
    "s_mov_b32 s47, 26624\n\t"
    "v_mfma_f32_16x16x32_bf16 v[164:167], a[152:155], v[120:123], v[164:167]\n\t"
    "ds_read_b64_tr_b16 a[188:189], %[ob0] offset:28928\n\t"
    "buffer_store_dwordx4 v[36:39], %[poff], %[srdP], s47 offen sc1\n\t"
    "v_mfma_f32_16x16x32_bf16 v[164:167], a[156:159], v[124:127], v[164:167]\n\t"
    "ds_read_b64_tr_b16 a[190:191], %[ob0] offset:30976\n\t"
    "s_waitcnt lgkmcnt(8)\n\t"
    "v_mfma_f32_16x16x32_bf16 v[168:171], a[160:163], v[64:67], 0\n\t"
    "ds_read_b64_tr_b16 a[128:129], %[ob0] offset:512\n\t"
    "s_add_u32 m0, s50, 106496\n\t"
    "s_mov_b32 s45, 256\n\t"
    "v_mfma_f32_16x16x32_bf16 v[168:171], a[164:167], v[68:71], v[168:171]\n\t"
    "ds_read_b64_tr_b16 a[130:131], %[ob0] offset:2560\n\t"
    "buffer_load_dwordx4 %[dsrc], %[srdDn], s45 offen lds\n\t"
    "v_mfma_f32_16x16x32_bf16 v[168:171], a[168:171], v[72:75], v[168:171]\n\t"
    "ds_read_b64_tr_b16 a[132:133], %[ob0] offset:4608\n\t"
    "v_cvt_pk_bf16_f32 v40, v144, v145\n\t"
    "v_mfma_f32_16x16x32_bf16 v[168:171], a[172:175], v[76:79], v[168:171]\n\t"
    "ds_read_b64_tr_b16 a[134:135], %[ob0] offset:6656\n\t"
    "v_cvt_pk_bf16_f32 v41, v146, v147\n\t"
    "v_mfma_f32_16x16x32_bf16 v[172:175], a[160:163], v[80:83], 0\n\t"
    "ds_read_b64_tr_b16 a[136:137], %[ob0] offset:8704\n\t"
    "v_cvt_pk_bf16_f32 v42, v152, v153\n\t"
    "v_mfma_f32_16x16x32_bf16 v[172:175], a[164:167], v[84:87], v[172:175]\n\t"
    "ds_read_b64_tr_b16 a[138:139], %[ob0] offset:10752\n\t"
    "v_cvt_pk_bf16_f32 v43, v154, v155\n\t"
    "s_mov_b32 s47, 9216\n\t"
    "v_mfma_f32_16x16x32_bf16 v[172:175], a[168:171], v[88:91], v[172:175]\n\t"
    "ds_read_b64_tr_b16 a[140:141], %[ob0] offset:12800\n\t"
    "buffer_store_dwordx4 v[40:43], %[poff], %[srdP], s47 offen sc1\n\t"
    "v_mfma_f32_16x16x32_bf16 v[172:175], a[172:175], v[92:95], v[172:175]\n\t"
    "ds_read_b64_tr_b16 a[142:143], %[ob0] offset:14848\n\t"
    "s_waitcnt lgkmcnt(8)\n\t"
    "v_mfma_f32_16x16x32_bf16 v[168:171], a[176:179], v[96:99], v[168:171]\n\t"
    "ds_read_b64_tr_b16 a[144:145], %[ob0] offset:16896\n\t"
    "s_add_u32 m0, s50, 110592\n\t"
    "s_mov_b32 s45, 384\n\t"
    "v_mfma_f32_16x16x32_bf16 v[168:171], a[180:183], v[100:103], v[168:171]\n\t"
    "ds_read_b64_tr_b16 a[146:147], %[ob0] offset:18944\n\t"
    "buffer_load_dwordx4 %[dsrc], %[srdDn], s45 offen lds\n\t"
    "v_mfma_f32_16x16x32_bf16 v[168:171], a[184:187], v[104:107], v[168:171]\n\t"
    "ds_read_b64_tr_b16 a[148:149], %[ob0] offset:20992\n\t"
    "v_cvt_pk_bf16_f32 v44, v148, v149\n\t"
    "v_mfma_f32_16x16x32_bf16 v[168:171], a[188:191], v[108:111], v[168:171]\n\t"
    "ds_read_b64_tr_b16 a[150:151], %[ob0] offset:23040\n\t"
    "v_cvt_pk_bf16_f32 v45, v150, v151\n\t"
    "v_mfma_f32_16x16x32_bf16 v[172:175], a[176:179], v[112:115], v[172:175]\n\t"
    "ds_read_b64_tr_b16 a[152:153], %[ob0] offset:25088\n\t"
    "v_cvt_pk_bf16_f32 v46, v156, v157\n\t"
    "v_mfma_f32_16x16x32_bf16 v[172:175], a[180:183], v[116:119], v[172:175]\n\t"
    "ds_read_b64_tr_b16 a[154:155], %[ob0] offset:27136\n\t"
    "v_cvt_pk_bf16_f32 v47, v158, v159\n\t"
    "s_mov_b32 s47, 27648\n\t"
    "v_mfma_f32_16x16x32_bf16 v[172:175], a[184:187], v[120:123], v[172:175]\n\t"
    "ds_read_b64_tr_b16 a[156:157], %[ob0] offset:29184\n\t"
    "buffer_store_dwordx4 v[44:47], %[poff], %[srdP], s47 offen sc1\n\t"
    "v_mfma_f32_16x16x32_bf16 v[172:175], a[188:191], v[124:127], v[172:175]\n\t"
    "ds_read_b64_tr_b16 a[158:159], %[ob0] offset:31232\n\t"
    "s_waitcnt lgkmcnt(8)\n\t"
    "v_mfma_f32_16x16x32_bf16 v[176:179], a[128:131], v[64:67], 0\n\t"
    "ds_read_b64_tr_b16 a[160:161], %[ob0] offset:768\n\t"
    "s_add_u32 m0, s50, 114688\n\t"
    "s_mov_b32 s45, 512\n\t"
    "v_mfma_f32_16x16x32_bf16 v[176:179], a[132:135], v[68:71], v[176:179]\n\t"
    "ds_read_b64_tr_b16 a[162:163], %[ob0] offset:2816\n\t"
    "buffer_load_dwordx4 %[dsrc], %[srdDn], s45 offen lds\n\t"
    "v_mfma_f32_16x16x32_bf16 v[176:179], a[136:139], v[72:75], v[176:179]\n\t"
    "ds_read_b64_tr_b16 a[164:165], %[ob0] offset:4864\n\t"
    "v_mfma_f32_16x16x32_bf16 v[176:179], a[140:143], v[76:79], v[176:179]\n\t"
    "ds_read_b64_tr_b16 a[166:167], %[ob0] offset:6912\n\t"
    "v_mfma_f32_16x16x32_bf16 v[180:183], a[128:131], v[80:83], 0\n\t"
    "ds_read_b64_tr_b16 a[168:169], %[ob0] offset:8960\n\t"
    "v_mfma_f32_16x16x32_bf16 v[180:183], a[132:135], v[84:87], v[180:183]\n\t"
    "ds_read_b64_tr_b16 a[170:171], %[ob0] offset:11008\n\t"
    "v_mfma_f32_16x16x32_bf16 v[180:183], a[136:139], v[88:91], v[180:183]\n\t"
    "ds_read_b64_tr_b16 a[172:173], %[ob0] offset:13056\n\t"
    "v_mfma_f32_16x16x32_bf16 v[180:183], a[140:143], v[92:95], v[180:183]\n\t"
    "ds_read_b64_tr_b16 a[174:175], %[ob0] offset:15104\n\t"
    "s_waitcnt lgkmcnt(8)\n\t"
    "v_mfma_f32_16x16x32_bf16 v[176:179], a[144:147], v[96:99], v[176:179]\n\t"
    "ds_read_b64_tr_b16 a[176:177], %[ob0] offset:17152\n\t"
    "s_add_u32 m0, s50, 118784\n\t"
    "s_mov_b32 s45, 640\n\t"
    "v_mfma_f32_16x16x32_bf16 v[176:179], a[148:151], v[100:103], v[176:179]\n\t"
    "ds_read_b64_tr_b16 a[178:179], %[ob0] offset:19200\n\t"
    "buffer_load_dwordx4 %[dsrc], %[srdDn], s45 offen lds\n\t"
    "v_mfma_f32_16x16x32_bf16 v[176:179], a[152:155], v[104:107], v[176:179]\n\t"
    "ds_read_b64_tr_b16 a[180:181], %[ob0] offset:21248\n\t"
    "v_mfma_f32_16x16x32_bf16 v[176:179], a[156:159], v[108:111], v[176:179]\n\t"
    "ds_read_b64_tr_b16 a[182:183], %[ob0] offset:23296\n\t"
    "v_mfma_f32_16x16x32_bf16 v[180:183], a[144:147], v[112:115], v[180:183]\n\t"
    "ds_read_b64_tr_b16 a[184:185], %[ob0] offset:25344\n\t"
    "v_mfma_f32_16x16x32_bf16 v[180:183], a[148:151], v[116:119], v[180:183]\n\t"
    "ds_read_b64_tr_b16 a[186:187], %[ob0] offset:27392\n\t"
    "v_mfma_f32_16x16x32_bf16 v[180:183], a[152:155], v[120:123], v[180:183]\n\t"
    "ds_read_b64_tr_b16 a[188:189], %[ob0] offset:29440\n\t"
    "v_mfma_f32_16x16x32_bf16 v[180:183], a[156:159], v[124:127], v[180:183]\n\t"
    "ds_read_b64_tr_b16 a[190:191], %[ob0] offset:31488\n\t"
    "s_waitcnt vmcnt(34)\n\t"
    "s_waitcnt lgkmcnt(0)\n\t"
    "s_barrier\n\t"
    "s_waitcnt lgkmcnt(8)\n\t"
    "v_mfma_f32_16x16x32_bf16 v[184:187], a[160:163], v[64:67], 0\n\t"
    "ds_read_b64_tr_b16 a[128:129], %[ob0] offset:32768\n\t"
    "s_add_u32 m0, s50, 122880\n\t"
    "s_mov_b32 s45, 768\n\t"
    "v_mfma_f32_16x16x32_bf16 v[184:187], a[164:167], v[68:71], v[184:187]\n\t"
    "ds_read_b64_tr_b16 a[130:131], %[ob0] offset:34816\n\t"
    "buffer_load_dwordx4 %[dsrc], %[srdDn], s45 offen lds\n\t"
    "v_mfma_f32_16x16x32_bf16 v[184:187], a[168:171], v[72:75], v[184:187]\n\t"
    "ds_read_b64_tr_b16 a[132:133], %[ob0] offset:36864\n\t"
    "v_mfma_f32_16x16x32_bf16 v[184:187], a[172:175], v[76:79], v[184:187]\n\t"
    "ds_read_b64_tr_b16 a[134:135], %[ob0] offset:38912\n\t"
    "v_mfma_f32_16x16x32_bf16 v[188:191], a[160:163], v[80:83], 0\n\t"
    "ds_read_b64_tr_b16 a[136:137], %[ob0] offset:40960\n\t"
    "v_mfma_f32_16x16x32_bf16 v[188:191], a[164:167], v[84:87], v[188:191]\n\t"
    "ds_read_b64_tr_b16 a[138:139], %[ob0] offset:43008\n\t"
    "v_mfma_f32_16x16x32_bf16 v[188:191], a[168:171], v[88:91], v[188:191]\n\t"
    "ds_read_b64_tr_b16 a[140:141], %[ob0] offset:45056\n\t"
    "v_mfma_f32_16x16x32_bf16 v[188:191], a[172:175], v[92:95], v[188:191]\n\t"
    "ds_read_b64_tr_b16 a[142:143], %[ob0] offset:47104\n\t"
    "s_waitcnt lgkmcnt(8)\n\t"
    "v_mfma_f32_16x16x32_bf16 v[184:187], a[176:179], v[96:99], v[184:187]\n\t"
    "ds_read_b64_tr_b16 a[144:145], %[ob0] offset:49152\n\t"
    "s_add_u32 m0, s50, 126976\n\t"
    "s_mov_b32 s45, 896\n\t"
    "v_mfma_f32_16x16x32_bf16 v[184:187], a[180:183], v[100:103], v[184:187]\n\t"
    "ds_read_b64_tr_b16 a[146:147], %[ob0] offset:51200\n\t"
    "buffer_load_dwordx4 %[dsrc], %[srdDn], s45 offen lds\n\t"
    "v_mfma_f32_16x16x32_bf16 v[184:187], a[184:187], v[104:107], v[184:187]\n\t"
    "ds_read_b64_tr_b16 a[148:149], %[ob0] offset:53248\n\t"
    "v_mfma_f32_16x16x32_bf16 v[184:187], a[188:191], v[108:111], v[184:187]\n\t"
    "ds_read_b64_tr_b16 a[150:151], %[ob0] offset:55296\n\t"
    "v_mfma_f32_16x16x32_bf16 v[188:191], a[176:179], v[112:115], v[188:191]\n\t"
    "ds_read_b64_tr_b16 a[152:153], %[ob0] offset:57344\n\t"
    "v_mfma_f32_16x16x32_bf16 v[188:191], a[180:183], v[116:119], v[188:191]\n\t"
    "ds_read_b64_tr_b16 a[154:155], %[ob0] offset:59392\n\t"
    "v_mfma_f32_16x16x32_bf16 v[188:191], a[184:187], v[120:123], v[188:191]\n\t"
    "ds_read_b64_tr_b16 a[156:157], %[ob0] offset:61440\n\t"
    "v_mfma_f32_16x16x32_bf16 v[188:191], a[188:191], v[124:127], v[188:191]\n\t"
    "ds_read_b64_tr_b16 a[158:159], %[ob0] offset:63488\n\t"
    "s_waitcnt lgkmcnt(8)\n\t"
    "v_mfma_f32_16x16x32_bf16 v[128:131], a[128:131], v[64:67], 0\n\t"
    "ds_read_b64_tr_b16 a[160:161], %[ob0] offset:33024\n\t"
    "s_add_u32 m0, s50, 0\n\t"
    "s_mov_b32 s45, 0\n\t"
    "v_mfma_f32_16x16x32_bf16 v[128:131], a[132:135], v[68:71], v[128:131]\n\t"
    "ds_read_b64_tr_b16 a[162:163], %[ob0] offset:35072\n\t"
    "buffer_load_dwordx4 %[gsrc], %[srdG], s45 offen lds\n\t"
    "v_mfma_f32_16x16x32_bf16 v[128:131], a[136:139], v[72:75], v[128:131]\n\t"
    "ds_read_b64_tr_b16 a[164:165], %[ob0] offset:37120\n\t"
    "v_cvt_pk_bf16_f32 v48, v160, v161\n\t"
    "v_mfma_f32_16x16x32_bf16 v[128:131], a[140:143], v[76:79], v[128:131]\n\t"
    "ds_read_b64_tr_b16 a[166:167], %[ob0] offset:39168\n\t"
    "v_cvt_pk_bf16_f32 v49, v162, v163\n\t"
    "v_mfma_f32_16x16x32_bf16 v[132:135], a[128:131], v[80:83], 0\n\t"
    "ds_read_b64_tr_b16 a[168:169], %[ob0] offset:41216\n\t"
    "v_cvt_pk_bf16_f32 v50, v168, v169\n\t"
    "v_mfma_f32_16x16x32_bf16 v[132:135], a[132:135], v[84:87], v[132:135]\n\t"
    "ds_read_b64_tr_b16 a[170:171], %[ob0] offset:43264\n\t"
    "v_cvt_pk_bf16_f32 v51, v170, v171\n\t"
    "s_mov_b32 s47, 10240\n\t"
    "v_mfma_f32_16x16x32_bf16 v[132:135], a[136:139], v[88:91], v[132:135]\n\t"
    "ds_read_b64_tr_b16 a[172:173], %[ob0] offset:45312\n\t"
    "buffer_store_dwordx4 v[48:51], %[poff], %[srdP], s47 offen sc1\n\t"
    "v_mfma_f32_16x16x32_bf16 v[132:135], a[140:143], v[92:95], v[132:135]\n\t"
    "ds_read_b64_tr_b16 a[174:175], %[ob0] offset:47360\n\t"
    "s_waitcnt lgkmcnt(8)\n\t"
    "v_mfma_f32_16x16x32_bf16 v[128:131], a[144:147], v[96:99], v[128:131]\n\t"
    "ds_read_b64_tr_b16 a[176:177], %[ob0] offset:49408\n\t"
    "s_add_u32 m0, s50, 4096\n\t"
    "s_mov_b32 s45, 294912\n\t"
    "v_mfma_f32_16x16x32_bf16 v[128:131], a[148:151], v[100:103], v[128:131]\n\t"
    "ds_read_b64_tr_b16 a[178:179], %[ob0] offset:51456\n\t"
    "buffer_load_dwordx4 %[gsrc], %[srdG], s45 offen lds\n\t"
    "v_mfma_f32_16x16x32_bf16 v[128:131], a[152:155], v[104:107], v[128:131]\n\t"
    "ds_read_b64_tr_b16 a[180:181], %[ob0] offset:53504\n\t"
    "v_cvt_pk_bf16_f32 v52, v164, v165\n\t"
    "v_mfma_f32_16x16x32_bf16 v[128:131], a[156:159], v[108:111], v[128:131]\n\t"
    "ds_read_b64_tr_b16 a[182:183], %[ob0] offset:55552\n\t"
    "v_cvt_pk_bf16_f32 v53, v166, v167\n\t"
    "v_mfma_f32_16x16x32_bf16 v[132:135], a[144:147], v[112:115], v[132:135]\n\t"
    "ds_read_b64_tr_b16 a[184:185], %[ob0] offset:57600\n\t"
    "v_cvt_pk_bf16_f32 v54, v172, v173\n\t"
    "v_mfma_f32_16x16x32_bf16 v[132:135], a[148:151], v[116:119], v[132:135]\n\t"
    "ds_read_b64_tr_b16 a[186:187], %[ob0] offset:59648\n\t"
    "v_cvt_pk_bf16_f32 v55, v174, v175\n\t"
    "s_mov_b32 s47, 28672\n\t"
    "v_mfma_f32_16x16x32_bf16 v[132:135], a[152:155], v[120:123], v[132:135]\n\t"
    "ds_read_b64_tr_b16 a[188:189], %[ob0] offset:61696\n\t"
    "buffer_store_dwordx4 v[52:55], %[poff], %[srdP], s47 offen sc1\n\t"
    "v_mfma_f32_16x16x32_bf16 v[132:135], a[156:159], v[124:127], v[132:135]\n\t"
    "ds_read_b64_tr_b16 a[190:191], %[ob0] offset:63744\n\t"
    "s_waitcnt lgkmcnt(8)\n\t"
    "v_mfma_f32_16x16x32_bf16 v[136:139], a[160:163], v[64:67], 0\n\t"
    "ds_read_b64_tr_b16 a[128:129], %[ob0] offset:33280\n\t"
    "s_add_u32 m0, s50, 8192\n\t"
    "s_mov_b32 s45, 589824\n\t"
    "v_mfma_f32_16x16x32_bf16 v[136:139], a[164:167], v[68:71], v[136:139]\n\t"
    "ds_read_b64_tr_b16 a[130:131], %[ob0] offset:35328\n\t"
    "buffer_load_dwordx4 %[gsrc], %[srdG], s45 offen lds\n\t"
    "v_mfma_f32_16x16x32_bf16 v[136:139], a[168:171], v[72:75], v[136:139]\n\t"
    "ds_read_b64_tr_b16 a[132:133], %[ob0] offset:37376\n\t"
    "v_cvt_pk_bf16_f32 v56, v176, v177\n\t"
    "v_mfma_f32_16x16x32_bf16 v[136:139], a[172:175], v[76:79], v[136:139]\n\t"
    "ds_read_b64_tr_b16 a[134:135], %[ob0] offset:39424\n\t"
    "v_cvt_pk_bf16_f32 v57, v178, v179\n\t"
    "v_mfma_f32_16x16x32_bf16 v[140:143], a[160:163], v[80:83], 0\n\t"
    "ds_read_b64_tr_b16 a[136:137], %[ob0] offset:41472\n\t"
    "v_cvt_pk_bf16_f32 v58, v184, v185\n\t"
    "v_mfma_f32_16x16x32_bf16 v[140:143], a[164:167], v[84:87], v[140:143]\n\t"
    "ds_read_b64_tr_b16 a[138:139], %[ob0] offset:43520\n\t"
    "v_cvt_pk_bf16_f32 v59, v186, v187\n\t"
    "s_mov_b32 s47, 11264\n\t"
    "v_mfma_f32_16x16x32_bf16 v[140:143], a[168:171], v[88:91], v[140:143]\n\t"
    "ds_read_b64_tr_b16 a[140:141], %[ob0] offset:45568\n\t"
    "buffer_store_dwordx4 v[56:59], %[poff], %[srdP], s47 offen sc1\n\t"
    "v_mfma_f32_16x16x32_bf16 v[140:143], a[172:175], v[92:95], v[140:143]\n\t"
    "ds_read_b64_tr_b16 a[142:143], %[ob0] offset:47616\n\t"
    "s_waitcnt lgkmcnt(8)\n\t"
    "v_mfma_f32_16x16x32_bf16 v[136:139], a[176:179], v[96:99], v[136:139]\n\t"
    "ds_read_b64_tr_b16 a[144:145], %[ob0] offset:49664\n\t"
    "s_add_u32 m0, s50, 12288\n\t"
    "s_mov_b32 s45, 884736\n\t"
    "v_mfma_f32_16x16x32_bf16 v[136:139], a[180:183], v[100:103], v[136:139]\n\t"
    "ds_read_b64_tr_b16 a[146:147], %[ob0] offset:51712\n\t"
    "buffer_load_dwordx4 %[gsrc], %[srdG], s45 offen lds\n\t"
    "v_mfma_f32_16x16x32_bf16 v[136:139], a[184:187], v[104:107], v[136:139]\n\t"
    "ds_read_b64_tr_b16 a[148:149], %[ob0] offset:53760\n\t"
    "v_cvt_pk_bf16_f32 v60, v180, v181\n\t"
    "v_mfma_f32_16x16x32_bf16 v[136:139], a[188:191], v[108:111], v[136:139]\n\t"
    "ds_read_b64_tr_b16 a[150:151], %[ob0] offset:55808\n\t"
    "v_cvt_pk_bf16_f32 v61, v182, v183\n\t"
    "v_mfma_f32_16x16x32_bf16 v[140:143], a[176:179], v[112:115], v[140:143]\n\t"
    "ds_read_b64_tr_b16 a[152:153], %[ob0] offset:57856\n\t"
    "v_cvt_pk_bf16_f32 v62, v188, v189\n\t"
    "v_mfma_f32_16x16x32_bf16 v[140:143], a[180:183], v[116:119], v[140:143]\n\t"
    "ds_read_b64_tr_b16 a[154:155], %[ob0] offset:59904\n\t"
    "v_cvt_pk_bf16_f32 v63, v190, v191\n\t"
    "s_mov_b32 s47, 29696\n\t"
    "v_mfma_f32_16x16x32_bf16 v[140:143], a[184:187], v[120:123], v[140:143]\n\t"
    "ds_read_b64_tr_b16 a[156:157], %[ob0] offset:61952\n\t"
    "buffer_store_dwordx4 v[60:63], %[poff], %[srdP], s47 offen sc1\n\t"
    "v_mfma_f32_16x16x32_bf16 v[140:143], a[188:191], v[124:127], v[140:143]\n\t"
    "ds_read_b64_tr_b16 a[158:159], %[ob0] offset:64000\n\t"
    "s_waitcnt lgkmcnt(8)\n\t"
    "v_mfma_f32_16x16x32_bf16 v[144:147], a[128:131], v[64:67], 0\n\t"
    "ds_read_b64_tr_b16 a[160:161], %[ob0] offset:33536\n\t"
    "s_add_u32 m0, s50, 16384\n\t"
    "s_mov_b32 s45, 1179648\n\t"
    "v_mfma_f32_16x16x32_bf16 v[144:147], a[132:135], v[68:71], v[144:147]\n\t"
    "ds_read_b64_tr_b16 a[162:163], %[ob0] offset:35584\n\t"
    "buffer_load_dwordx4 %[gsrc], %[srdG], s45 offen lds\n\t"
    "v_mfma_f32_16x16x32_bf16 v[144:147], a[136:139], v[72:75], v[144:147]\n\t"
    "ds_read_b64_tr_b16 a[164:165], %[ob0] offset:37632\n\t"
    "v_mfma_f32_16x16x32_bf16 v[144:147], a[140:143], v[76:79], v[144:147]\n\t"
    "ds_read_b64_tr_b16 a[166:167], %[ob0] offset:39680\n\t"
    "v_mfma_f32_16x16x32_bf16 v[148:151], a[128:131], v[80:83], 0\n\t"
    "ds_read_b64_tr_b16 a[168:169], %[ob0] offset:41728\n\t"
    "v_mfma_f32_16x16x32_bf16 v[148:151], a[132:135], v[84:87], v[148:151]\n\t"
    "ds_read_b64_tr_b16 a[170:171], %[ob0] offset:43776\n\t"
    "v_mfma_f32_16x16x32_bf16 v[148:151], a[136:139], v[88:91], v[148:151]\n\t"
    "ds_read_b64_tr_b16 a[172:173], %[ob0] offset:45824\n\t"
    "v_mfma_f32_16x16x32_bf16 v[148:151], a[140:143], v[92:95], v[148:151]\n\t"
    "ds_read_b64_tr_b16 a[174:175], %[ob0] offset:47872\n\t"
    "s_waitcnt lgkmcnt(8)\n\t"
    "v_mfma_f32_16x16x32_bf16 v[144:147], a[144:147], v[96:99], v[144:147]\n\t"
    "ds_read_b64_tr_b16 a[176:177], %[ob0] offset:49920\n\t"
    "s_add_u32 m0, s50, 20480\n\t"
    "s_mov_b32 s45, 1474560\n\t"
    "v_mfma_f32_16x16x32_bf16 v[144:147], a[148:151], v[100:103], v[144:147]\n\t"
    "ds_read_b64_tr_b16 a[178:179], %[ob0] offset:51968\n\t"
    "buffer_load_dwordx4 %[gsrc], %[srdG], s45 offen lds\n\t"
    "v_mfma_f32_16x16x32_bf16 v[144:147], a[152:155], v[104:107], v[144:147]\n\t"
    "ds_read_b64_tr_b16 a[180:181], %[ob0] offset:54016\n\t"
    "v_mfma_f32_16x16x32_bf16 v[144:147], a[156:159], v[108:111], v[144:147]\n\t"
    "ds_read_b64_tr_b16 a[182:183], %[ob0] offset:56064\n\t"
    "v_mfma_f32_16x16x32_bf16 v[148:151], a[144:147], v[112:115], v[148:151]\n\t"
    "ds_read_b64_tr_b16 a[184:185], %[ob0] offset:58112\n\t"
    "v_mfma_f32_16x16x32_bf16 v[148:151], a[148:151], v[116:119], v[148:151]\n\t"
    "ds_read_b64_tr_b16 a[186:187], %[ob0] offset:60160\n\t"
    "v_mfma_f32_16x16x32_bf16 v[148:151], a[152:155], v[120:123], v[148:151]\n\t"
    "ds_read_b64_tr_b16 a[188:189], %[ob0] offset:62208\n\t"
    "v_mfma_f32_16x16x32_bf16 v[148:151], a[156:159], v[124:127], v[148:151]\n\t"
    "ds_read_b64_tr_b16 a[190:191], %[ob0] offset:64256\n\t"
    "s_waitcnt vmcnt(34)\n\t"
    "s_waitcnt lgkmcnt(0)\n\t"
    "s_barrier\n\t"
    "s_waitcnt lgkmcnt(8)\n\t"
    "v_mfma_f32_16x16x32_bf16 v[152:155], a[160:163], v[64:67], 0\n\t"
    "ds_read_b64_tr_b16 a[128:129], v239 offset:0\n\t"
    "s_add_u32 m0, s50, 24576\n\t"
    "s_mov_b32 s45, 1769472\n\t"
    "v_mfma_f32_16x16x32_bf16 v[152:155], a[164:167], v[68:71], v[152:155]\n\t"
    "ds_read_b64_tr_b16 a[130:131], v239 offset:2048\n\t"
    "buffer_load_dwordx4 %[gsrc], %[srdG], s45 offen lds\n\t"
    "v_mfma_f32_16x16x32_bf16 v[152:155], a[168:171], v[72:75], v[152:155]\n\t"
    "ds_read_b64_tr_b16 a[132:133], v239 offset:4096\n\t"
    "v_mfma_f32_16x16x32_bf16 v[152:155], a[172:175], v[76:79], v[152:155]\n\t"
    "ds_read_b64_tr_b16 a[134:135], v239 offset:6144\n\t"
    "v_mfma_f32_16x16x32_bf16 v[156:159], a[160:163], v[80:83], 0\n\t"
    "ds_read_b64_tr_b16 a[136:137], v239 offset:8192\n\t"
    "v_mfma_f32_16x16x32_bf16 v[156:159], a[164:167], v[84:87], v[156:159]\n\t"
    "ds_read_b64_tr_b16 a[138:139], v239 offset:10240\n\t"
    "v_mfma_f32_16x16x32_bf16 v[156:159], a[168:171], v[88:91], v[156:159]\n\t"
    "ds_read_b64_tr_b16 a[140:141], v239 offset:12288\n\t"
    "v_mfma_f32_16x16x32_bf16 v[156:159], a[172:175], v[92:95], v[156:159]\n\t"
    "ds_read_b64_tr_b16 a[142:143], v239 offset:14336\n\t"
    "s_waitcnt lgkmcnt(8)\n\t"
    "v_mfma_f32_16x16x32_bf16 v[152:155], a[176:179], v[96:99], v[152:155]\n\t"
    "ds_read_b64_tr_b16 a[144:145], v239 offset:16384\n\t"
    "s_add_u32 m0, s50, 28672\n\t"
    "s_mov_b32 s45, 2064384\n\t"
    "v_mfma_f32_16x16x32_bf16 v[152:155], a[180:183], v[100:103], v[152:155]\n\t"
    "ds_read_b64_tr_b16 a[146:147], v239 offset:18432\n\t"
    "buffer_load_dwordx4 %[gsrc], %[srdG], s45 offen lds\n\t"
    "v_mfma_f32_16x16x32_bf16 v[152:155], a[184:187], v[104:107], v[152:155]\n\t"
    "ds_read_b64_tr_b16 a[148:149], v239 offset:20480\n\t"
    "v_mfma_f32_16x16x32_bf16 v[152:155], a[188:191], v[108:111], v[152:155]\n\t"
    "ds_read_b64_tr_b16 a[150:151], v239 offset:22528\n\t"
    "v_mfma_f32_16x16x32_bf16 v[156:159], a[176:179], v[112:115], v[156:159]\n\t"
    "ds_read_b64_tr_b16 a[152:153], v239 offset:24576\n\t"
    "v_mfma_f32_16x16x32_bf16 v[156:159], a[180:183], v[116:119], v[156:159]\n\t"
    "ds_read_b64_tr_b16 a[154:155], v239 offset:26624\n\t"
    "v_mfma_f32_16x16x32_bf16 v[156:159], a[184:187], v[120:123], v[156:159]\n\t"
    "ds_read_b64_tr_b16 a[156:157], v239 offset:28672\n\t"
    "v_mfma_f32_16x16x32_bf16 v[156:159], a[188:191], v[124:127], v[156:159]\n\t"
    "ds_read_b64_tr_b16 a[158:159], v239 offset:30720\n\t"
    "s_waitcnt lgkmcnt(8)\n\t"
    "v_mfma_f32_16x16x32_bf16 v[160:163], a[128:131], v[64:67], 0\n\t"
    "ds_read_b64_tr_b16 a[160:161], v239 offset:256\n\t"
    "s_add_u32 m0, s50, 32768\n\t"
    "s_mov_b32 s45, 2359296\n\t"
    "v_mfma_f32_16x16x32_bf16 v[160:163], a[132:135], v[68:71], v[160:163]\n\t"
    "ds_read_b64_tr_b16 a[162:163], v239 offset:2304\n\t"
    "buffer_load_dwordx4 %[gsrc], %[srdG], s45 offen lds\n\t"
    "v_mfma_f32_16x16x32_bf16 v[160:163], a[136:139], v[72:75], v[160:163]\n\t"
    "ds_read_b64_tr_b16 a[164:165], v239 offset:4352\n\t"
    "v_cvt_pk_bf16_f32 v32, v128, v129\n\t"
    "v_mfma_f32_16x16x32_bf16 v[160:163], a[140:143], v[76:79], v[160:163]\n\t"
    "ds_read_b64_tr_b16 a[166:167], v239 offset:6400\n\t"
    "v_cvt_pk_bf16_f32 v33, v130, v131\n\t"
    "v_mfma_f32_16x16x32_bf16 v[164:167], a[128:131], v[80:83], 0\n\t"
    "ds_read_b64_tr_b16 a[168:169], v239 offset:8448\n\t"
    "v_cvt_pk_bf16_f32 v34, v136, v137\n\t"
    "v_mfma_f32_16x16x32_bf16 v[164:167], a[132:135], v[84:87], v[164:167]\n\t"
    "ds_read_b64_tr_b16 a[170:171], v239 offset:10496\n\t"
    "v_cvt_pk_bf16_f32 v35, v138, v139\n\t"
    "s_mov_b32 s47, 12288\n\t"
    "v_mfma_f32_16x16x32_bf16 v[164:167], a[136:139], v[88:91], v[164:167]\n\t"
    "ds_read_b64_tr_b16 a[172:173], v239 offset:12544\n\t"
    "buffer_store_dwordx4 v[32:35], %[poff], %[srdP], s47 offen sc1\n\t"
    "v_mfma_f32_16x16x32_bf16 v[164:167], a[140:143], v[92:95], v[164:167]\n\t"
    "ds_read_b64_tr_b16 a[174:175], v239 offset:14592\n\t"
    "s_waitcnt lgkmcnt(8)\n\t"
    "v_mfma_f32_16x16x32_bf16 v[160:163], a[144:147], v[96:99], v[160:163]\n\t"
    "ds_read_b64_tr_b16 a[176:177], v239 offset:16640\n\t"
    "s_add_u32 m0, s50, 36864\n\t"
    "s_mov_b32 s45, 2654208\n\t"
    "v_mfma_f32_16x16x32_bf16 v[160:163], a[148:151], v[100:103], v[160:163]\n\t"
    "ds_read_b64_tr_b16 a[178:179], v239 offset:18688\n\t"
    "buffer_load_dwordx4 %[gsrc], %[srdG], s45 offen lds\n\t"
    "v_mfma_f32_16x16x32_bf16 v[160:163], a[152:155], v[104:107], v[160:163]\n\t"
    "ds_read_b64_tr_b16 a[180:181], v239 offset:20736\n\t"
    "v_cvt_pk_bf16_f32 v36, v132, v133\n\t"
    "v_mfma_f32_16x16x32_bf16 v[160:163], a[156:159], v[108:111], v[160:163]\n\t"
    "ds_read_b64_tr_b16 a[182:183], v239 offset:22784\n\t"
    "v_cvt_pk_bf16_f32 v37, v134, v135\n\t"
    "v_mfma_f32_16x16x32_bf16 v[164:167], a[144:147], v[112:115], v[164:167]\n\t"
    "ds_read_b64_tr_b16 a[184:185], v239 offset:24832\n\t"
    "v_cvt_pk_bf16_f32 v38, v140, v141\n\t"
    "v_mfma_f32_16x16x32_bf16 v[164:167], a[148:151], v[116:119], v[164:167]\n\t"
    "ds_read_b64_tr_b16 a[186:187], v239 offset:26880\n\t"
    "v_cvt_pk_bf16_f32 v39, v142, v143\n\t"
    "s_mov_b32 s47, 30720\n\t"
    "v_mfma_f32_16x16x32_bf16 v[164:167], a[152:155], v[120:123], v[164:167]\n\t"
    "ds_read_b64_tr_b16 a[188:189], v239 offset:28928\n\t"
    "buffer_store_dwordx4 v[36:39], %[poff], %[srdP], s47 offen sc1\n\t"
    "v_mfma_f32_16x16x32_bf16 v[164:167], a[156:159], v[124:127], v[164:167]\n\t"
    "ds_read_b64_tr_b16 a[190:191], v239 offset:30976\n\t"
    "s_waitcnt lgkmcnt(8)\n\t"
    "v_mfma_f32_16x16x32_bf16 v[168:171], a[160:163], v[64:67], 0\n\t"
    "ds_read_b64_tr_b16 a[128:129], v239 offset:512\n\t"
    "s_add_u32 m0, s50, 40960\n\t"
    "s_mov_b32 s45, 2949120\n\t"
    "v_mfma_f32_16x16x32_bf16 v[168:171], a[164:167], v[68:71], v[168:171]\n\t"
    "ds_read_b64_tr_b16 a[130:131], v239 offset:2560\n\t"
    "buffer_load_dwordx4 %[gsrc], %[srdG], s45 offen lds\n\t"
    "v_mfma_f32_16x16x32_bf16 v[168:171], a[168:171], v[72:75], v[168:171]\n\t"
    "ds_read_b64_tr_b16 a[132:133], v239 offset:4608\n\t"
    "v_cvt_pk_bf16_f32 v40, v144, v145\n\t"
    "v_mfma_f32_16x16x32_bf16 v[168:171], a[172:175], v[76:79], v[168:171]\n\t"
    "ds_read_b64_tr_b16 a[134:135], v239 offset:6656\n\t"
    "v_cvt_pk_bf16_f32 v41, v146, v147\n\t"
    "v_mfma_f32_16x16x32_bf16 v[172:175], a[160:163], v[80:83], 0\n\t"
    "ds_read_b64_tr_b16 a[136:137], v239 offset:8704\n\t"
    "v_cvt_pk_bf16_f32 v42, v152, v153\n\t"
    "v_mfma_f32_16x16x32_bf16 v[172:175], a[164:167], v[84:87], v[172:175]\n\t"
    "ds_read_b64_tr_b16 a[138:139], v239 offset:10752\n\t"
    "v_cvt_pk_bf16_f32 v43, v154, v155\n\t"
    "s_mov_b32 s47, 13312\n\t"
    "v_mfma_f32_16x16x32_bf16 v[172:175], a[168:171], v[88:91], v[172:175]\n\t"
    "ds_read_b64_tr_b16 a[140:141], v239 offset:12800\n\t"
    "buffer_store_dwordx4 v[40:43], %[poff], %[srdP], s47 offen sc1\n\t"
    "v_mfma_f32_16x16x32_bf16 v[172:175], a[172:175], v[92:95], v[172:175]\n\t"
    "ds_read_b64_tr_b16 a[142:143], v239 offset:14848\n\t"
    "s_waitcnt lgkmcnt(8)\n\t"
    "v_mfma_f32_16x16x32_bf16 v[168:171], a[176:179], v[96:99], v[168:171]\n\t"
    "ds_read_b64_tr_b16 a[144:145], v239 offset:16896\n\t"
    "s_add_u32 m0, s50, 45056\n\t"
    "s_mov_b32 s45, 3244032\n\t"
    "v_mfma_f32_16x16x32_bf16 v[168:171], a[180:183], v[100:103], v[168:171]\n\t"
    "ds_read_b64_tr_b16 a[146:147], v239 offset:18944\n\t"
    "buffer_load_dwordx4 %[gsrc], %[srdG], s45 offen lds\n\t"
    "v_mfma_f32_16x16x32_bf16 v[168:171], a[184:187], v[104:107], v[168:171]\n\t"
    "ds_read_b64_tr_b16 a[148:149], v239 offset:20992\n\t"
    "v_cvt_pk_bf16_f32 v44, v148, v149\n\t"
    "v_mfma_f32_16x16x32_bf16 v[168:171], a[188:191], v[108:111], v[168:171]\n\t"
    "ds_read_b64_tr_b16 a[150:151], v239 offset:23040\n\t"
    "v_cvt_pk_bf16_f32 v45, v150, v151\n\t"
    "v_mfma_f32_16x16x32_bf16 v[172:175], a[176:179], v[112:115], v[172:175]\n\t"
    "ds_read_b64_tr_b16 a[152:153], v239 offset:25088\n\t"
    "v_cvt_pk_bf16_f32 v46, v156, v157\n\t"
    "v_mfma_f32_16x16x32_bf16 v[172:175], a[180:183], v[116:119], v[172:175]\n\t"
    "ds_read_b64_tr_b16 a[154:155], v239 offset:27136\n\t"
    "v_cvt_pk_bf16_f32 v47, v158, v159\n\t"
    "s_mov_b32 s47, 31744\n\t"
    "v_mfma_f32_16x16x32_bf16 v[172:175], a[184:187], v[120:123], v[172:175]\n\t"
    "ds_read_b64_tr_b16 a[156:157], v239 offset:29184\n\t"
    "buffer_store_dwordx4 v[44:47], %[poff], %[srdP], s47 offen sc1\n\t"
    "v_mfma_f32_16x16x32_bf16 v[172:175], a[188:191], v[124:127], v[172:175]\n\t"
    "ds_read_b64_tr_b16 a[158:159], v239 offset:31232\n\t"
    "s_waitcnt lgkmcnt(8)\n\t"
    "v_mfma_f32_16x16x32_bf16 v[176:179], a[128:131], v[64:67], 0\n\t"
    "ds_read_b64_tr_b16 a[160:161], v239 offset:768\n\t"
    "s_add_u32 m0, s50, 49152\n\t"
    "s_mov_b32 s45, 3538944\n\t"
    "v_mfma_f32_16x16x32_bf16 v[176:179], a[132:135], v[68:71], v[176:179]\n\t"
    "ds_read_b64_tr_b16 a[162:163], v239 offset:2816\n\t"
    "buffer_load_dwordx4 %[gsrc], %[srdG], s45 offen lds\n\t"
    "v_mfma_f32_16x16x32_bf16 v[176:179], a[136:139], v[72:75], v[176:179]\n\t"
    "ds_read_b64_tr_b16 a[164:165], v239 offset:4864\n\t"
    "v_mfma_f32_16x16x32_bf16 v[176:179], a[140:143], v[76:79], v[176:179]\n\t"
    "ds_read_b64_tr_b16 a[166:167], v239 offset:6912\n\t"
    "v_mfma_f32_16x16x32_bf16 v[180:183], a[128:131], v[80:83], 0\n\t"
    "ds_read_b64_tr_b16 a[168:169], v239 offset:8960\n\t"
    "v_mfma_f32_16x16x32_bf16 v[180:183], a[132:135], v[84:87], v[180:183]\n\t"
    "ds_read_b64_tr_b16 a[170:171], v239 offset:11008\n\t"
    "v_mfma_f32_16x16x32_bf16 v[180:183], a[136:139], v[88:91], v[180:183]\n\t"
    "ds_read_b64_tr_b16 a[172:173], v239 offset:13056\n\t"
    "v_mfma_f32_16x16x32_bf16 v[180:183], a[140:143], v[92:95], v[180:183]\n\t"
    "ds_read_b64_tr_b16 a[174:175], v239 offset:15104\n\t"
    "s_waitcnt lgkmcnt(8)\n\t"
    "v_mfma_f32_16x16x32_bf16 v[176:179], a[144:147], v[96:99], v[176:179]\n\t"
    "ds_read_b64_tr_b16 a[176:177], v239 offset:17152\n\t"
    "s_add_u32 m0, s50, 53248\n\t"
    "s_mov_b32 s45, 3833856\n\t"
    "v_mfma_f32_16x16x32_bf16 v[176:179], a[148:151], v[100:103], v[176:179]\n\t"
    "ds_read_b64_tr_b16 a[178:179], v239 offset:19200\n\t"
    "buffer_load_dwordx4 %[gsrc], %[srdG], s45 offen lds\n\t"
    "v_mfma_f32_16x16x32_bf16 v[176:179], a[152:155], v[104:107], v[176:179]\n\t"
    "ds_read_b64_tr_b16 a[180:181], v239 offset:21248\n\t"
    "v_mfma_f32_16x16x32_bf16 v[176:179], a[156:159], v[108:111], v[176:179]\n\t"
    "ds_read_b64_tr_b16 a[182:183], v239 offset:23296\n\t"
    "v_mfma_f32_16x16x32_bf16 v[180:183], a[144:147], v[112:115], v[180:183]\n\t"
    "ds_read_b64_tr_b16 a[184:185], v239 offset:25344\n\t"
    "v_mfma_f32_16x16x32_bf16 v[180:183], a[148:151], v[116:119], v[180:183]\n\t"
    "ds_read_b64_tr_b16 a[186:187], v239 offset:27392\n\t"
    "v_mfma_f32_16x16x32_bf16 v[180:183], a[152:155], v[120:123], v[180:183]\n\t"
    "ds_read_b64_tr_b16 a[188:189], v239 offset:29440\n\t"
    "v_mfma_f32_16x16x32_bf16 v[180:183], a[156:159], v[124:127], v[180:183]\n\t"
    "ds_read_b64_tr_b16 a[190:191], v239 offset:31488\n\t"
    "s_waitcnt vmcnt(34)\n\t"
    "s_waitcnt lgkmcnt(0)\n\t"
    "s_barrier\n\t"
    "s_waitcnt lgkmcnt(8)\n\t"
    "v_mfma_f32_16x16x32_bf16 v[184:187], a[160:163], v[64:67], 0\n\t"
    "ds_read_b64_tr_b16 a[128:129], v240 offset:0\n\t"
    "s_add_u32 m0, s50, 57344\n\t"
    "s_mov_b32 s45, 4128768\n\t"
    "v_mfma_f32_16x16x32_bf16 v[184:187], a[164:167], v[68:71], v[184:187]\n\t"
    "ds_read_b64_tr_b16 a[130:131], v240 offset:2048\n\t"
    "buffer_load_dwordx4 %[gsrc], %[srdG], s45 offen lds\n\t"
    "v_mfma_f32_16x16x32_bf16 v[184:187], a[168:171], v[72:75], v[184:187]\n\t"
    "ds_read_b64_tr_b16 a[132:133], v240 offset:4096\n\t"
    "v_mfma_f32_16x16x32_bf16 v[184:187], a[172:175], v[76:79], v[184:187]\n\t"
    "ds_read_b64_tr_b16 a[134:135], v240 offset:6144\n\t"
    "v_mfma_f32_16x16x32_bf16 v[188:191], a[160:163], v[80:83], 0\n\t"
    "ds_read_b64_tr_b16 a[136:137], v240 offset:8192\n\t"
    "v_mfma_f32_16x16x32_bf16 v[188:191], a[164:167], v[84:87], v[188:191]\n\t"
    "ds_read_b64_tr_b16 a[138:139], v240 offset:10240\n\t"
    "v_mfma_f32_16x16x32_bf16 v[188:191], a[168:171], v[88:91], v[188:191]\n\t"
    "ds_read_b64_tr_b16 a[140:141], v240 offset:12288\n\t"
    "v_mfma_f32_16x16x32_bf16 v[188:191], a[172:175], v[92:95], v[188:191]\n\t"
    "ds_read_b64_tr_b16 a[142:143], v240 offset:14336\n\t"
    "s_waitcnt lgkmcnt(8)\n\t"
    "v_mfma_f32_16x16x32_bf16 v[184:187], a[176:179], v[96:99], v[184:187]\n\t"
    "ds_read_b64_tr_b16 a[144:145], v240 offset:16384\n\t"
    "s_add_u32 m0, s50, 61440\n\t"
    "s_mov_b32 s45, 4423680\n\t"
    "v_mfma_f32_16x16x32_bf16 v[184:187], a[180:183], v[100:103], v[184:187]\n\t"
    "ds_read_b64_tr_b16 a[146:147], v240 offset:18432\n\t"
    "buffer_load_dwordx4 %[gsrc], %[srdG], s45 offen lds\n\t"
    "v_mfma_f32_16x16x32_bf16 v[184:187], a[184:187], v[104:107], v[184:187]\n\t"
    "ds_read_b64_tr_b16 a[148:149], v240 offset:20480\n\t"
    "v_mfma_f32_16x16x32_bf16 v[184:187], a[188:191], v[108:111], v[184:187]\n\t"
    "ds_read_b64_tr_b16 a[150:151], v240 offset:22528\n\t"
    "v_mfma_f32_16x16x32_bf16 v[188:191], a[176:179], v[112:115], v[188:191]\n\t"
    "ds_read_b64_tr_b16 a[152:153], v240 offset:24576\n\t"
    "v_mfma_f32_16x16x32_bf16 v[188:191], a[180:183], v[116:119], v[188:191]\n\t"
    "ds_read_b64_tr_b16 a[154:155], v240 offset:26624\n\t"
    "v_mfma_f32_16x16x32_bf16 v[188:191], a[184:187], v[120:123], v[188:191]\n\t"
    "ds_read_b64_tr_b16 a[156:157], v240 offset:28672\n\t"
    "v_mfma_f32_16x16x32_bf16 v[188:191], a[188:191], v[124:127], v[188:191]\n\t"
    "ds_read_b64_tr_b16 a[158:159], v240 offset:30720\n\t"
    "s_waitcnt lgkmcnt(8)\n\t"
    "v_mfma_f32_16x16x32_bf16 v[128:131], a[128:131], v[64:67], 0\n\t"
    "ds_read_b64_tr_b16 a[160:161], v240 offset:256\n\t"
    "s_add_u32 m0, s50, 65536\n\t"
    "s_mov_b32 s45, 4718592\n\t"
    "v_mfma_f32_16x16x32_bf16 v[128:131], a[132:135], v[68:71], v[128:131]\n\t"
    "ds_read_b64_tr_b16 a[162:163], v240 offset:2304\n\t"
    "buffer_load_dwordx4 %[gsrc], %[srdG], s45 offen lds\n\t"
    "v_mfma_f32_16x16x32_bf16 v[128:131], a[136:139], v[72:75], v[128:131]\n\t"
    "ds_read_b64_tr_b16 a[164:165], v240 offset:4352\n\t"
    "v_cvt_pk_bf16_f32 v48, v160, v161\n\t"
    "v_mfma_f32_16x16x32_bf16 v[128:131], a[140:143], v[76:79], v[128:131]\n\t"
    "ds_read_b64_tr_b16 a[166:167], v240 offset:6400\n\t"
    "v_cvt_pk_bf16_f32 v49, v162, v163\n\t"
    "v_mfma_f32_16x16x32_bf16 v[132:135], a[128:131], v[80:83], 0\n\t"
    "ds_read_b64_tr_b16 a[168:169], v240 offset:8448\n\t"
    "v_cvt_pk_bf16_f32 v50, v168, v169\n\t"
    "v_mfma_f32_16x16x32_bf16 v[132:135], a[132:135], v[84:87], v[132:135]\n\t"
    "ds_read_b64_tr_b16 a[170:171], v240 offset:10496\n\t"
    "v_cvt_pk_bf16_f32 v51, v170, v171\n\t"
    "s_mov_b32 s47, 14336\n\t"
    "v_mfma_f32_16x16x32_bf16 v[132:135], a[136:139], v[88:91], v[132:135]\n\t"
    "ds_read_b64_tr_b16 a[172:173], v240 offset:12544\n\t"
    "buffer_store_dwordx4 v[48:51], %[poff], %[srdP], s47 offen sc1\n\t"
    "v_mfma_f32_16x16x32_bf16 v[132:135], a[140:143], v[92:95], v[132:135]\n\t"
    "ds_read_b64_tr_b16 a[174:175], v240 offset:14592\n\t"
    "s_waitcnt lgkmcnt(8)\n\t"
    "v_mfma_f32_16x16x32_bf16 v[128:131], a[144:147], v[96:99], v[128:131]\n\t"
    "ds_read_b64_tr_b16 a[176:177], v240 offset:16640\n\t"
    "s_add_u32 m0, s50, 69632\n\t"
    "s_mov_b32 s45, 5013504\n\t"
    "v_mfma_f32_16x16x32_bf16 v[128:131], a[148:151], v[100:103], v[128:131]\n\t"
    "ds_read_b64_tr_b16 a[178:179], v240 offset:18688\n\t"
    "buffer_load_dwordx4 %[gsrc], %[srdG], s45 offen lds\n\t"
    "v_mfma_f32_16x16x32_bf16 v[128:131], a[152:155], v[104:107], v[128:131]\n\t"
    "ds_read_b64_tr_b16 a[180:181], v240 offset:20736\n\t"
    "v_cvt_pk_bf16_f32 v52, v164, v165\n\t"
    "v_mfma_f32_16x16x32_bf16 v[128:131], a[156:159], v[108:111], v[128:131]\n\t"
    "ds_read_b64_tr_b16 a[182:183], v240 offset:22784\n\t"
    "v_cvt_pk_bf16_f32 v53, v166, v167\n\t"
    "v_mfma_f32_16x16x32_bf16 v[132:135], a[144:147], v[112:115], v[132:135]\n\t"
    "ds_read_b64_tr_b16 a[184:185], v240 offset:24832\n\t"
    "v_cvt_pk_bf16_f32 v54, v172, v173\n\t"
    "v_mfma_f32_16x16x32_bf16 v[132:135], a[148:151], v[116:119], v[132:135]\n\t"
    "ds_read_b64_tr_b16 a[186:187], v240 offset:26880\n\t"
    "v_cvt_pk_bf16_f32 v55, v174, v175\n\t"
    "s_mov_b32 s47, 32768\n\t"
    "v_mfma_f32_16x16x32_bf16 v[132:135], a[152:155], v[120:123], v[132:135]\n\t"
    "ds_read_b64_tr_b16 a[188:189], v240 offset:28928\n\t"
    "buffer_store_dwordx4 v[52:55], %[poff], %[srdP], s47 offen sc1\n\t"
    "v_mfma_f32_16x16x32_bf16 v[132:135], a[156:159], v[124:127], v[132:135]\n\t"
    "ds_read_b64_tr_b16 a[190:191], v240 offset:30976\n\t"
    "s_waitcnt lgkmcnt(8)\n\t"
    "v_mfma_f32_16x16x32_bf16 v[136:139], a[160:163], v[64:67], 0\n\t"
    "ds_read_b64_tr_b16 a[128:129], v240 offset:512\n\t"
    "s_add_u32 m0, s50, 73728\n\t"
    "s_mov_b32 s45, 1024\n\t"
    "v_mfma_f32_16x16x32_bf16 v[136:139], a[164:167], v[68:71], v[136:139]\n\t"
    "ds_read_b64_tr_b16 a[130:131], v240 offset:2560\n\t"
    "buffer_load_dwordx4 %[dsrc], %[srdDn], s45 offen lds\n\t"
    "v_mfma_f32_16x16x32_bf16 v[136:139], a[168:171], v[72:75], v[136:139]\n\t"
    "ds_read_b64_tr_b16 a[132:133], v240 offset:4608\n\t"
    "v_cvt_pk_bf16_f32 v56, v176, v177\n\t"
    "v_mfma_f32_16x16x32_bf16 v[136:139], a[172:175], v[76:79], v[136:139]\n\t"
    "ds_read_b64_tr_b16 a[134:135], v240 offset:6656\n\t"
    "v_cvt_pk_bf16_f32 v57, v178, v179\n\t"
    "v_mfma_f32_16x16x32_bf16 v[140:143], a[160:163], v[80:83], 0\n\t"
    "ds_read_b64_tr_b16 a[136:137], v240 offset:8704\n\t"
    "v_cvt_pk_bf16_f32 v58, v184, v185\n\t"
    "v_mfma_f32_16x16x32_bf16 v[140:143], a[164:167], v[84:87], v[140:143]\n\t"
    "ds_read_b64_tr_b16 a[138:139], v240 offset:10752\n\t"
    "v_cvt_pk_bf16_f32 v59, v186, v187\n\t"
    "s_mov_b32 s47, 15360\n\t"
    "v_mfma_f32_16x16x32_bf16 v[140:143], a[168:171], v[88:91], v[140:143]\n\t"
    "ds_read_b64_tr_b16 a[140:141], v240 offset:12800\n\t"
    "buffer_store_dwordx4 v[56:59], %[poff], %[srdP], s47 offen sc1\n\t"
    "v_mfma_f32_16x16x32_bf16 v[140:143], a[172:175], v[92:95], v[140:143]\n\t"
    "ds_read_b64_tr_b16 a[142:143], v240 offset:14848\n\t"
    "s_waitcnt lgkmcnt(8)\n\t"
    "v_mfma_f32_16x16x32_bf16 v[136:139], a[176:179], v[96:99], v[136:139]\n\t"
    "ds_read_b64_tr_b16 a[144:145], v240 offset:16896\n\t"
    "v_mfma_f32_16x16x32_bf16 v[136:139], a[180:183], v[100:103], v[136:139]\n\t"
    "ds_read_b64_tr_b16 a[146:147], v240 offset:18944\n\t"
    "v_mfma_f32_16x16x32_bf16 v[136:139], a[184:187], v[104:107], v[136:139]\n\t"
    "ds_read_b64_tr_b16 a[148:149], v240 offset:20992\n\t"
    "v_cvt_pk_bf16_f32 v60, v180, v181\n\t"
    "v_mfma_f32_16x16x32_bf16 v[136:139], a[188:191], v[108:111], v[136:139]\n\t"
    "ds_read_b64_tr_b16 a[150:151], v240 offset:23040\n\t"
    "v_cvt_pk_bf16_f32 v61, v182, v183\n\t"
    "v_mfma_f32_16x16x32_bf16 v[140:143], a[176:179], v[112:115], v[140:143]\n\t"
    "ds_read_b64_tr_b16 a[152:153], v240 offset:25088\n\t"
    "v_cvt_pk_bf16_f32 v62, v188, v189\n\t"
    "v_mfma_f32_16x16x32_bf16 v[140:143], a[180:183], v[116:119], v[140:143]\n\t"
    "ds_read_b64_tr_b16 a[154:155], v240 offset:27136\n\t"
    "v_cvt_pk_bf16_f32 v63, v190, v191\n\t"
    "s_mov_b32 s47, 33792\n\t"
    "v_mfma_f32_16x16x32_bf16 v[140:143], a[184:187], v[120:123], v[140:143]\n\t"
    "ds_read_b64_tr_b16 a[156:157], v240 offset:29184\n\t"
    "buffer_store_dwordx4 v[60:63], %[poff], %[srdP], s47 offen sc1\n\t"
    "v_mfma_f32_16x16x32_bf16 v[140:143], a[188:191], v[124:127], v[140:143]\n\t"
    "ds_read_b64_tr_b16 a[158:159], v240 offset:31232\n\t"
    "s_waitcnt lgkmcnt(8)\n\t"
    "v_mfma_f32_16x16x32_bf16 v[144:147], a[128:131], v[64:67], 0\n\t"
    "ds_read_b64_tr_b16 a[160:161], v240 offset:768\n\t"
    "v_mfma_f32_16x16x32_bf16 v[144:147], a[132:135], v[68:71], v[144:147]\n\t"
    "ds_read_b64_tr_b16 a[162:163], v240 offset:2816\n\t"
    "v_mfma_f32_16x16x32_bf16 v[144:147], a[136:139], v[72:75], v[144:147]\n\t"
    "ds_read_b64_tr_b16 a[164:165], v240 offset:4864\n\t"
    "v_mfma_f32_16x16x32_bf16 v[144:147], a[140:143], v[76:79], v[144:147]\n\t"
    "ds_read_b64_tr_b16 a[166:167], v240 offset:6912\n\t"
    "v_mfma_f32_16x16x32_bf16 v[148:151], a[128:131], v[80:83], 0\n\t"
    "ds_read_b64_tr_b16 a[168:169], v240 offset:8960\n\t"
    "v_mfma_f32_16x16x32_bf16 v[148:151], a[132:135], v[84:87], v[148:151]\n\t"
    "ds_read_b64_tr_b16 a[170:171], v240 offset:11008\n\t"
    "v_mfma_f32_16x16x32_bf16 v[148:151], a[136:139], v[88:91], v[148:151]\n\t"
    "ds_read_b64_tr_b16 a[172:173], v240 offset:13056\n\t"
    "v_mfma_f32_16x16x32_bf16 v[148:151], a[140:143], v[92:95], v[148:151]\n\t"
    "ds_read_b64_tr_b16 a[174:175], v240 offset:15104\n\t"
    "s_waitcnt lgkmcnt(8)\n\t"
    "v_mfma_f32_16x16x32_bf16 v[144:147], a[144:147], v[96:99], v[144:147]\n\t"
    "ds_read_b64_tr_b16 a[176:177], v240 offset:17152\n\t"
    "v_mfma_f32_16x16x32_bf16 v[144:147], a[148:151], v[100:103], v[144:147]\n\t"
    "ds_read_b64_tr_b16 a[178:179], v240 offset:19200\n\t"
    "v_mfma_f32_16x16x32_bf16 v[144:147], a[152:155], v[104:107], v[144:147]\n\t"
    "ds_read_b64_tr_b16 a[180:181], v240 offset:21248\n\t"
    "v_mfma_f32_16x16x32_bf16 v[144:147], a[156:159], v[108:111], v[144:147]\n\t"
    "ds_read_b64_tr_b16 a[182:183], v240 offset:23296\n\t"
    "v_mfma_f32_16x16x32_bf16 v[148:151], a[144:147], v[112:115], v[148:151]\n\t"
    "ds_read_b64_tr_b16 a[184:185], v240 offset:25344\n\t"
    "v_mfma_f32_16x16x32_bf16 v[148:151], a[148:151], v[116:119], v[148:151]\n\t"
    "ds_read_b64_tr_b16 a[186:187], v240 offset:27392\n\t"
    "v_mfma_f32_16x16x32_bf16 v[148:151], a[152:155], v[120:123], v[148:151]\n\t"
    "ds_read_b64_tr_b16 a[188:189], v240 offset:29440\n\t"
    "v_mfma_f32_16x16x32_bf16 v[148:151], a[156:159], v[124:127], v[148:151]\n\t"
    "ds_read_b64_tr_b16 a[190:191], v240 offset:31488\n\t"
    "s_waitcnt lgkmcnt(8)\n\t"
    "v_mfma_f32_16x16x32_bf16 v[152:155], a[160:163], v[64:67], 0\n\t"
    "v_mfma_f32_16x16x32_bf16 v[152:155], a[164:167], v[68:71], v[152:155]\n\t"
    "v_mfma_f32_16x16x32_bf16 v[152:155], a[168:171], v[72:75], v[152:155]\n\t"
    "v_mfma_f32_16x16x32_bf16 v[152:155], a[172:175], v[76:79], v[152:155]\n\t"
    "v_mfma_f32_16x16x32_bf16 v[156:159], a[160:163], v[80:83], 0\n\t"
    "v_mfma_f32_16x16x32_bf16 v[156:159], a[164:167], v[84:87], v[156:159]\n\t"
    "v_mfma_f32_16x16x32_bf16 v[156:159], a[168:171], v[88:91], v[156:159]\n\t"
    "v_mfma_f32_16x16x32_bf16 v[156:159], a[172:175], v[92:95], v[156:159]\n\t"
    "s_waitcnt lgkmcnt(0)\n\t"
    "v_mfma_f32_16x16x32_bf16 v[152:155], a[176:179], v[96:99], v[152:155]\n\t"
    "v_mfma_f32_16x16x32_bf16 v[152:155], a[180:183], v[100:103], v[152:155]\n\t"
    "v_mfma_f32_16x16x32_bf16 v[152:155], a[184:187], v[104:107], v[152:155]\n\t"
    "v_mfma_f32_16x16x32_bf16 v[152:155], a[188:191], v[108:111], v[152:155]\n\t"
    "v_mfma_f32_16x16x32_bf16 v[156:159], a[176:179], v[112:115], v[156:159]\n\t"
    "v_mfma_f32_16x16x32_bf16 v[156:159], a[180:183], v[116:119], v[156:159]\n\t"
    "v_mfma_f32_16x16x32_bf16 v[156:159], a[184:187], v[120:123], v[156:159]\n\t"
    "v_mfma_f32_16x16x32_bf16 v[156:159], a[188:191], v[124:127], v[156:159]\n\t"
    "s_nop 7\n\t"
    "v_cvt_pk_bf16_f32 v32, v128, v129\n\t"
    "v_cvt_pk_bf16_f32 v33, v130, v131\n\t"
    "v_cvt_pk_bf16_f32 v34, v136, v137\n\t"
    "v_cvt_pk_bf16_f32 v35, v138, v139\n\t"
    "s_mov_b32 s47, 16384\n\t"
    "buffer_store_dwordx4 v[32:35], %[poff], %[srdP], s47 offen sc1\n\t"
    "v_cvt_pk_bf16_f32 v36, v132, v133\n\t"
    "v_cvt_pk_bf16_f32 v37, v134, v135\n\t"
    "v_cvt_pk_bf16_f32 v38, v140, v141\n\t"
    "v_cvt_pk_bf16_f32 v39, v142, v143\n\t"
    "s_mov_b32 s47, 34816\n\t"
    "buffer_store_dwordx4 v[36:39], %[poff], %[srdP], s47 offen sc1\n\t"
    "v_cvt_pk_bf16_f32 v40, v144, v145\n\t"
    "v_cvt_pk_bf16_f32 v41, v146, v147\n\t"
    "v_cvt_pk_bf16_f32 v42, v152, v153\n\t"
    "v_cvt_pk_bf16_f32 v43, v154, v155\n\t"
    "s_mov_b32 s47, 17408\n\t"
    "buffer_store_dwordx4 v[40:43], %[poff], %[srdP], s47 offen sc1\n\t"
    "v_cvt_pk_bf16_f32 v44, v148, v149\n\t"
    "v_cvt_pk_bf16_f32 v45, v150, v151\n\t"
    "v_cvt_pk_bf16_f32 v46, v156, v157\n\t"
    "v_cvt_pk_bf16_f32 v47, v158, v159\n\t"
    "s_mov_b32 s47, 35840\n\t"
    "buffer_store_dwordx4 v[44:47], %[poff], %[srdP], s47 offen sc1\n\t"
    "s_branch END%=\n\t"
    "FB%=:\n\t"
    "s_waitcnt vmcnt(0) lgkmcnt(0)\n\t"
    "s_barrier\n\t"
    "v_mov_b32 v64, 0\n\t"
    "v_mov_b32 v65, 0\n\t"
    "v_mov_b32 v66, 0\n\t"
    "v_mov_b32 v67, 0\n\t"
    "v_mov_b32 v68, 0\n\t"
    "v_mov_b32 v69, 0\n\t"
    "v_mov_b32 v70, 0\n\t"
    "v_mov_b32 v71, 0\n\t"
    "v_mov_b32 v72, 0\n\t"
    "v_mov_b32 v73, 0\n\t"
    "v_mov_b32 v74, 0\n\t"
    "v_mov_b32 v75, 0\n\t"
    "v_mov_b32 v76, 0\n\t"
    "v_mov_b32 v77, 0\n\t"
    "v_mov_b32 v78, 0\n\t"
    "v_mov_b32 v79, 0\n\t"
    "v_mov_b32 v80, 0\n\t"
    "v_mov_b32 v81, 0\n\t"
    "v_mov_b32 v82, 0\n\t"
    "v_mov_b32 v83, 0\n\t"
    "v_mov_b32 v84, 0\n\t"
    "v_mov_b32 v85, 0\n\t"
    "v_mov_b32 v86, 0\n\t"
    "v_mov_b32 v87, 0\n\t"
    "v_mov_b32 v88, 0\n\t"
    "v_mov_b32 v89, 0\n\t"
    "v_mov_b32 v90, 0\n\t"
    "v_mov_b32 v91, 0\n\t"
    "v_mov_b32 v92, 0\n\t"
    "v_mov_b32 v93, 0\n\t"
    "v_mov_b32 v94, 0\n\t"
    "v_mov_b32 v95, 0\n\t"
    "v_mov_b32 v96, 0\n\t"
    "v_mov_b32 v97, 0\n\t"
    "v_mov_b32 v98, 0\n\t"
    "v_mov_b32 v99, 0\n\t"
    "v_mov_b32 v100, 0\n\t"
    "v_mov_b32 v101, 0\n\t"
    "v_mov_b32 v102, 0\n\t"
    "v_mov_b32 v103, 0\n\t"
    "v_mov_b32 v104, 0\n\t"
    "v_mov_b32 v105, 0\n\t"
    "v_mov_b32 v106, 0\n\t"
    "v_mov_b32 v107, 0\n\t"
    "v_mov_b32 v108, 0\n\t"
    "v_mov_b32 v109, 0\n\t"
    "v_mov_b32 v110, 0\n\t"
    "v_mov_b32 v111, 0\n\t"
    "v_mov_b32 v112, 0\n\t"
    "v_mov_b32 v113, 0\n\t"
    "v_mov_b32 v114, 0\n\t"
    "v_mov_b32 v115, 0\n\t"
    "v_mov_b32 v116, 0\n\t"
    "v_mov_b32 v117, 0\n\t"
    "v_mov_b32 v118, 0\n\t"
    "v_mov_b32 v119, 0\n\t"
    "v_mov_b32 v120, 0\n\t"
    "v_mov_b32 v121, 0\n\t"
    "v_mov_b32 v122, 0\n\t"
    "v_mov_b32 v123, 0\n\t"
    "v_mov_b32 v124, 0\n\t"
    "v_mov_b32 v125, 0\n\t"
    "v_mov_b32 v126, 0\n\t"
    "v_mov_b32 v127, 0\n\t"
    "v_mov_b32 v128, 0\n\t"
    "v_mov_b32 v129, 0\n\t"
    "v_mov_b32 v130, 0\n\t"
    "v_mov_b32 v131, 0\n\t"
    "v_mov_b32 v132, 0\n\t"
    "v_mov_b32 v133, 0\n\t"
    "v_mov_b32 v134, 0\n\t"
    "v_mov_b32 v135, 0\n\t"
    "v_mov_b32 v136, 0\n\t"
    "v_mov_b32 v137, 0\n\t"
    "v_mov_b32 v138, 0\n\t"
    "v_mov_b32 v139, 0\n\t"
    "v_mov_b32 v140, 0\n\t"
    "v_mov_b32 v141, 0\n\t"
    "v_mov_b32 v142, 0\n\t"
    "v_mov_b32 v143, 0\n\t"
    "v_mov_b32 v144, 0\n\t"
    "v_mov_b32 v145, 0\n\t"
    "v_mov_b32 v146, 0\n\t"
    "v_mov_b32 v147, 0\n\t"
    "v_mov_b32 v148, 0\n\t"
    "v_mov_b32 v149, 0\n\t"
    "v_mov_b32 v150, 0\n\t"
    "v_mov_b32 v151, 0\n\t"
    "v_mov_b32 v152, 0\n\t"
    "v_mov_b32 v153, 0\n\t"
    "v_mov_b32 v154, 0\n\t"
    "v_mov_b32 v155, 0\n\t"
    "v_mov_b32 v156, 0\n\t"
    "v_mov_b32 v157, 0\n\t"
    "v_mov_b32 v158, 0\n\t"
    "v_mov_b32 v159, 0\n\t"
    "v_mov_b32 v160, 0\n\t"
    "v_mov_b32 v161, 0\n\t"
    "v_mov_b32 v162, 0\n\t"
    "v_mov_b32 v163, 0\n\t"
    "v_mov_b32 v164, 0\n\t"
    "v_mov_b32 v165, 0\n\t"
    "v_mov_b32 v166, 0\n\t"
    "v_mov_b32 v167, 0\n\t"
    "v_mov_b32 v168, 0\n\t"
    "v_mov_b32 v169, 0\n\t"
    "v_mov_b32 v170, 0\n\t"
    "v_mov_b32 v171, 0\n\t"
    "v_mov_b32 v172, 0\n\t"
    "v_mov_b32 v173, 0\n\t"
    "v_mov_b32 v174, 0\n\t"
    "v_mov_b32 v175, 0\n\t"
    "v_mov_b32 v176, 0\n\t"
    "v_mov_b32 v177, 0\n\t"
    "v_mov_b32 v178, 0\n\t"
    "v_mov_b32 v179, 0\n\t"
    "v_mov_b32 v180, 0\n\t"
    "v_mov_b32 v181, 0\n\t"
    "v_mov_b32 v182, 0\n\t"
    "v_mov_b32 v183, 0\n\t"
    "v_mov_b32 v184, 0\n\t"
    "v_mov_b32 v185, 0\n\t"
    "v_mov_b32 v186, 0\n\t"
    "v_mov_b32 v187, 0\n\t"
    "v_mov_b32 v188, 0\n\t"
    "v_mov_b32 v189, 0\n\t"
    "v_mov_b32 v190, 0\n\t"
    "v_mov_b32 v191, 0\n\t"
    "v_mov_b32 v208, 0\n\t"
    "v_mov_b32 v209, 0\n\t"
    "v_mov_b32 v210, 0\n\t"
    "v_mov_b32 v211, 0\n\t"
    "v_mov_b32 v212, 0\n\t"
    "v_mov_b32 v213, 0\n\t"
    "v_mov_b32 v214, 0\n\t"
    "v_mov_b32 v215, 0\n\t"
    "v_mov_b32 v216, 0\n\t"
    "v_mov_b32 v217, 0\n\t"
    "v_mov_b32 v218, 0\n\t"
    "v_mov_b32 v219, 0\n\t"
    "v_mov_b32 v220, 0\n\t"
    "v_mov_b32 v221, 0\n\t"
    "v_mov_b32 v222, 0\n\t"
    "v_mov_b32 v223, 0\n\t"
    "v_mov_b32 v232, 0xff7fffff\n\t"
    "v_mov_b32 v233, 0xff7fffff\n\t"
    "v_mov_b32 v234, 0xff7fffff\n\t"
    "v_mov_b32 v235, 0xff7fffff\n\t"
    "s_mov_b32 s40, 0\n\t"
    "FBT%=:\n\t"
    "s_add_u32 m0, s49, 0\n\t"
    "s_add_u32 s45, s40, 0\n\t"
    "buffer_load_dwordx4 %[ksrc0], %[srdK], s45 offen offset:0 lds\n\t"
    "buffer_load_dwordx4 v242, %[srdK], s45 offen offset:1024 lds\n\t"
    "buffer_load_dwordx4 v243, %[srdK], s45 offen offset:2048 lds\n\t"
    "buffer_load_dwordx4 v244, %[srdK], s45 offen offset:3072 lds\n\t"
    "s_add_u32 m0, s49, 4096\n\t"
    "s_add_u32 s45, s40, 4096\n\t"
    "buffer_load_dwordx4 %[ksrc0], %[srdK], s45 offen offset:0 lds\n\t"
    "buffer_load_dwordx4 v242, %[srdK], s45 offen offset:1024 lds\n\t"
    "buffer_load_dwordx4 v243, %[srdK], s45 offen offset:2048 lds\n\t"
    "buffer_load_dwordx4 v244, %[srdK], s45 offen offset:3072 lds\n\t"
    "s_add_u32 m0, s49, 65536\n\t"
    "s_add_u32 s45, s40, 0\n\t"
    "buffer_load_dwordx4 %[vsrc0], %[srdV], s45 offen offset:0 lds\n\t"
    "buffer_load_dwordx4 v245, %[srdV], s45 offen offset:1024 lds\n\t"
    "buffer_load_dwordx4 %[vsrc0], %[srdV], s45 offen offset:2048 lds\n\t"
    "buffer_load_dwordx4 v245, %[srdV], s45 offen offset:3072 lds\n\t"
    "s_add_u32 m0, s49, 69632\n\t"
    "s_add_u32 s45, s40, 4096\n\t"
    "buffer_load_dwordx4 %[vsrc0], %[srdV], s45 offen offset:0 lds\n\t"
    "buffer_load_dwordx4 v245, %[srdV], s45 offen offset:1024 lds\n\t"
    "buffer_load_dwordx4 %[vsrc0], %[srdV], s45 offen offset:2048 lds\n\t"
    "buffer_load_dwordx4 v245, %[srdV], s45 offen offset:3072 lds\n\t"
    "v_mov_b32 v241, %[vb]\n\t"
    "s_mov_b32 s44, 4\n\t"
    "s_waitcnt vmcnt(0)\n\t"
    "s_barrier\n\t"
    "FBK%=:\n\t"
    "ds_read_b128 a[192:195], v228 offset:0\n\t"
    "ds_read_b128 a[196:199], v229 offset:0\n\t"
    "ds_read_b128 a[200:203], v230 offset:0\n\t"
    "ds_read_b128 a[204:207], v231 offset:0\n\t"
    "ds_read_b128 a[208:211], v228 offset:4096\n\t"
    "ds_read_b128 a[212:215], v229 offset:4096\n\t"
    "ds_read_b128 a[216:219], v230 offset:4096\n\t"
    "ds_read_b128 a[220:223], v231 offset:4096\n\t"
    "s_waitcnt lgkmcnt(0)\n\t"
    "v_mfma_f32_16x16x32_bf16 v[32:35], a[192:195], a[128:131], 0\n\t"
    "v_mfma_f32_16x16x32_bf16 v[32:35], a[196:199], a[132:135], v[32:35]\n\t"
    "v_mfma_f32_16x16x32_bf16 v[32:35], a[200:203], a[136:139], v[32:35]\n\t"
    "v_mfma_f32_16x16x32_bf16 v[32:35], a[204:207], a[140:143], v[32:35]\n\t"
    "v_mfma_f32_16x16x32_bf16 v[36:39], a[192:195], a[144:147], 0\n\t"
    "v_mfma_f32_16x16x32_bf16 v[36:39], a[196:199], a[148:151], v[36:39]\n\t"
    "v_mfma_f32_16x16x32_bf16 v[36:39], a[200:203], a[152:155], v[36:39]\n\t"
    "v_mfma_f32_16x16x32_bf16 v[36:39], a[204:207], a[156:159], v[36:39]\n\t"
    "v_mfma_f32_16x16x32_bf16 v[40:43], a[192:195], a[160:163], 0\n\t"
    "v_mfma_f32_16x16x32_bf16 v[40:43], a[196:199], a[164:167], v[40:43]\n\t"
    "v_mfma_f32_16x16x32_bf16 v[40:43], a[200:203], a[168:171], v[40:43]\n\t"
    "v_mfma_f32_16x16x32_bf16 v[40:43], a[204:207], a[172:175], v[40:43]\n\t"
    "v_mfma_f32_16x16x32_bf16 v[44:47], a[192:195], a[176:179], 0\n\t"
    "v_mfma_f32_16x16x32_bf16 v[44:47], a[196:199], a[180:183], v[44:47]\n\t"
    "v_mfma_f32_16x16x32_bf16 v[44:47], a[200:203], a[184:187], v[44:47]\n\t"
    "v_mfma_f32_16x16x32_bf16 v[44:47], a[204:207], a[188:191], v[44:47]\n\t"
    "v_mfma_f32_16x16x32_bf16 v[48:51], a[208:211], a[128:131], 0\n\t"
    "v_mfma_f32_16x16x32_bf16 v[48:51], a[212:215], a[132:135], v[48:51]\n\t"
    "v_mfma_f32_16x16x32_bf16 v[48:51], a[216:219], a[136:139], v[48:51]\n\t"
    "v_mfma_f32_16x16x32_bf16 v[48:51], a[220:223], a[140:143], v[48:51]\n\t"
    "v_mfma_f32_16x16x32_bf16 v[52:55], a[208:211], a[144:147], 0\n\t"
    "v_mfma_f32_16x16x32_bf16 v[52:55], a[212:215], a[148:151], v[52:55]\n\t"
    "v_mfma_f32_16x16x32_bf16 v[52:55], a[216:219], a[152:155], v[52:55]\n\t"
    "v_mfma_f32_16x16x32_bf16 v[52:55], a[220:223], a[156:159], v[52:55]\n\t"
    "v_mfma_f32_16x16x32_bf16 v[56:59], a[208:211], a[160:163], 0\n\t"
    "v_mfma_f32_16x16x32_bf16 v[56:59], a[212:215], a[164:167], v[56:59]\n\t"
    "v_mfma_f32_16x16x32_bf16 v[56:59], a[216:219], a[168:171], v[56:59]\n\t"
    "v_mfma_f32_16x16x32_bf16 v[56:59], a[220:223], a[172:175], v[56:59]\n\t"
    "v_mfma_f32_16x16x32_bf16 v[60:63], a[208:211], a[176:179], 0\n\t"
    "v_mfma_f32_16x16x32_bf16 v[60:63], a[212:215], a[180:183], v[60:63]\n\t"
    "v_mfma_f32_16x16x32_bf16 v[60:63], a[216:219], a[184:187], v[60:63]\n\t"
    "v_mfma_f32_16x16x32_bf16 v[60:63], a[220:223], a[188:191], v[60:63]\n\t"
    "s_nop 7\n\t"
    "s_nop 1\n\t"
    "v_cvt_pk_bf16_f32 v32, 0, v32\n\t"
    "v_cvt_pk_bf16_f32 v33, 0, v33\n\t"
    "v_cvt_pk_bf16_f32 v34, 0, v34\n\t"
    "v_cvt_pk_bf16_f32 v35, 0, v35\n\t"
    "v_cvt_pk_bf16_f32 v36, 0, v36\n\t"
    "v_cvt_pk_bf16_f32 v37, 0, v37\n\t"
    "v_cvt_pk_bf16_f32 v38, 0, v38\n\t"
    "v_cvt_pk_bf16_f32 v39, 0, v39\n\t"
    "v_cvt_pk_bf16_f32 v48, 0, v48\n\t"
    "v_cvt_pk_bf16_f32 v49, 0, v49\n\t"
    "v_cvt_pk_bf16_f32 v50, 0, v50\n\t"
    "v_cvt_pk_bf16_f32 v51, 0, v51\n\t"
    "v_cvt_pk_bf16_f32 v52, 0, v52\n\t"
    "v_cvt_pk_bf16_f32 v53, 0, v53\n\t"
    "v_cvt_pk_bf16_f32 v54, 0, v54\n\t"
    "v_cvt_pk_bf16_f32 v55, 0, v55\n\t"
    "v_mul_f32 v32, s56, v32\n\t"
    "v_mul_f32 v33, s56, v33\n\t"
    "v_mul_f32 v34, s56, v34\n\t"
    "v_mul_f32 v35, s56, v35\n\t"
    "v_mul_f32 v36, s56, v36\n\t"
    "v_mul_f32 v37, s56, v37\n\t"
    "v_mul_f32 v38, s56, v38\n\t"
    "v_mul_f32 v39, s56, v39\n\t"
    "v_mul_f32 v48, s56, v48\n\t"
    "v_mul_f32 v49, s56, v49\n\t"
    "v_mul_f32 v50, s56, v50\n\t"
    "v_mul_f32 v51, s56, v51\n\t"
    "v_mul_f32 v52, s56, v52\n\t"
    "v_mul_f32 v53, s56, v53\n\t"
    "v_mul_f32 v54, s56, v54\n\t"
    "v_mul_f32 v55, s56, v55\n\t"
    "v_cvt_pk_bf16_f32 v32, 0, v32\n\t"
    "v_cvt_pk_bf16_f32 v33, 0, v33\n\t"
    "v_cvt_pk_bf16_f32 v34, 0, v34\n\t"
    "v_cvt_pk_bf16_f32 v35, 0, v35\n\t"
    "v_cvt_pk_bf16_f32 v36, 0, v36\n\t"
    "v_cvt_pk_bf16_f32 v37, 0, v37\n\t"
    "v_cvt_pk_bf16_f32 v38, 0, v38\n\t"
    "v_cvt_pk_bf16_f32 v39, 0, v39\n\t"
    "v_cvt_pk_bf16_f32 v48, 0, v48\n\t"
    "v_cvt_pk_bf16_f32 v49, 0, v49\n\t"
    "v_cvt_pk_bf16_f32 v50, 0, v50\n\t"
    "v_cvt_pk_bf16_f32 v51, 0, v51\n\t"
    "v_cvt_pk_bf16_f32 v52, 0, v52\n\t"
    "v_cvt_pk_bf16_f32 v53, 0, v53\n\t"
    "v_cvt_pk_bf16_f32 v54, 0, v54\n\t"
    "v_cvt_pk_bf16_f32 v55, 0, v55\n\t"
    "v_mul_f32 v32, s58, v32\n\t"
    "v_mul_f32 v33, s58, v33\n\t"
    "v_mul_f32 v34, s58, v34\n\t"
    "v_mul_f32 v35, s58, v35\n\t"
    "v_mul_f32 v36, s58, v36\n\t"
    "v_mul_f32 v37, s58, v37\n\t"
    "v_mul_f32 v38, s58, v38\n\t"
    "v_mul_f32 v39, s58, v39\n\t"
    "v_mul_f32 v48, s58, v48\n\t"
    "v_mul_f32 v49, s58, v49\n\t"
    "v_mul_f32 v50, s58, v50\n\t"
    "v_mul_f32 v51, s58, v51\n\t"
    "v_mul_f32 v52, s58, v52\n\t"
    "v_mul_f32 v53, s58, v53\n\t"
    "v_mul_f32 v54, s58, v54\n\t"
    "v_mul_f32 v55, s58, v55\n\t"
    "v_max3_f32 v246, v32, v33, v34\n\t"
    "v_max3_f32 v246, v246, v35, v48\n\t"
    "v_max3_f32 v246, v246, v49, v50\n\t"
    "v_max3_f32 v246, v246, v51, v232\n\t"
    "v_mov_b32 v247, v246\n\t"
    "s_nop 1\n\t"
    "v_permlane32_swap_b32 v246, v247\n\t"
    "s_nop 1\n\t"
    "v_max_f32 v246, v246, v247\n\t"
    "v_mov_b32 v247, v246\n\t"
    "s_nop 1\n\t"
    "v_permlane16_swap_b32 v246, v247\n\t"
    "s_nop 1\n\t"
    "v_max_f32 v246, v246, v247\n\t"
    "v_sub_f32 v252, v232, v246\n\t"
    "v_exp_f32 v252, v252\n\t"
    "v_mov_b32 v232, v246\n\t"
    "v_sub_f32 v32, v32, v232\n\t"
    "v_sub_f32 v33, v33, v232\n\t"
    "v_sub_f32 v34, v34, v232\n\t"
    "v_sub_f32 v35, v35, v232\n\t"
    "v_sub_f32 v48, v48, v232\n\t"
    "v_sub_f32 v49, v49, v232\n\t"
    "v_sub_f32 v50, v50, v232\n\t"
    "v_sub_f32 v51, v51, v232\n\t"
    "v_exp_f32 v32, v32\n\t"
    "v_exp_f32 v33, v33\n\t"
    "v_exp_f32 v34, v34\n\t"
    "v_exp_f32 v35, v35\n\t"
    "v_exp_f32 v48, v48\n\t"
    "v_exp_f32 v49, v49\n\t"
    "v_exp_f32 v50, v50\n\t"
    "v_exp_f32 v51, v51\n\t"
    "s_nop 0\n\t"
    "v_cvt_pk_bf16_f32 v192, v32, v33\n\t"
    "v_cvt_pk_bf16_f32 v193, v34, v35\n\t"
    "v_cvt_pk_bf16_f32 v194, v48, v49\n\t"
    "v_cvt_pk_bf16_f32 v195, v50, v51\n\t"
    "v_max3_f32 v246, v36, v37, v38\n\t"
    "v_max3_f32 v246, v246, v39, v52\n\t"
    "v_max3_f32 v246, v246, v53, v54\n\t"
    "v_max3_f32 v246, v246, v55, v233\n\t"
    "v_mov_b32 v247, v246\n\t"
    "s_nop 1\n\t"
    "v_permlane32_swap_b32 v246, v247\n\t"
    "s_nop 1\n\t"
    "v_max_f32 v246, v246, v247\n\t"
    "v_mov_b32 v247, v246\n\t"
    "s_nop 1\n\t"
    "v_permlane16_swap_b32 v246, v247\n\t"
    "s_nop 1\n\t"
    "v_max_f32 v246, v246, v247\n\t"
    "v_sub_f32 v253, v233, v246\n\t"
    "v_exp_f32 v253, v253\n\t"
    "v_mov_b32 v233, v246\n\t"
    "v_sub_f32 v36, v36, v233\n\t"
    "v_sub_f32 v37, v37, v233\n\t"
    "v_sub_f32 v38, v38, v233\n\t"
    "v_sub_f32 v39, v39, v233\n\t"
    "v_sub_f32 v52, v52, v233\n\t"
    "v_sub_f32 v53, v53, v233\n\t"
    "v_sub_f32 v54, v54, v233\n\t"
    "v_sub_f32 v55, v55, v233\n\t"
    "v_exp_f32 v36, v36\n\t"
    "v_exp_f32 v37, v37\n\t"
    "v_exp_f32 v38, v38\n\t"
    "v_exp_f32 v39, v39\n\t"
    "v_exp_f32 v52, v52\n\t"
    "v_exp_f32 v53, v53\n\t"
    "v_exp_f32 v54, v54\n\t"
    "v_exp_f32 v55, v55\n\t"
    "s_nop 0\n\t"
    "v_cvt_pk_bf16_f32 v196, v36, v37\n\t"
    "v_cvt_pk_bf16_f32 v197, v38, v39\n\t"
    "v_cvt_pk_bf16_f32 v198, v52, v53\n\t"
    "v_cvt_pk_bf16_f32 v199, v54, v55\n\t"
    "v_max3_f32 v246, v40, v41, v42\n\t"
    "v_max3_f32 v246, v246, v43, v56\n\t"
    "v_max3_f32 v246, v246, v57, v58\n\t"
    "v_max3_f32 v246, v246, v59, v234\n\t"
    "v_mov_b32 v247, v246\n\t"
    "s_nop 1\n\t"
    "v_permlane32_swap_b32 v246, v247\n\t"
    "s_nop 1\n\t"
    "v_max_f32 v246, v246, v247\n\t"
    "v_mov_b32 v247, v246\n\t"
    "s_nop 1\n\t"
    "v_permlane16_swap_b32 v246, v247\n\t"
    "s_nop 1\n\t"
    "v_max_f32 v246, v246, v247\n\t"
    "v_sub_f32 v254, v234, v246\n\t"
    "v_exp_f32 v254, v254\n\t"
    "v_mov_b32 v234, v246\n\t"
    "v_sub_f32 v40, v40, v234\n\t"
    "v_sub_f32 v41, v41, v234\n\t"
    "v_sub_f32 v42, v42, v234\n\t"
    "v_sub_f32 v43, v43, v234\n\t"
    "v_sub_f32 v56, v56, v234\n\t"
    "v_sub_f32 v57, v57, v234\n\t"
    "v_sub_f32 v58, v58, v234\n\t"
    "v_sub_f32 v59, v59, v234\n\t"
    "v_exp_f32 v40, v40\n\t"
    "v_exp_f32 v41, v41\n\t"
    "v_exp_f32 v42, v42\n\t"
    "v_exp_f32 v43, v43\n\t"
    "v_exp_f32 v56, v56\n\t"
    "v_exp_f32 v57, v57\n\t"
    "v_exp_f32 v58, v58\n\t"
    "v_exp_f32 v59, v59\n\t"
    "s_nop 0\n\t"
    "v_cvt_pk_bf16_f32 v200, v40, v41\n\t"
    "v_cvt_pk_bf16_f32 v201, v42, v43\n\t"
    "v_cvt_pk_bf16_f32 v202, v56, v57\n\t"
    "v_cvt_pk_bf16_f32 v203, v58, v59\n\t"
    "v_max3_f32 v246, v44, v45, v46\n\t"
    "v_max3_f32 v246, v246, v47, v60\n\t"
    "v_max3_f32 v246, v246, v61, v62\n\t"
    "v_max3_f32 v246, v246, v63, v235\n\t"
    "v_mov_b32 v247, v246\n\t"
    "s_nop 1\n\t"
    "v_permlane32_swap_b32 v246, v247\n\t"
    "s_nop 1\n\t"
    "v_max_f32 v246, v246, v247\n\t"
    "v_mov_b32 v247, v246\n\t"
    "s_nop 1\n\t"
    "v_permlane16_swap_b32 v246, v247\n\t"
    "s_nop 1\n\t"
    "v_max_f32 v246, v246, v247\n\t"
    "v_sub_f32 v255, v235, v246\n\t"
    "v_exp_f32 v255, v255\n\t"
    "v_mov_b32 v235, v246\n\t"
    "v_sub_f32 v44, v44, v235\n\t"
    "v_sub_f32 v45, v45, v235\n\t"
    "v_sub_f32 v46, v46, v235\n\t"
    "v_sub_f32 v47, v47, v235\n\t"
    "v_sub_f32 v60, v60, v235\n\t"
    "v_sub_f32 v61, v61, v235\n\t"
    "v_sub_f32 v62, v62, v235\n\t"
    "v_sub_f32 v63, v63, v235\n\t"
    "v_exp_f32 v44, v44\n\t"
    "v_exp_f32 v45, v45\n\t"
    "v_exp_f32 v46, v46\n\t"
    "v_exp_f32 v47, v47\n\t"
    "v_exp_f32 v60, v60\n\t"
    "v_exp_f32 v61, v61\n\t"
    "v_exp_f32 v62, v62\n\t"
    "v_exp_f32 v63, v63\n\t"
    "s_nop 0\n\t"
    "v_cvt_pk_bf16_f32 v204, v44, v45\n\t"
    "v_cvt_pk_bf16_f32 v205, v46, v47\n\t"
    "v_cvt_pk_bf16_f32 v206, v60, v61\n\t"
    "v_cvt_pk_bf16_f32 v207, v62, v63\n\t"
    "v_mul_f32 v64, v64, v252\n\t"
    "v_mul_f32 v65, v65, v252\n\t"
    "v_mul_f32 v66, v66, v252\n\t"
    "v_mul_f32 v67, v67, v252\n\t"
    "v_mul_f32 v68, v68, v253\n\t"
    "v_mul_f32 v69, v69, v253\n\t"
    "v_mul_f32 v70, v70, v253\n\t"
    "v_mul_f32 v71, v71, v253\n\t"
    "v_mul_f32 v72, v72, v254\n\t"
    "v_mul_f32 v73, v73, v254\n\t"
    "v_mul_f32 v74, v74, v254\n\t"
    "v_mul_f32 v75, v75, v254\n\t"
    "v_mul_f32 v76, v76, v255\n\t"
    "v_mul_f32 v77, v77, v255\n\t"
    "v_mul_f32 v78, v78, v255\n\t"
    "v_mul_f32 v79, v79, v255\n\t"
    "v_mul_f32 v80, v80, v252\n\t"
    "v_mul_f32 v81, v81, v252\n\t"
    "v_mul_f32 v82, v82, v252\n\t"
    "v_mul_f32 v83, v83, v252\n\t"
    "v_mul_f32 v84, v84, v253\n\t"
    "v_mul_f32 v85, v85, v253\n\t"
    "v_mul_f32 v86, v86, v253\n\t"
    "v_mul_f32 v87, v87, v253\n\t"
    "v_mul_f32 v88, v88, v254\n\t"
    "v_mul_f32 v89, v89, v254\n\t"
    "v_mul_f32 v90, v90, v254\n\t"
    "v_mul_f32 v91, v91, v254\n\t"
    "v_mul_f32 v92, v92, v255\n\t"
    "v_mul_f32 v93, v93, v255\n\t"
    "v_mul_f32 v94, v94, v255\n\t"
    "v_mul_f32 v95, v95, v255\n\t"
    "v_mul_f32 v96, v96, v252\n\t"
    "v_mul_f32 v97, v97, v252\n\t"
    "v_mul_f32 v98, v98, v252\n\t"
    "v_mul_f32 v99, v99, v252\n\t"
    "v_mul_f32 v100, v100, v253\n\t"
    "v_mul_f32 v101, v101, v253\n\t"
    "v_mul_f32 v102, v102, v253\n\t"
    "v_mul_f32 v103, v103, v253\n\t"
    "v_mul_f32 v104, v104, v254\n\t"
    "v_mul_f32 v105, v105, v254\n\t"
    "v_mul_f32 v106, v106, v254\n\t"
    "v_mul_f32 v107, v107, v254\n\t"
    "v_mul_f32 v108, v108, v255\n\t"
    "v_mul_f32 v109, v109, v255\n\t"
    "v_mul_f32 v110, v110, v255\n\t"
    "v_mul_f32 v111, v111, v255\n\t"
    "v_mul_f32 v112, v112, v252\n\t"
    "v_mul_f32 v113, v113, v252\n\t"
    "v_mul_f32 v114, v114, v252\n\t"
    "v_mul_f32 v115, v115, v252\n\t"
    "v_mul_f32 v116, v116, v253\n\t"
    "v_mul_f32 v117, v117, v253\n\t"
    "v_mul_f32 v118, v118, v253\n\t"
    "v_mul_f32 v119, v119, v253\n\t"
    "v_mul_f32 v120, v120, v254\n\t"
    "v_mul_f32 v121, v121, v254\n\t"
    "v_mul_f32 v122, v122, v254\n\t"
    "v_mul_f32 v123, v123, v254\n\t"
    "v_mul_f32 v124, v124, v255\n\t"
    "v_mul_f32 v125, v125, v255\n\t"
    "v_mul_f32 v126, v126, v255\n\t"
    "v_mul_f32 v127, v127, v255\n\t"
    "v_mul_f32 v128, v128, v252\n\t"
    "v_mul_f32 v129, v129, v252\n\t"
    "v_mul_f32 v130, v130, v252\n\t"
    "v_mul_f32 v131, v131, v252\n\t"
    "v_mul_f32 v132, v132, v253\n\t"
    "v_mul_f32 v133, v133, v253\n\t"
    "v_mul_f32 v134, v134, v253\n\t"
    "v_mul_f32 v135, v135, v253\n\t"
    "v_mul_f32 v136, v136, v254\n\t"
    "v_mul_f32 v137, v137, v254\n\t"
    "v_mul_f32 v138, v138, v254\n\t"
    "v_mul_f32 v139, v139, v254\n\t"
    "v_mul_f32 v140, v140, v255\n\t"
    "v_mul_f32 v141, v141, v255\n\t"
    "v_mul_f32 v142, v142, v255\n\t"
    "v_mul_f32 v143, v143, v255\n\t"
    "v_mul_f32 v144, v144, v252\n\t"
    "v_mul_f32 v145, v145, v252\n\t"
    "v_mul_f32 v146, v146, v252\n\t"
    "v_mul_f32 v147, v147, v252\n\t"
    "v_mul_f32 v148, v148, v253\n\t"
    "v_mul_f32 v149, v149, v253\n\t"
    "v_mul_f32 v150, v150, v253\n\t"
    "v_mul_f32 v151, v151, v253\n\t"
    "v_mul_f32 v152, v152, v254\n\t"
    "v_mul_f32 v153, v153, v254\n\t"
    "v_mul_f32 v154, v154, v254\n\t"
    "v_mul_f32 v155, v155, v254\n\t"
    "v_mul_f32 v156, v156, v255\n\t"
    "v_mul_f32 v157, v157, v255\n\t"
    "v_mul_f32 v158, v158, v255\n\t"
    "v_mul_f32 v159, v159, v255\n\t"
    "v_mul_f32 v160, v160, v252\n\t"
    "v_mul_f32 v161, v161, v252\n\t"
    "v_mul_f32 v162, v162, v252\n\t"
    "v_mul_f32 v163, v163, v252\n\t"
    "v_mul_f32 v164, v164, v253\n\t"
    "v_mul_f32 v165, v165, v253\n\t"
    "v_mul_f32 v166, v166, v253\n\t"
    "v_mul_f32 v167, v167, v253\n\t"
    "v_mul_f32 v168, v168, v254\n\t"
    "v_mul_f32 v169, v169, v254\n\t"
    "v_mul_f32 v170, v170, v254\n\t"
    "v_mul_f32 v171, v171, v254\n\t"
    "v_mul_f32 v172, v172, v255\n\t"
    "v_mul_f32 v173, v173, v255\n\t"
    "v_mul_f32 v174, v174, v255\n\t"
    "v_mul_f32 v175, v175, v255\n\t"
    "v_mul_f32 v176, v176, v252\n\t"
    "v_mul_f32 v177, v177, v252\n\t"
    "v_mul_f32 v178, v178, v252\n\t"
    "v_mul_f32 v179, v179, v252\n\t"
    "v_mul_f32 v180, v180, v253\n\t"
    "v_mul_f32 v181, v181, v253\n\t"
    "v_mul_f32 v182, v182, v253\n\t"
    "v_mul_f32 v183, v183, v253\n\t"
    "v_mul_f32 v184, v184, v254\n\t"
    "v_mul_f32 v185, v185, v254\n\t"
    "v_mul_f32 v186, v186, v254\n\t"
    "v_mul_f32 v187, v187, v254\n\t"
    "v_mul_f32 v188, v188, v255\n\t"
    "v_mul_f32 v189, v189, v255\n\t"
    "v_mul_f32 v190, v190, v255\n\t"
    "v_mul_f32 v191, v191, v255\n\t"
    "v_mul_f32 v208, v208, v252\n\t"
    "v_mul_f32 v209, v209, v252\n\t"
    "v_mul_f32 v210, v210, v252\n\t"
    "v_mul_f32 v211, v211, v252\n\t"
    "v_mul_f32 v212, v212, v253\n\t"
    "v_mul_f32 v213, v213, v253\n\t"
    "v_mul_f32 v214, v214, v253\n\t"
    "v_mul_f32 v215, v215, v253\n\t"
    "v_mul_f32 v216, v216, v254\n\t"
    "v_mul_f32 v217, v217, v254\n\t"
    "v_mul_f32 v218, v218, v254\n\t"
    "v_mul_f32 v219, v219, v254\n\t"
    "v_mul_f32 v220, v220, v255\n\t"
    "v_mul_f32 v221, v221, v255\n\t"
    "v_mul_f32 v222, v222, v255\n\t"
    "v_mul_f32 v223, v223, v255\n\t"
    "ds_read_b64_tr_b16 a[224:225], v241 offset:0\n\t"
    "ds_read_b64_tr_b16 a[226:227], v241 offset:4096\n\t"
    "s_waitcnt lgkmcnt(0)\n\t"
    "v_mfma_f32_16x16x32_bf16 v[64:67], a[224:227], v[192:195], v[64:67]\n\t"
    "v_mfma_f32_16x16x32_bf16 v[68:71], a[224:227], v[196:199], v[68:71]\n\t"
    "v_mfma_f32_16x16x32_bf16 v[72:75], a[224:227], v[200:203], v[72:75]\n\t"
    "v_mfma_f32_16x16x32_bf16 v[76:79], a[224:227], v[204:207], v[76:79]\n\t"
    "ds_read_b64_tr_b16 a[240:241], v241 offset:256\n\t"
    "ds_read_b64_tr_b16 a[242:243], v241 offset:4352\n\t"
    "s_waitcnt lgkmcnt(0)\n\t"
    "v_mfma_f32_16x16x32_bf16 v[80:83], a[240:243], v[192:195], v[80:83]\n\t"
    "v_mfma_f32_16x16x32_bf16 v[84:87], a[240:243], v[196:199], v[84:87]\n\t"
    "v_mfma_f32_16x16x32_bf16 v[88:91], a[240:243], v[200:203], v[88:91]\n\t"
    "v_mfma_f32_16x16x32_bf16 v[92:95], a[240:243], v[204:207], v[92:95]\n\t"
    "ds_read_b64_tr_b16 a[224:225], v241 offset:512\n\t"
    "ds_read_b64_tr_b16 a[226:227], v241 offset:4608\n\t"
    "s_waitcnt lgkmcnt(0)\n\t"
    "v_mfma_f32_16x16x32_bf16 v[96:99], a[224:227], v[192:195], v[96:99]\n\t"
    "v_mfma_f32_16x16x32_bf16 v[100:103], a[224:227], v[196:199], v[100:103]\n\t"
    "v_mfma_f32_16x16x32_bf16 v[104:107], a[224:227], v[200:203], v[104:107]\n\t"
    "v_mfma_f32_16x16x32_bf16 v[108:111], a[224:227], v[204:207], v[108:111]\n\t"
    "ds_read_b64_tr_b16 a[240:241], v241 offset:768\n\t"
    "ds_read_b64_tr_b16 a[242:243], v241 offset:4864\n\t"
    "s_waitcnt lgkmcnt(0)\n\t"
    "v_mfma_f32_16x16x32_bf16 v[112:115], a[240:243], v[192:195], v[112:115]\n\t"
    "v_mfma_f32_16x16x32_bf16 v[116:119], a[240:243], v[196:199], v[116:119]\n\t"
    "v_mfma_f32_16x16x32_bf16 v[120:123], a[240:243], v[200:203], v[120:123]\n\t"
    "v_mfma_f32_16x16x32_bf16 v[124:127], a[240:243], v[204:207], v[124:127]\n\t"
    "ds_read_b64_tr_b16 a[224:225], v241 offset:1024\n\t"
    "ds_read_b64_tr_b16 a[226:227], v241 offset:5120\n\t"
    "s_waitcnt lgkmcnt(0)\n\t"
    "v_mfma_f32_16x16x32_bf16 v[128:131], a[224:227], v[192:195], v[128:131]\n\t"
    "v_mfma_f32_16x16x32_bf16 v[132:135], a[224:227], v[196:199], v[132:135]\n\t"
    "v_mfma_f32_16x16x32_bf16 v[136:139], a[224:227], v[200:203], v[136:139]\n\t"
    "v_mfma_f32_16x16x32_bf16 v[140:143], a[224:227], v[204:207], v[140:143]\n\t"
    "ds_read_b64_tr_b16 a[240:241], v241 offset:1280\n\t"
    "ds_read_b64_tr_b16 a[242:243], v241 offset:5376\n\t"
    "s_waitcnt lgkmcnt(0)\n\t"
    "v_mfma_f32_16x16x32_bf16 v[144:147], a[240:243], v[192:195], v[144:147]\n\t"
    "v_mfma_f32_16x16x32_bf16 v[148:151], a[240:243], v[196:199], v[148:151]\n\t"
    "v_mfma_f32_16x16x32_bf16 v[152:155], a[240:243], v[200:203], v[152:155]\n\t"
    "v_mfma_f32_16x16x32_bf16 v[156:159], a[240:243], v[204:207], v[156:159]\n\t"
    "ds_read_b64_tr_b16 a[224:225], v241 offset:1536\n\t"
    "ds_read_b64_tr_b16 a[226:227], v241 offset:5632\n\t"
    "s_waitcnt lgkmcnt(0)\n\t"
    "v_mfma_f32_16x16x32_bf16 v[160:163], a[224:227], v[192:195], v[160:163]\n\t"
    "v_mfma_f32_16x16x32_bf16 v[164:167], a[224:227], v[196:199], v[164:167]\n\t"
    "v_mfma_f32_16x16x32_bf16 v[168:171], a[224:227], v[200:203], v[168:171]\n\t"
    "v_mfma_f32_16x16x32_bf16 v[172:175], a[224:227], v[204:207], v[172:175]\n\t"
    "ds_read_b64_tr_b16 a[240:241], v241 offset:1792\n\t"
    "ds_read_b64_tr_b16 a[242:243], v241 offset:5888\n\t"
    "s_waitcnt lgkmcnt(0)\n\t"
    "v_mfma_f32_16x16x32_bf16 v[176:179], a[240:243], v[192:195], v[176:179]\n\t"
    "v_mfma_f32_16x16x32_bf16 v[180:183], a[240:243], v[196:199], v[180:183]\n\t"
    "v_mfma_f32_16x16x32_bf16 v[184:187], a[240:243], v[200:203], v[184:187]\n\t"
    "v_mfma_f32_16x16x32_bf16 v[188:191], a[240:243], v[204:207], v[188:191]\n\t"
    "v_mfma_f32_16x16x32_bf16 v[208:211], v[224:227], v[192:195], v[208:211]\n\t"
    "v_mfma_f32_16x16x32_bf16 v[212:215], v[224:227], v[196:199], v[212:215]\n\t"
    "v_mfma_f32_16x16x32_bf16 v[216:219], v[224:227], v[200:203], v[216:219]\n\t"
    "v_mfma_f32_16x16x32_bf16 v[220:223], v[224:227], v[204:207], v[220:223]\n\t"
    "v_add_u32 v228, 0x2000, v228\n\t"
    "v_add_u32 v229, 0x2000, v229\n\t"
    "v_add_u32 v230, 0x2000, v230\n\t"
    "v_add_u32 v231, 0x2000, v231\n\t"
    "v_add_u32 v241, 0x2000, v241\n\t"
    "s_nop 7\n\t"
    "s_sub_u32 s44, s44, 1\n\t"
    "s_cmp_lg_u32 s44, 0\n\t"
    "s_cbranch_scc1 FBK%=\n\t"
    "v_subrev_u32 v228, 0x8000, v228\n\t"
    "v_subrev_u32 v229, 0x8000, v229\n\t"
    "v_subrev_u32 v230, 0x8000, v230\n\t"
    "v_subrev_u32 v231, 0x8000, v231\n\t"
    "s_waitcnt lgkmcnt(0)\n\t"
    "s_barrier\n\t"
    "s_add_u32 s40, s40, 0x8000\n\t"
    "s_cmp_lt_u32 s40, 0x80000\n\t"
    "s_cbranch_scc1 FBT%=\n\t"
    "s_nop 7\n\t"
    "v_accvgpr_write_b32 a0, v64\n\t"
    "v_accvgpr_write_b32 a1, v65\n\t"
    "v_accvgpr_write_b32 a2, v66\n\t"
    "v_accvgpr_write_b32 a3, v67\n\t"
    "v_accvgpr_write_b32 a4, v68\n\t"
    "v_accvgpr_write_b32 a5, v69\n\t"
    "v_accvgpr_write_b32 a6, v70\n\t"
    "v_accvgpr_write_b32 a7, v71\n\t"
    "v_accvgpr_write_b32 a8, v72\n\t"
    "v_accvgpr_write_b32 a9, v73\n\t"
    "v_accvgpr_write_b32 a10, v74\n\t"
    "v_accvgpr_write_b32 a11, v75\n\t"
    "v_accvgpr_write_b32 a12, v76\n\t"
    "v_accvgpr_write_b32 a13, v77\n\t"
    "v_accvgpr_write_b32 a14, v78\n\t"
    "v_accvgpr_write_b32 a15, v79\n\t"
    "v_accvgpr_write_b32 a16, v80\n\t"
    "v_accvgpr_write_b32 a17, v81\n\t"
    "v_accvgpr_write_b32 a18, v82\n\t"
    "v_accvgpr_write_b32 a19, v83\n\t"
    "v_accvgpr_write_b32 a20, v84\n\t"
    "v_accvgpr_write_b32 a21, v85\n\t"
    "v_accvgpr_write_b32 a22, v86\n\t"
    "v_accvgpr_write_b32 a23, v87\n\t"
    "v_accvgpr_write_b32 a24, v88\n\t"
    "v_accvgpr_write_b32 a25, v89\n\t"
    "v_accvgpr_write_b32 a26, v90\n\t"
    "v_accvgpr_write_b32 a27, v91\n\t"
    "v_accvgpr_write_b32 a28, v92\n\t"
    "v_accvgpr_write_b32 a29, v93\n\t"
    "v_accvgpr_write_b32 a30, v94\n\t"
    "v_accvgpr_write_b32 a31, v95\n\t"
    "v_accvgpr_write_b32 a32, v96\n\t"
    "v_accvgpr_write_b32 a33, v97\n\t"
    "v_accvgpr_write_b32 a34, v98\n\t"
    "v_accvgpr_write_b32 a35, v99\n\t"
    "v_accvgpr_write_b32 a36, v100\n\t"
    "v_accvgpr_write_b32 a37, v101\n\t"
    "v_accvgpr_write_b32 a38, v102\n\t"
    "v_accvgpr_write_b32 a39, v103\n\t"
    "v_accvgpr_write_b32 a40, v104\n\t"
    "v_accvgpr_write_b32 a41, v105\n\t"
    "v_accvgpr_write_b32 a42, v106\n\t"
    "v_accvgpr_write_b32 a43, v107\n\t"
    "v_accvgpr_write_b32 a44, v108\n\t"
    "v_accvgpr_write_b32 a45, v109\n\t"
    "v_accvgpr_write_b32 a46, v110\n\t"
    "v_accvgpr_write_b32 a47, v111\n\t"
    "v_accvgpr_write_b32 a48, v112\n\t"
    "v_accvgpr_write_b32 a49, v113\n\t"
    "v_accvgpr_write_b32 a50, v114\n\t"
    "v_accvgpr_write_b32 a51, v115\n\t"
    "v_accvgpr_write_b32 a52, v116\n\t"
    "v_accvgpr_write_b32 a53, v117\n\t"
    "v_accvgpr_write_b32 a54, v118\n\t"
    "v_accvgpr_write_b32 a55, v119\n\t"
    "v_accvgpr_write_b32 a56, v120\n\t"
    "v_accvgpr_write_b32 a57, v121\n\t"
    "v_accvgpr_write_b32 a58, v122\n\t"
    "v_accvgpr_write_b32 a59, v123\n\t"
    "v_accvgpr_write_b32 a60, v124\n\t"
    "v_accvgpr_write_b32 a61, v125\n\t"
    "v_accvgpr_write_b32 a62, v126\n\t"
    "v_accvgpr_write_b32 a63, v127\n\t"
    "v_accvgpr_write_b32 a64, v128\n\t"
    "v_accvgpr_write_b32 a65, v129\n\t"
    "v_accvgpr_write_b32 a66, v130\n\t"
    "v_accvgpr_write_b32 a67, v131\n\t"
    "v_accvgpr_write_b32 a68, v132\n\t"
    "v_accvgpr_write_b32 a69, v133\n\t"
    "v_accvgpr_write_b32 a70, v134\n\t"
    "v_accvgpr_write_b32 a71, v135\n\t"
    "v_accvgpr_write_b32 a72, v136\n\t"
    "v_accvgpr_write_b32 a73, v137\n\t"
    "v_accvgpr_write_b32 a74, v138\n\t"
    "v_accvgpr_write_b32 a75, v139\n\t"
    "v_accvgpr_write_b32 a76, v140\n\t"
    "v_accvgpr_write_b32 a77, v141\n\t"
    "v_accvgpr_write_b32 a78, v142\n\t"
    "v_accvgpr_write_b32 a79, v143\n\t"
    "v_accvgpr_write_b32 a80, v144\n\t"
    "v_accvgpr_write_b32 a81, v145\n\t"
    "v_accvgpr_write_b32 a82, v146\n\t"
    "v_accvgpr_write_b32 a83, v147\n\t"
    "v_accvgpr_write_b32 a84, v148\n\t"
    "v_accvgpr_write_b32 a85, v149\n\t"
    "v_accvgpr_write_b32 a86, v150\n\t"
    "v_accvgpr_write_b32 a87, v151\n\t"
    "v_accvgpr_write_b32 a88, v152\n\t"
    "v_accvgpr_write_b32 a89, v153\n\t"
    "v_accvgpr_write_b32 a90, v154\n\t"
    "v_accvgpr_write_b32 a91, v155\n\t"
    "v_accvgpr_write_b32 a92, v156\n\t"
    "v_accvgpr_write_b32 a93, v157\n\t"
    "v_accvgpr_write_b32 a94, v158\n\t"
    "v_accvgpr_write_b32 a95, v159\n\t"
    "v_accvgpr_write_b32 a96, v160\n\t"
    "v_accvgpr_write_b32 a97, v161\n\t"
    "v_accvgpr_write_b32 a98, v162\n\t"
    "v_accvgpr_write_b32 a99, v163\n\t"
    "v_accvgpr_write_b32 a100, v164\n\t"
    "v_accvgpr_write_b32 a101, v165\n\t"
    "v_accvgpr_write_b32 a102, v166\n\t"
    "v_accvgpr_write_b32 a103, v167\n\t"
    "v_accvgpr_write_b32 a104, v168\n\t"
    "v_accvgpr_write_b32 a105, v169\n\t"
    "v_accvgpr_write_b32 a106, v170\n\t"
    "v_accvgpr_write_b32 a107, v171\n\t"
    "v_accvgpr_write_b32 a108, v172\n\t"
    "v_accvgpr_write_b32 a109, v173\n\t"
    "v_accvgpr_write_b32 a110, v174\n\t"
    "v_accvgpr_write_b32 a111, v175\n\t"
    "v_accvgpr_write_b32 a112, v176\n\t"
    "v_accvgpr_write_b32 a113, v177\n\t"
    "v_accvgpr_write_b32 a114, v178\n\t"
    "v_accvgpr_write_b32 a115, v179\n\t"
    "v_accvgpr_write_b32 a116, v180\n\t"
    "v_accvgpr_write_b32 a117, v181\n\t"
    "v_accvgpr_write_b32 a118, v182\n\t"
    "v_accvgpr_write_b32 a119, v183\n\t"
    "v_accvgpr_write_b32 a120, v184\n\t"
    "v_accvgpr_write_b32 a121, v185\n\t"
    "v_accvgpr_write_b32 a122, v186\n\t"
    "v_accvgpr_write_b32 a123, v187\n\t"
    "v_accvgpr_write_b32 a124, v188\n\t"
    "v_accvgpr_write_b32 a125, v189\n\t"
    "v_accvgpr_write_b32 a126, v190\n\t"
    "v_accvgpr_write_b32 a127, v191\n\t"
    "s_add_u32 m0, s49, 0\n\t"
    "s_mov_b32 s48, 0\n\t"
    "buffer_load_dwordx4 %[osrc0], %[srdWo], s48 offen offset:0 lds\n\t"
    "buffer_load_dwordx4 v236, %[srdWo], s48 offen offset:1024 lds\n\t"
    "buffer_load_dwordx4 v237, %[srdWo], s48 offen offset:2048 lds\n\t"
    "buffer_load_dwordx4 v238, %[srdWo], s48 offen offset:3072 lds\n\t"
    "s_add_u32 m0, s49, 4096\n\t"
    "s_mov_b32 s48, 36864\n\t"
    "buffer_load_dwordx4 %[osrc0], %[srdWo], s48 offen offset:0 lds\n\t"
    "buffer_load_dwordx4 v236, %[srdWo], s48 offen offset:1024 lds\n\t"
    "buffer_load_dwordx4 v237, %[srdWo], s48 offen offset:2048 lds\n\t"
    "buffer_load_dwordx4 v238, %[srdWo], s48 offen offset:3072 lds\n\t"
    "s_add_u32 m0, s49, 32768\n\t"
    "s_mov_b32 s48, 128\n\t"
    "buffer_load_dwordx4 %[osrc0], %[srdWo], s48 offen offset:0 lds\n\t"
    "buffer_load_dwordx4 v236, %[srdWo], s48 offen offset:1024 lds\n\t"
    "buffer_load_dwordx4 v237, %[srdWo], s48 offen offset:2048 lds\n\t"
    "buffer_load_dwordx4 v238, %[srdWo], s48 offen offset:3072 lds\n\t"
    "s_add_u32 m0, s49, 36864\n\t"
    "s_mov_b32 s48, 36992\n\t"
    "buffer_load_dwordx4 %[osrc0], %[srdWo], s48 offen offset:0 lds\n\t"
    "buffer_load_dwordx4 v236, %[srdWo], s48 offen offset:1024 lds\n\t"
    "buffer_load_dwordx4 v237, %[srdWo], s48 offen offset:2048 lds\n\t"
    "buffer_load_dwordx4 v238, %[srdWo], s48 offen offset:3072 lds\n\t"
    "s_add_u32 m0, s49, 65536\n\t"
    "s_mov_b32 s48, 256\n\t"
    "buffer_load_dwordx4 %[osrc0], %[srdWo], s48 offen offset:0 lds\n\t"
    "buffer_load_dwordx4 v236, %[srdWo], s48 offen offset:1024 lds\n\t"
    "buffer_load_dwordx4 v237, %[srdWo], s48 offen offset:2048 lds\n\t"
    "buffer_load_dwordx4 v238, %[srdWo], s48 offen offset:3072 lds\n\t"
    "s_add_u32 m0, s49, 69632\n\t"
    "s_mov_b32 s48, 37120\n\t"
    "buffer_load_dwordx4 %[osrc0], %[srdWo], s48 offen offset:0 lds\n\t"
    "buffer_load_dwordx4 v236, %[srdWo], s48 offen offset:1024 lds\n\t"
    "buffer_load_dwordx4 v237, %[srdWo], s48 offen offset:2048 lds\n\t"
    "buffer_load_dwordx4 v238, %[srdWo], s48 offen offset:3072 lds\n\t"
    "s_add_u32 m0, s49, 131072\n\t"
    "s_mov_b32 s48, 384\n\t"
    "buffer_load_dwordx4 %[osrc0], %[srdWo], s48 offen offset:0 lds\n\t"
    "buffer_load_dwordx4 v236, %[srdWo], s48 offen offset:1024 lds\n\t"
    "buffer_load_dwordx4 v237, %[srdWo], s48 offen offset:2048 lds\n\t"
    "buffer_load_dwordx4 v238, %[srdWo], s48 offen offset:3072 lds\n\t"
    "s_add_u32 m0, s49, 135168\n\t"
    "s_mov_b32 s48, 37248\n\t"
    "buffer_load_dwordx4 %[osrc0], %[srdWo], s48 offen offset:0 lds\n\t"
    "buffer_load_dwordx4 v236, %[srdWo], s48 offen offset:1024 lds\n\t"
    "buffer_load_dwordx4 v237, %[srdWo], s48 offen offset:2048 lds\n\t"
    "buffer_load_dwordx4 v238, %[srdWo], s48 offen offset:3072 lds\n\t"
    "s_waitcnt vmcnt(0)\n\t"
    "s_mov_b32 s61, 1\n\t"
    "s_branch NORM%=\n\t"
    "END%=:\n\t"
    "s_nop 1\n\t"
    :
    : [srdK] "s"(srdK), [srdV] "s"(srdV), [srdW] "s"(srdW), [srdH] "s"(srdH), [wid] "s"(w), [ldsb] "s"(ldsb),
      [kb0] "v"(kb0), [vb] "v"(vb), [ksrc0] "v"(ksrc0), [ones] "v"(ones), [vsrc0] "v"(vsrc0),
      [wsrc0] "v"(wsrc0), [hoff] "v"(hoff), [srdWo] "s"(srdWo), [srdP] "s"(srdP), [ob0] "v"(ob0), [osrc0] "v"(osrc0), [poff] "v"(poff), [srdG] "s"(srdG), [srdDn] "s"(srdDn), [gsrc] "v"(gsrc), [dsrc] "v"(dsrc), [srdPF] "s"(srdPF), [pfv] "v"(pfv), [pfstep] "s"(pfstep)
    :
      "v32", "v33", "v34", "v35", "v36", "v37", "v38", "v39", "v40", "v41", "v42", "v43", "v44", "v45", "v46", "v47",
      "v48", "v49", "v50", "v51", "v52", "v53", "v54", "v55", "v56", "v57", "v58", "v59", "v60", "v61", "v62", "v63",
      "v64", "v65", "v66", "v67", "v68", "v69", "v70", "v71", "v72", "v73", "v74", "v75", "v76", "v77", "v78", "v79",
      "v80", "v81", "v82", "v83", "v84", "v85", "v86", "v87", "v88", "v89", "v90", "v91", "v92", "v93", "v94", "v95",
      "v96", "v97", "v98", "v99", "v100", "v101", "v102", "v103", "v104", "v105", "v106", "v107", "v108", "v109", "v110", "v111",
      "v112", "v113", "v114", "v115", "v116", "v117", "v118", "v119", "v120", "v121", "v122", "v123", "v124", "v125", "v126", "v127",
      "v128", "v129", "v130", "v131", "v132", "v133", "v134", "v135", "v136", "v137", "v138", "v139", "v140", "v141", "v142", "v143",
      "v144", "v145", "v146", "v147", "v148", "v149", "v150", "v151", "v152", "v153", "v154", "v155", "v156", "v157", "v158", "v159",
      "v160", "v161", "v162", "v163", "v164", "v165", "v166", "v167", "v168", "v169", "v170", "v171", "v172", "v173", "v174", "v175",
      "v176", "v177", "v178", "v179", "v180", "v181", "v182", "v183", "v184", "v185", "v186", "v187", "v188", "v189", "v190", "v191",
      "v192", "v193", "v194", "v195", "v196", "v197", "v198", "v199", "v200", "v201", "v202", "v203", "v204", "v205", "v206", "v207",
      "v208", "v209", "v210", "v211", "v212", "v213", "v214", "v215", "v216", "v217", "v218", "v219", "v220", "v221", "v222", "v223",
      "v224", "v225", "v226", "v227", "v228", "v229", "v230", "v231", "v232", "v233", "v234", "v235", "v236", "v237", "v238", "v239",
      "v240", "v241", "v242", "v243", "v244", "v245", "v246", "v247", "v248", "v249", "v250", "v251", "v252", "v253", "v254", "v255",
      "a0", "a1", "a2", "a3", "a4", "a5", "a6", "a7", "a8", "a9", "a10", "a11", "a12", "a13", "a14", "a15",
      "a16", "a17", "a18", "a19", "a20", "a21", "a22", "a23", "a24", "a25", "a26", "a27", "a28", "a29", "a30", "a31",
      "a32", "a33", "a34", "a35", "a36", "a37", "a38", "a39", "a40", "a41", "a42", "a43", "a44", "a45", "a46", "a47",
      "a48", "a49", "a50", "a51", "a52", "a53", "a54", "a55", "a56", "a57", "a58", "a59", "a60", "a61", "a62", "a63",
      "a64", "a65", "a66", "a67", "a68", "a69", "a70", "a71", "a72", "a73", "a74", "a75", "a76", "a77", "a78", "a79",
      "a80", "a81", "a82", "a83", "a84", "a85", "a86", "a87", "a88", "a89", "a90", "a91", "a92", "a93", "a94", "a95",
      "a96", "a97", "a98", "a99", "a100", "a101", "a102", "a103", "a104", "a105", "a106", "a107", "a108", "a109", "a110", "a111",
      "a112", "a113", "a114", "a115", "a116", "a117", "a118", "a119", "a120", "a121", "a122", "a123", "a124", "a125", "a126", "a127",
      "a128", "a129", "a130", "a131", "a132", "a133", "a134", "a135", "a136", "a137", "a138", "a139", "a140", "a141", "a142", "a143",
      "a144", "a145", "a146", "a147", "a148", "a149", "a150", "a151", "a152", "a153", "a154", "a155", "a156", "a157", "a158", "a159",
      "a160", "a161", "a162", "a163", "a164", "a165", "a166", "a167", "a168", "a169", "a170", "a171", "a172", "a173", "a174", "a175",
      "a176", "a177", "a178", "a179", "a180", "a181", "a182", "a183", "a184", "a185", "a186", "a187", "a188", "a189", "a190", "a191",
      "a192", "a193", "a194", "a195", "a196", "a197", "a198", "a199", "a200", "a201", "a202", "a203", "a204", "a205", "a206", "a207",
      "a208", "a209", "a210", "a211", "a212", "a213", "a214", "a215", "a216", "a217", "a218", "a219", "a220", "a221", "a222", "a223",
      "a224", "a225", "a226", "a227", "a228", "a229", "a230", "a231", "a232", "a233", "a234", "a235", "a236", "a237", "a238", "a239",
      "a240", "a241", "a242", "a243", "a244", "a245", "a246", "a247", "a248", "a249", "a250", "a251", "a252", "a253", "a254", "a255",
      "s40", "s41", "s42", "s43", "s44", "s45", "s46", "s47", "s48", "s49", "s50", "s51", "s56", "s57", "s58", "s59",
      "s60", "s61", "s62", "s64", "s65", "vcc", "scc", "memory");
  tail_phase(lds, ws, x, wr, wg, wu, wd, out, tag, rb, kvh, w);
  asm volatile(".Lk_attn_end:");
}


extern "C" void kernel_launch(void* const* d_in, const int* in_sizes, int n_in, void* d_out, int out_size, void* d_ws, size_t ws_size, hipStream_t stream) {
  const u16* x = (const u16*)d_in[0]; const u16* kc = (const u16*)d_in[1]; const u16* vc = (const u16*)d_in[2];
  const u16* wq = (const u16*)d_in[3]; const u16* c = (const u16*)d_in[10]; const u16* wm = (const u16*)d_in[11];
  char* ws = (char*)d_ws;
  hipFuncSetAttribute((const void*)k_attn, hipFuncAttributeMaxDynamicSharedMemorySize, 163840);
  k_attn<<<256, 256, 163840, stream>>>(wq, kc, vc, (const u16*)d_in[4], x, (const u16*)d_in[6], (const u16*)d_in[7], (const u16*)d_in[8], (const u16*)d_in[9], c, wm, (u16*)d_out, ws);
}
```
